```python
import torch

def _softmax(x, axis=-1):
    return torch.softmax(x, dim=axis)
def _gelu(x):
    return torch.nn.functional.gelu(x, approximate="tanh")
def _silu(x):
    return torch.nn.functional.silu(x)
def _sigmoid(x):
    return torch.sigmoid(x)
def _layernorm(x, axis=-1, eps=1e-5):
    m = x.mean(dim=axis, keepdim=True)
    v = x.var(dim=axis, keepdim=True, unbiased=False)
    return (x - m) / torch.sqrt(v + eps)
def _scatter_add(tgt, idx, src, axis=0):
    return tgt.clone().index_add_(axis, idx, src)
def _segment_sum(src, seg, n):
    out = torch.zeros((n,) + src.shape[1:], dtype=src.dtype)
    return out.index_add_(0, seg, src)
def _pad(x, pw):
    flat = tuple(v for pair in reversed(pw) for v in pair)
    return torch.nn.functional.pad(x, flat)

kernel_name = 'parallel_sum__compressed_kv_cla__mixer_channel_mlp__prompt'

def setup_inputs():

    _w = (lambda a: (lambda t: t.to(torch.bfloat16) if t.is_floating_point() else t)(torch.as_tensor(a)))
    def _A_setup():
        B, H, Sq, Sk, D, M = 1, 32, 2048, 2048, 128, 512
        Dc = 4 * D
        return {
            "x": _w((lambda *s: torch.randn(*s, dtype=torch.bfloat16) * 0.05)(B, Sq, M)),
            "c_kv": _w((lambda *s: torch.randn(*s, dtype=torch.bfloat16) * 0.4)(B, Sk, Dc)),
            "w_q": _w((lambda *s: torch.randn(*s, dtype=torch.bfloat16) * 3.2)(M, H, D) * (M ** -0.5)),
            "w_uk": _w((lambda *s: torch.randn(*s, dtype=torch.bfloat16) * 3.2)(Dc, H, D) * (Dc ** -0.5)),
            "w_uv": _w((lambda *s: torch.randn(*s, dtype=torch.bfloat16) * 0.4)(Dc, H, D) * (Dc ** -0.5)),
            "w_o": _w((lambda *s: torch.randn(*s, dtype=torch.bfloat16) * 0.4)(H, D, M) * ((H * D) ** -0.5)),
        }
    _A_in = _A_setup()

    def _B_setup():
        B, S, D, F = 1, 2048, 512, 1376
        return {
            "x": _w((lambda *s: torch.randn(*s, dtype=torch.bfloat16) * 0.05)(B, S, D)),
            "w1": _w((lambda *s: torch.randn(*s, dtype=torch.bfloat16) * 0.8)(D, F) * (D ** -0.5)),
            "b1": _w((lambda *s: torch.randn(*s, dtype=torch.bfloat16) * 0.4)(F)),
            "w2": _w((lambda *s: torch.randn(*s, dtype=torch.bfloat16) * 0.05)(F, D) * (F ** -0.5)),
            "b2": _w((lambda *s: torch.randn(*s, dtype=torch.bfloat16) * 0.05)(D)),
        }
    _B_in = _B_setup()

    return {
        "A_x": _A_in["x"],
        "A_c_kv": _A_in["c_kv"],
        "A_w_q": _A_in["w_q"],
        "A_w_uk": _A_in["w_uk"],
        "A_w_uv": _A_in["w_uv"],
        "A_w_o": _A_in["w_o"],
        "B_x": _B_in["x"],
        "B_w1": _B_in["w1"],
        "B_b1": _B_in["b1"],
        "B_w2": _B_in["w2"],
        "B_b2": _B_in["b2"],
    }

def reference(A_x, A_c_kv, A_w_q, A_w_uk, A_w_uv, A_w_o, B_x, B_w1, B_b1, B_w2, B_b2):
    def _A_ref(x, c_kv, w_q, w_uk, w_uv, w_o):
        B, Sq, M = x.shape[0], x.shape[1], x.shape[2]
        Sk = c_kv.shape[1]
        H, D = w_q.shape[1], w_q.shape[2]
        q = torch.einsum("bqm,mhd->bhqd", x, w_q)
        k = torch.einsum("bkc,chd->bhkd", c_kv, w_uk)
        v = torch.einsum("bkc,chd->bhkd", c_kv, w_uv)
        scores = torch.einsum("bhqd,bhkd->bhqk", q, k) * (D ** -0.5)
        off = Sk - Sq
        qi = torch.arange(Sq)[:, None]
        kj = torch.arange(Sk)[None, :]
        cmask = kj <= (qi + off)
        scores = torch.where(cmask[None, None], scores, -1e9)
        p = _softmax(scores, axis=-1)
        ctx = torch.einsum("bhqk,bhkd->bhqd", p, v)
        return torch.einsum("bhqd,hdm->bqm", ctx, w_o)

    def _B_ref(x, w1, b1, w2, b2):
        y = _layernorm(x, axis=-1)
        h = _gelu(torch.einsum("bsd,df->bsf", y, w1) + b1)
        return torch.einsum("bsf,fd->bsd", h, w2) + b2

    x = A_x
    xn = _layernorm(x, axis=-1)
    return x + _A_ref(xn, A_c_kv, A_w_q, A_w_uk, A_w_uv, A_w_o) + _B_ref(xn, B_w1, B_b1, B_w2, B_b2)
```

```cpp
#include <hip/hip_runtime.h>
#include <hip/hip_bf16.h>
#include <stdint.h>

typedef __bf16 bf16x8 __attribute__((ext_vector_type(8)));
typedef short s16x8 __attribute__((ext_vector_type(8)));
typedef float f32x4 __attribute__((ext_vector_type(4)));
typedef float f32x2 __attribute__((ext_vector_type(2)));
typedef __bf16 bf16x2 __attribute__((ext_vector_type(2)));
typedef unsigned u32x2 __attribute__((ext_vector_type(2)));
typedef unsigned u32x4 __attribute__((ext_vector_type(4)));
typedef unsigned u32x8 __attribute__((ext_vector_type(8)));
typedef unsigned u32x16 __attribute__((ext_vector_type(16)));
typedef unsigned short u16;
#define LAS __attribute__((address_space(3)))
#define DI __device__ __forceinline__
#define MFMA(a, b, c) __builtin_amdgcn_mfma_f32_16x16x32_bf16((a), (b), (c), 0, 0, 0)
#define SRD(v, o, p, n) do { const unsigned long long a_ = (unsigned long long)(p); v[o] = (unsigned)a_; v[o + 1] = (unsigned)(a_ >> 32) & 0xffffu; v[o + 2] = (n); v[o + 3] = 0x20000u; } while (0)

constexpr int SEQ = 2048, DM = 512, NH = 32, HD = 128, FF = 1376, NHD = NH * HD;
constexpr float QSCALE = 0.12751743082f;
constexpr int KS3 = 128 + 43;
constexpr size_t WS_XN = 0;
constexpr size_t WS_ST = WS_XN + (size_t)2 * 1024 * 1024;
constexpr size_t WS_KF = WS_ST + 65536;
constexpr size_t WS_VF = WS_KF + (size_t)16 * 1024 * 1024;
constexpr size_t WS_AF = WS_VF + (size_t)16 * 1024 * 1024;
constexpr size_t WS_PX = (WS_AF + (size_t)128 * KS3 * 1024 + 131071) / 131072 * 131072;
constexpr size_t WS_CF = WS_PX + (size_t)64 * 131072;
constexpr size_t WS_RF = WS_CF + 8 * 8192;
constexpr unsigned WS_TOTAL = (unsigned)(WS_RF + 32 * 2048);
constexpr unsigned AFB = 128u * KS3 * 1024u;

DI unsigned cvtpk(float lo, float hi) { f32x2 v = {lo, hi}; bf16x2 b = __builtin_convertvector(v, bf16x2); return __builtin_bit_cast(unsigned, b); }
DI bf16x8 pack2(const f32x4& a, const f32x4& b) { u32x4 w = {cvtpk(a[0], a[1]), cvtpk(a[2], a[3]), cvtpk(b[0], b[1]), cvtpk(b[2], b[3])}; return __builtin_bit_cast(bf16x8, w); }
DI float bf_lo(unsigned w) { return __uint_as_float(w << 16); }
DI float bf_hi(unsigned w) { return __uint_as_float(w & 0xffff0000u); }
template <int CTRL> DI float dpp_add(float v) { return v + __uint_as_float(__builtin_amdgcn_update_dpp(0u, __float_as_uint(v), CTRL, 0xf, 0xf, true)); }
DI float red32(float v) {
    v = dpp_add<0xB1>(v); v = dpp_add<0x4E>(v); v = dpp_add<0x141>(v); v = dpp_add<0x140>(v);
    auto r = __builtin_amdgcn_permlane16_swap(__float_as_uint(v), __float_as_uint(v), false, false); return __uint_as_float(r[0]) + __uint_as_float(r[1]);
}
DI float g4_max(float v) {
    auto r = __builtin_amdgcn_permlane16_swap(__float_as_uint(v), __float_as_uint(v), false, false); v = fmaxf(__uint_as_float(r[0]), __uint_as_float(r[1]));
    auto s = __builtin_amdgcn_permlane32_swap(__float_as_uint(v), __float_as_uint(v), false, false); return fmaxf(__uint_as_float(s[0]), __uint_as_float(s[1]));
}
DI float g4_add(float v) {
    auto r = __builtin_amdgcn_permlane16_swap(__float_as_uint(v), __float_as_uint(v), false, false); v = __uint_as_float(r[0]) + __uint_as_float(r[1]);
    auto s = __builtin_amdgcn_permlane32_swap(__float_as_uint(v), __float_as_uint(v), false, false); return __uint_as_float(s[0]) + __uint_as_float(s[1]);
}

#define GAS __attribute__((address_space(1)))
DI void attn_safe(const GAS u16* wq, GAS char* ws, int h, int rt, int L) {
    const int i = L & 15, g = L >> 4;
    f32x4 q[8];
#pragma unroll
    for (int nb = 0; nb < 8; ++nb) q[nb] = f32x4{};
    for (int s = 0; s < 16; ++s) {
        const bf16x8 xf = *(const GAS bf16x8*)(ws + WS_XN + ((size_t)rt * 16 + s) * 1024 + L * 16);
#pragma unroll
        for (int nb = 0; nb < 8; ++nb) {
            s16x8 wv;
#pragma unroll
            for (int j = 0; j < 8; ++j) wv[j] = (short)wq[(size_t)(32 * s + 8 * g + j) * NHD + h * HD + 16 * nb + i];
            q[nb] = MFMA(__builtin_bit_cast(bf16x8, wv), xf, q[nb]);
        }
    }
    bf16x8 qf[4];
#pragma unroll
    for (int ds = 0; ds < 4; ++ds) qf[ds] = pack2(q[2 * ds], q[2 * ds + 1]);
    f32x4 o[8];
#pragma unroll
    for (int dt = 0; dt < 8; ++dt) o[dt] = f32x4{};
    float m = -1e38f, l = 0.f;
    for (int u = 0; u <= (rt >> 1); ++u) {
        const GAS char* kp = ws + WS_KF + ((size_t)h * 64 + u) * 8192 + L * 16;
        const GAS char* vp = ws + WS_VF + ((size_t)h * 64 + u) * 8192 + L * 16;
        f32x4 sc[2] = {f32x4{}, f32x4{}};
#pragma unroll
        for (int f = 0; f < 8; ++f) sc[f & 1] = MFMA(*(const GAS bf16x8*)(kp + f * 1024), qf[f >> 1], sc[f & 1]);
        float mx = m;
#pragma unroll
        for (int e = 0; e < 8; ++e) { if (32 * u + 16 * (e >> 2) + 4 * g + (e & 3) > 16 * rt + i) sc[e >> 2][e & 3] = -__builtin_inff(); mx = fmaxf(mx, sc[e >> 2][e & 3]); }
        mx = g4_max(mx);
        const float al = __builtin_amdgcn_exp2f(m - mx); m = mx;
        float ps = 0;
#pragma unroll
        for (int e = 0; e < 8; ++e) { sc[e >> 2][e & 3] = __builtin_amdgcn_exp2f(sc[e >> 2][e & 3] - mx); ps += sc[e >> 2][e & 3]; }
        l = l * al + ps;
        const bf16x8 pf = pack2(sc[0], sc[1]);
#pragma unroll
        for (int dt = 0; dt < 8; ++dt) { o[dt] *= al; o[dt] = MFMA(*(const GAS bf16x8*)(vp + dt * 1024), pf, o[dt]); }
    }
    const float il = 1.f / g4_add(l);
    GAS char* dst = ws + WS_AF + ((size_t)rt * KS3 + 4 * h) * 1024 + L * 16;
#pragma unroll
    for (int ds = 0; ds < 4; ++ds) {
        const u32x4 v = __builtin_bit_cast(u32x4, pack2(o[2 * ds] * il, o[2 * ds + 1] * il));
        __hip_atomic_store((GAS unsigned long long*)(dst + ds * 1024), ((unsigned long long)v[1] << 32) | v[0], __ATOMIC_RELAXED, __HIP_MEMORY_SCOPE_AGENT);
        __hip_atomic_store((GAS unsigned long long*)(dst + ds * 1024 + 8), ((unsigned long long)v[3] << 32) | v[2], __ATOMIC_RELAXED, __HIP_MEMORY_SCOPE_AGENT);
    }
}

__global__ __launch_bounds__(256, 1) void mk_block(const u16* __restrict__ x, const u16* __restrict__ ckv, const u16* __restrict__ wq, const u16* __restrict__ wuk, const u16* __restrict__ wuv,
                                                   const u16* __restrict__ wo, const u16* __restrict__ w1, const u16* __restrict__ b1, const u16* __restrict__ w2, const u16* __restrict__ b2,
                                                   char* __restrict__ ws, u16* __restrict__ out) {
    u32x4 s0, t_ws, t_c, t_x, t_wq, vo, t_k4; u32x8 t_kv, t_w1, sc, t_d0, t_d1; u32x16 vv, vk; u32x2 tg, vw; u32x4 sf0, sf1, sf2;
    {
        const int tid = threadIdx.x, L = tid & 63, w = __builtin_amdgcn_readfirstlane(tid >> 6), r = L & 31, hh = L >> 5;
        const int bid = blockIdx.x, idx = bid >> 3, h = 4 * (bid & 7) + (idx >> 3), jp = idx & 7;
        const int kh = w & 1, rh = w >> 1, i16 = L & 15, g = L >> 4, q = i16 >> 2, p = L & 3;
        const int key0 = 256 * jp + 64 * w;
        SRD(t_c, 0, (const char*)ckv + (size_t)key0 * 1024, 65536u); SRD(t_kv, 0, (const char*)wuk + h * 256, 512u * 8192u - h * 256u); SRD(t_kv, 4, (const char*)wuv + h * 256, 512u * 8192u - h * 256u);
        u32x2 m2 = {(unsigned)(4096 * rh + 1024 * kh), (unsigned)(1024 * w)}; u32x8 vc;
        vw[0] = (8 * rh + ((L >> 2) & 7)) * 8192 + (64 * kh + 32 * hh) * 2 + 16 * (p ^ (2 * rh)); vw[1] = vw[0] + 16 * 8192 - 2048;
#pragma unroll
        for (int pc = 0; pc < 8; ++pc) vc[pc] = (8 * pc + (L >> 3)) * 1024 + 16 * ((L & 7) ^ (4 * (pc & 1) + g)) - 1024 * (pc & 3);
        asm volatile(
R"ASM(
s_lshl_b32 s95, s51, 3
s_add_u32 m0, s50, 0
s_mov_b32 s93, 0
buffer_load_dwordx4 v4, s[16:19], s93 offen lds
buffer_load_dwordx4 v5, s[16:19], s93 offen offset:2048 lds
s_add_u32 m0, s95, 131072
s_mov_b32 s92, 0
buffer_load_dwordx4 v42, s[12:15], s92 offen offset:0 lds
buffer_load_dwordx4 v43, s[12:15], s92 offen offset:1024 lds
buffer_load_dwordx4 v44, s[12:15], s92 offen offset:2048 lds
buffer_load_dwordx4 v45, s[12:15], s92 offen offset:3072 lds
s_add_u32 m0, s95, 135168
s_mov_b32 s92, 0
buffer_load_dwordx4 v46, s[12:15], s92 offen offset:0 lds
buffer_load_dwordx4 v47, s[12:15], s92 offen offset:1024 lds
buffer_load_dwordx4 v48, s[12:15], s92 offen offset:2048 lds
buffer_load_dwordx4 v49, s[12:15], s92 offen offset:3072 lds
s_add_u32 m0, s50, 8192
s_mov_b32 s93, 262144
buffer_load_dwordx4 v4, s[16:19], s93 offen lds
buffer_load_dwordx4 v5, s[16:19], s93 offen offset:2048 lds
s_add_u32 m0, s50, 16384
s_mov_b32 s93, 524288
buffer_load_dwordx4 v4, s[16:19], s93 offen lds
buffer_load_dwordx4 v5, s[16:19], s93 offen offset:2048 lds
s_add_u32 m0, s95, 65536
s_mov_b32 s92, 128
buffer_load_dwordx4 v42, s[12:15], s92 offen offset:0 lds
buffer_load_dwordx4 v43, s[12:15], s92 offen offset:1024 lds
buffer_load_dwordx4 v44, s[12:15], s92 offen offset:2048 lds
buffer_load_dwordx4 v45, s[12:15], s92 offen offset:3072 lds
s_add_u32 m0, s95, 69632
s_mov_b32 s92, 128
buffer_load_dwordx4 v46, s[12:15], s92 offen offset:0 lds
buffer_load_dwordx4 v47, s[12:15], s92 offen offset:1024 lds
buffer_load_dwordx4 v48, s[12:15], s92 offen offset:2048 lds
buffer_load_dwordx4 v49, s[12:15], s92 offen offset:3072 lds
s_add_u32 m0, s50, 24576
s_mov_b32 s93, 786432
buffer_load_dwordx4 v4, s[16:19], s93 offen lds
buffer_load_dwordx4 v5, s[16:19], s93 offen offset:2048 lds
)ASM"
            : "={s[92:95]}"(t_k4)
            : "{s[12:15]}"(t_c), "{s[16:23]}"(t_kv), "{s[50:51]}"(m2), "{v[4:5]}"(vw), "{v[42:49]}"(vc) : "memory", "scc", "vcc");
        const int odd = h & 1, blkA = odd ? 15 - jp : jp, blkB = 15 - blkA, blk1 = jp, blk2 = 15 - jp;
        const int fb0 = (3 * h) >> 1, fa0 = odd ? fb0 + 1 : fb0, fa1 = odd ? fb0 : fb0 + 1;
        const int row = 128 * ((h >> 4) ? blk2 : blk1) + 8 * (h & 15) + 2 * w + hh;
        const int kq = h >> 3, t = h & 7, rblk = (t >> 2) ? blk2 : blk1, nt = t & 3;
        s0[0] = (unsigned)(unsigned long long)b2; s0[1] = (unsigned)((unsigned long long)b2 >> 32); s0[2] = (unsigned)(unsigned long long)out; s0[3] = (unsigned)((unsigned long long)out >> 32);
        SRD(t_ws, 0, ws, WS_TOTAL); SRD(t_x, 0, x, 2097152u); SRD(t_wq, 0, (const char*)wq + h * 256, 512u * 8192u - h * 256u); SRD(t_w1, 0, w1, 512u * 2752u); SRD(t_w1, 4, b1, 2752u);
        SRD(sc, 0, wo, 4096u * 1024u); SRD(sc, 4, w2, 1376u * 1024u);
        const unsigned rbA = 4 * blkA + w, rbB = 4 * blkB + w, AF = (unsigned)WS_AF, kb = (unsigned)WS_KF + h * 524288u;
        t_d0[0] = (unsigned)WS_XN + rbA * 32768u; t_d0[1] = (unsigned)WS_XN + rbB * 32768u; t_d0[2] = m2[0]; t_d0[3] = m2[1];
        t_d0[4] = kb + (blk1 + 1) * 32768u; t_d0[5] = t_d0[4] + 16777216u; t_d0[6] = blk1; t_d0[7] = blk2;
        t_d1[0] = AF + (2 * (4 * blk1 + w) * KS3 + 4 * h) * 1024u; t_d1[1] = AF + (2 * (4 * blk2 + w) * KS3 + 4 * h) * 1024u;
        t_d1[2] = fa0 < 43 ? AF + (2 * rbA * KS3 + 128 + fa0) * 1024u : WS_TOTAL; t_d1[3] = fa0 < 43 ? AF + (2 * rbB * KS3 + 128 + fa0) * 1024u : WS_TOTAL; t_d1[4] = fa1 < 43 ? AF + (2 * rbA * KS3 + 128 + fa1) * 1024u : WS_TOTAL;
        t_d1[5] = kh * 8192 + rh * 4096; t_d1[6] = (unsigned)WS_ST + rbA * 128u; t_d1[7] = (unsigned)WS_ST + rbB * 128u;
        const unsigned cfb = (unsigned)WS_CF + jp * 8192u, rfb = (unsigned)WS_RF + h * 2048u;
        sf0[0] = fa0 * 64u; sf0[1] = fa1 * 64u; sf0[2] = kb; sf0[3] = kb + (key0 >> 5) * 8192u;
        sf1[0] = w == 0 ? cfb + h * 256u : WS_TOTAL; sf1[1] = w == 0 ? rfb + jp * 256u : WS_TOTAL; sf1[2] = (w == 0 && h == 0) ? cfb + 128u : WS_TOTAL; sf1[3] = (w == 0 && jp == 0) ? rfb + 128u : WS_TOTAL;
        sf2[0] = cfb; sf2[1] = rfb; sf2[2] = AF + (8u * rblk * KS3 + 128 + 11 * kq) * 1024u; sf2[3] = 11u * kq * 32768u;
        int fcol = 32 * (hh ? fa1 : fa0); if (fcol > FF - 32) fcol = FF - 32;
        vv[0] = L * 16; vv[1] = L * 16 + rh * 4096 + kh * 16384; vv[2] = vw[0]; vv[3] = vw[1];
        vv[4] = (8 * w + ((L >> 2) & 7)) * 2752 + fcol * 2 + 16 * (p ^ (2 * kh));
        vv[5] = 65536 + 2048 * (2 * (g & 1) + (g >> 1)) + 64 * q + 8 * p + 32 * (g & 1); vv[6] = vv[5] ^ 32;
        vv[7] = 65536 + 8192 + 1024 * g + 64 * q + 8 * p + 32 * (g & 1); vv[8] = vv[7] ^ 32;
        vv[9] = i16 * 4; vv[10] = g * 8; vv[11] = i16 - 4 * g;
        vv[12] = 98304 + w * 16384 + L * 16; vv[13] = 98304 + (w ^ 1) * 16384 + odd * 8192 + L * 16;
        vv[14] = L * 16 + kh * 8192; vv[15] = 131072 + w * 8192 + L * 16;
        vk[0] = 65536 + w * 8192 + i16 * 128 + 16 * (g ^ (i16 >> 1)); vk[1] = vk[0] ^ 64;
#pragma unroll
        for (int pc = 0; pc < 8; ++pc) vk[2 + pc] = vc[pc];
        vk[10] = row * 1024 + r * 16; vk[11] = (unsigned)WS_XN + (row >> 4) * 16384 + (row & 15) * 16 + r * 256; vk[12] = r == 0 ? (unsigned)WS_ST + row * 4 : WS_TOTAL;
        vk[13] = (64 * jp + 16 * w + (L >> 2)) * 8192 + (L & 3) * 64; vk[14] = ((bid * 4 + w) * 64 + L) * 64; vk[15] = ((idx * 4 + w) * 16 + (L & 15)) * 64 + (L >> 4) * 1000000;
        vo[0] = L * 16 + 2 * w * KS3 * 1024; vo[1] = (8 * rh + ((L >> 2) & 7)) * 1024 + (128 * nt + 64 * kh + 32 * hh) * 2 + 16 * (p ^ (2 * (g & 1))); vo[2] = vk[14];
        { const int kbb = (L >> 3) & 1, qt = (L >> 1) & 3, kt = L & 1, rq = 2 * (w ^ (qt >> 1)) + (qt & 1), rk = 2 * (2 * kh + kbb) + kt;
          vo[3] = rq > rk ? 0u : (rq == rk ? 0xd3800000u : 0xfb800000u); }
    }
    unsigned redo;
    asm volatile(
R"ASM(
v_mov_b32 v62, 0
s_getpc_b64 s[64:65]
s_and_b32 s65, s65, 0xffff
s_mov_b32 s66, 72704
s_mov_b32 s67, 0x20000
buffer_load_dword v61, v55, s[64:67], 0 offen
buffer_load_dword v74, v62, s[8:11], s88 offen offset:128 sc1
buffer_load_dword v75, v62, s[8:11], s89 offen offset:128 sc1
buffer_load_dword v63, v53, s[16:19], 0 offen
v_subrev_u32 v56, 65536, v7
v_add_u32 v58, 32768, v7
v_add_u32 v24, 65536, v40
v_subrev_u32 v57, 65536, v8
v_add_u32 v59, 32768, v8
v_add_u32 v25, 65536, v41
v_add_u32 v18, 16384, v2
v_add_u32 v20, 49152, v7
v_add_u32 v21, 49152, v8
v_add_u32 v22, 49152, v9
v_add_u32 v23, 49152, v10
s_mov_b32 s97, 0
s_waitcnt vmcnt(18)
s_barrier
ds_read_b128 a[0:3], v24 offset:0
ds_read_b128 a[4:7], v24 offset:2048
ds_read_b128 a[8:11], v24 offset:4096
ds_read_b128 a[12:15], v24 offset:6144
ds_read_b128 a[16:19], v25 offset:0
ds_read_b128 a[20:23], v25 offset:2048
ds_read_b128 a[24:27], v25 offset:4096
ds_read_b128 a[28:31], v25 offset:6144
ds_read_b64_tr_b16 v[96:97], v56 offset:0
ds_read_b64_tr_b16 v[98:99], v56 offset:256
ds_read_b64_tr_b16 v[100:101], v57 offset:0
ds_read_b64_tr_b16 v[102:103], v57 offset:256
ds_read_b64_tr_b16 v[104:105], v56 offset:512
ds_read_b64_tr_b16 v[106:107], v56 offset:768
ds_read_b64_tr_b16 v[108:109], v57 offset:512
ds_read_b64_tr_b16 v[110:111], v57 offset:768
s_waitcnt vmcnt(16) lgkmcnt(0)
s_barrier
v_mfma_f32_16x16x32_bf16 v[192:195], v[96:99], a[0:3], 0
ds_read_b64_tr_b16 v[112:113], v56 offset:1024
v_mfma_f32_16x16x32_bf16 v[196:199], v[96:99], a[4:7], 0
ds_read_b64_tr_b16 v[114:115], v56 offset:1280
v_mfma_f32_16x16x32_bf16 v[200:203], v[96:99], a[8:11], 0
ds_read_b64_tr_b16 v[116:117], v57 offset:1024
v_mfma_f32_16x16x32_bf16 v[204:207], v[96:99], a[12:15], 0
ds_read_b64_tr_b16 v[118:119], v57 offset:1280
v_mfma_f32_16x16x32_bf16 v[128:131], v[100:103], a[0:3], 0
ds_read_b64_tr_b16 v[120:121], v56 offset:1536
v_mfma_f32_16x16x32_bf16 v[132:135], v[100:103], a[4:7], 0
ds_read_b64_tr_b16 v[122:123], v56 offset:1792
v_mfma_f32_16x16x32_bf16 v[136:139], v[100:103], a[8:11], 0
ds_read_b64_tr_b16 v[124:125], v57 offset:1536
v_mfma_f32_16x16x32_bf16 v[140:143], v[100:103], a[12:15], 0
ds_read_b64_tr_b16 v[126:127], v57 offset:1792
v_mfma_f32_16x16x32_bf16 v[208:211], v[104:107], a[0:3], 0
s_add_u32 m0, s50, 32768
s_mov_b32 s93, 1048576
v_mfma_f32_16x16x32_bf16 v[212:215], v[104:107], a[4:7], 0
buffer_load_dwordx4 v4, s[16:19], s93 offen lds
v_mfma_f32_16x16x32_bf16 v[216:219], v[104:107], a[8:11], 0
v_mfma_f32_16x16x32_bf16 v[220:223], v[104:107], a[12:15], 0
buffer_load_dwordx4 v5, s[16:19], s93 offen offset:2048 lds
v_mfma_f32_16x16x32_bf16 v[144:147], v[108:111], a[0:3], 0
s_add_u32 m0, s95, 98304
s_mov_b32 s92, 256
v_mfma_f32_16x16x32_bf16 v[148:151], v[108:111], a[4:7], 0
buffer_load_dwordx4 v42, s[12:15], s92 offen offset:0 lds
v_mfma_f32_16x16x32_bf16 v[152:155], v[108:111], a[8:11], 0
v_mfma_f32_16x16x32_bf16 v[156:159], v[108:111], a[12:15], 0
buffer_load_dwordx4 v43, s[12:15], s92 offen offset:1024 lds
s_waitcnt lgkmcnt(0)
v_mfma_f32_16x16x32_bf16 v[224:227], v[112:115], a[0:3], 0
ds_read_b64_tr_b16 v[96:97], v56 offset:8192
v_mfma_f32_16x16x32_bf16 v[228:231], v[112:115], a[4:7], 0
ds_read_b64_tr_b16 v[98:99], v56 offset:8448
v_mfma_f32_16x16x32_bf16 v[232:235], v[112:115], a[8:11], 0
ds_read_b64_tr_b16 v[100:101], v57 offset:8192
v_mfma_f32_16x16x32_bf16 v[236:239], v[112:115], a[12:15], 0
ds_read_b64_tr_b16 v[102:103], v57 offset:8448
v_mfma_f32_16x16x32_bf16 v[160:163], v[116:119], a[0:3], 0
ds_read_b64_tr_b16 v[104:105], v56 offset:8704
v_mfma_f32_16x16x32_bf16 v[164:167], v[116:119], a[4:7], 0
ds_read_b64_tr_b16 v[106:107], v56 offset:8960
v_mfma_f32_16x16x32_bf16 v[168:171], v[116:119], a[8:11], 0
ds_read_b64_tr_b16 v[108:109], v57 offset:8704
v_mfma_f32_16x16x32_bf16 v[172:175], v[116:119], a[12:15], 0
ds_read_b64_tr_b16 v[110:111], v57 offset:8960
v_mfma_f32_16x16x32_bf16 v[240:243], v[120:123], a[0:3], 0
v_mfma_f32_16x16x32_bf16 v[244:247], v[120:123], a[4:7], 0
buffer_load_dwordx4 v44, s[12:15], s92 offen offset:2048 lds
v_mfma_f32_16x16x32_bf16 v[248:251], v[120:123], a[8:11], 0
v_mfma_f32_16x16x32_bf16 v[252:255], v[120:123], a[12:15], 0
buffer_load_dwordx4 v45, s[12:15], s92 offen offset:3072 lds
v_mfma_f32_16x16x32_bf16 v[176:179], v[124:127], a[0:3], 0
s_add_u32 m0, s95, 102400
s_mov_b32 s92, 256
v_mfma_f32_16x16x32_bf16 v[180:183], v[124:127], a[4:7], 0
buffer_load_dwordx4 v46, s[12:15], s92 offen offset:0 lds
v_mfma_f32_16x16x32_bf16 v[184:187], v[124:127], a[8:11], 0
v_mfma_f32_16x16x32_bf16 v[188:191], v[124:127], a[12:15], 0
buffer_load_dwordx4 v47, s[12:15], s92 offen offset:1024 lds
s_waitcnt vmcnt(14) lgkmcnt(0)
s_barrier
v_mfma_f32_16x16x32_bf16 v[192:195], v[96:99], a[16:19], v[192:195]
ds_read_b64_tr_b16 v[112:113], v56 offset:9216
v_mfma_f32_16x16x32_bf16 v[196:199], v[96:99], a[20:23], v[196:199]
ds_read_b64_tr_b16 v[114:115], v56 offset:9472
v_mfma_f32_16x16x32_bf16 v[200:203], v[96:99], a[24:27], v[200:203]
ds_read_b64_tr_b16 v[116:117], v57 offset:9216
v_mfma_f32_16x16x32_bf16 v[204:207], v[96:99], a[28:31], v[204:207]
ds_read_b64_tr_b16 v[118:119], v57 offset:9472
v_mfma_f32_16x16x32_bf16 v[128:131], v[100:103], a[16:19], v[128:131]
ds_read_b64_tr_b16 v[120:121], v56 offset:9728
v_mfma_f32_16x16x32_bf16 v[132:135], v[100:103], a[20:23], v[132:135]
ds_read_b64_tr_b16 v[122:123], v56 offset:9984
v_mfma_f32_16x16x32_bf16 v[136:139], v[100:103], a[24:27], v[136:139]
ds_read_b64_tr_b16 v[124:125], v57 offset:9728
v_mfma_f32_16x16x32_bf16 v[140:143], v[100:103], a[28:31], v[140:143]
ds_read_b64_tr_b16 v[126:127], v57 offset:9984
v_mfma_f32_16x16x32_bf16 v[208:211], v[104:107], a[16:19], v[208:211]
ds_read_b128 a[32:35], v40 offset:0
v_mfma_f32_16x16x32_bf16 v[212:215], v[104:107], a[20:23], v[212:215]
ds_read_b128 a[36:39], v40 offset:2048
v_mfma_f32_16x16x32_bf16 v[216:219], v[104:107], a[24:27], v[216:219]
ds_read_b128 a[40:43], v40 offset:4096
v_mfma_f32_16x16x32_bf16 v[220:223], v[104:107], a[28:31], v[220:223]
ds_read_b128 a[44:47], v40 offset:6144
v_mfma_f32_16x16x32_bf16 v[144:147], v[108:111], a[16:19], v[144:147]
buffer_load_dwordx4 v48, s[12:15], s92 offen offset:2048 lds
v_mfma_f32_16x16x32_bf16 v[148:151], v[108:111], a[20:23], v[148:151]
buffer_load_dwordx4 v49, s[12:15], s92 offen offset:3072 lds
s_add_u32 m0, s50, 40960
s_mov_b32 s93, 1310720
v_mfma_f32_16x16x32_bf16 v[152:155], v[108:111], a[24:27], v[152:155]
buffer_load_dwordx4 v4, s[16:19], s93 offen lds
v_mfma_f32_16x16x32_bf16 v[156:159], v[108:111], a[28:31], v[156:159]
buffer_load_dwordx4 v5, s[16:19], s93 offen offset:2048 lds
s_waitcnt lgkmcnt(4)
v_mfma_f32_16x16x32_bf16 v[224:227], v[112:115], a[16:19], v[224:227]
ds_read_b64_tr_b16 v[96:97], v56 offset:16384
v_mfma_f32_16x16x32_bf16 v[228:231], v[112:115], a[20:23], v[228:231]
ds_read_b64_tr_b16 v[98:99], v56 offset:16640
v_mfma_f32_16x16x32_bf16 v[232:235], v[112:115], a[24:27], v[232:235]
ds_read_b64_tr_b16 v[100:101], v57 offset:16384
v_mfma_f32_16x16x32_bf16 v[236:239], v[112:115], a[28:31], v[236:239]
ds_read_b64_tr_b16 v[102:103], v57 offset:16640
v_mfma_f32_16x16x32_bf16 v[160:163], v[116:119], a[16:19], v[160:163]
ds_read_b64_tr_b16 v[104:105], v56 offset:16896
v_mfma_f32_16x16x32_bf16 v[164:167], v[116:119], a[20:23], v[164:167]
ds_read_b64_tr_b16 v[106:107], v56 offset:17152
v_mfma_f32_16x16x32_bf16 v[168:171], v[116:119], a[24:27], v[168:171]
ds_read_b64_tr_b16 v[108:109], v57 offset:16896
v_mfma_f32_16x16x32_bf16 v[172:175], v[116:119], a[28:31], v[172:175]
ds_read_b64_tr_b16 v[110:111], v57 offset:17152
v_mfma_f32_16x16x32_bf16 v[240:243], v[120:123], a[16:19], v[240:243]
ds_read_b128 a[48:51], v41 offset:0
v_mfma_f32_16x16x32_bf16 v[244:247], v[120:123], a[20:23], v[244:247]
ds_read_b128 a[52:55], v41 offset:2048
v_mfma_f32_16x16x32_bf16 v[248:251], v[120:123], a[24:27], v[248:251]
ds_read_b128 a[56:59], v41 offset:4096
v_mfma_f32_16x16x32_bf16 v[252:255], v[120:123], a[28:31], v[252:255]
ds_read_b128 a[60:63], v41 offset:6144
s_add_u32 m0, s50, 49152
s_mov_b32 s93, 1572864
v_mfma_f32_16x16x32_bf16 v[176:179], v[124:127], a[16:19], v[176:179]
buffer_load_dwordx4 v4, s[16:19], s93 offen lds
v_mfma_f32_16x16x32_bf16 v[180:183], v[124:127], a[20:23], v[180:183]
buffer_load_dwordx4 v5, s[16:19], s93 offen offset:2048 lds
s_add_u32 m0, s50, 57344
s_mov_b32 s93, 1835008
v_mfma_f32_16x16x32_bf16 v[184:187], v[124:127], a[24:27], v[184:187]
buffer_load_dwordx4 v4, s[16:19], s93 offen lds
v_mfma_f32_16x16x32_bf16 v[188:191], v[124:127], a[28:31], v[188:191]
buffer_load_dwordx4 v5, s[16:19], s93 offen offset:2048 lds
s_waitcnt vmcnt(20) lgkmcnt(0)
s_barrier
v_mfma_f32_16x16x32_bf16 v[192:195], v[96:99], a[32:35], v[192:195]
ds_read_b64_tr_b16 v[112:113], v56 offset:17408
v_mfma_f32_16x16x32_bf16 v[196:199], v[96:99], a[36:39], v[196:199]
ds_read_b64_tr_b16 v[114:115], v56 offset:17664
v_mfma_f32_16x16x32_bf16 v[200:203], v[96:99], a[40:43], v[200:203]
ds_read_b64_tr_b16 v[116:117], v57 offset:17408
v_mfma_f32_16x16x32_bf16 v[204:207], v[96:99], a[44:47], v[204:207]
ds_read_b64_tr_b16 v[118:119], v57 offset:17664
v_mfma_f32_16x16x32_bf16 v[128:131], v[100:103], a[32:35], v[128:131]
ds_read_b64_tr_b16 v[120:121], v56 offset:17920
v_mfma_f32_16x16x32_bf16 v[132:135], v[100:103], a[36:39], v[132:135]
ds_read_b64_tr_b16 v[122:123], v56 offset:18176
v_mfma_f32_16x16x32_bf16 v[136:139], v[100:103], a[40:43], v[136:139]
ds_read_b64_tr_b16 v[124:125], v57 offset:17920
v_mfma_f32_16x16x32_bf16 v[140:143], v[100:103], a[44:47], v[140:143]
ds_read_b64_tr_b16 v[126:127], v57 offset:18176
v_mfma_f32_16x16x32_bf16 v[208:211], v[104:107], a[32:35], v[208:211]
s_add_u32 m0, s95, 131072
s_mov_b32 s92, 384
v_mfma_f32_16x16x32_bf16 v[212:215], v[104:107], a[36:39], v[212:215]
buffer_load_dwordx4 v42, s[12:15], s92 offen offset:0 lds
)ASM"
R"ASM(
v_mfma_f32_16x16x32_bf16 v[216:219], v[104:107], a[40:43], v[216:219]
v_mfma_f32_16x16x32_bf16 v[220:223], v[104:107], a[44:47], v[220:223]
buffer_load_dwordx4 v43, s[12:15], s92 offen offset:1024 lds
v_mfma_f32_16x16x32_bf16 v[144:147], v[108:111], a[32:35], v[144:147]
v_mfma_f32_16x16x32_bf16 v[148:151], v[108:111], a[36:39], v[148:151]
buffer_load_dwordx4 v44, s[12:15], s92 offen offset:2048 lds
v_mfma_f32_16x16x32_bf16 v[152:155], v[108:111], a[40:43], v[152:155]
v_mfma_f32_16x16x32_bf16 v[156:159], v[108:111], a[44:47], v[156:159]
buffer_load_dwordx4 v45, s[12:15], s92 offen offset:3072 lds
s_waitcnt lgkmcnt(0)
v_mfma_f32_16x16x32_bf16 v[224:227], v[112:115], a[32:35], v[224:227]
ds_read_b64_tr_b16 v[96:97], v56 offset:24576
v_mfma_f32_16x16x32_bf16 v[228:231], v[112:115], a[36:39], v[228:231]
ds_read_b64_tr_b16 v[98:99], v56 offset:24832
v_mfma_f32_16x16x32_bf16 v[232:235], v[112:115], a[40:43], v[232:235]
ds_read_b64_tr_b16 v[100:101], v57 offset:24576
v_mfma_f32_16x16x32_bf16 v[236:239], v[112:115], a[44:47], v[236:239]
ds_read_b64_tr_b16 v[102:103], v57 offset:24832
v_mfma_f32_16x16x32_bf16 v[160:163], v[116:119], a[32:35], v[160:163]
ds_read_b64_tr_b16 v[104:105], v56 offset:25088
v_mfma_f32_16x16x32_bf16 v[164:167], v[116:119], a[36:39], v[164:167]
ds_read_b64_tr_b16 v[106:107], v56 offset:25344
v_mfma_f32_16x16x32_bf16 v[168:171], v[116:119], a[40:43], v[168:171]
ds_read_b64_tr_b16 v[108:109], v57 offset:25088
v_mfma_f32_16x16x32_bf16 v[172:175], v[116:119], a[44:47], v[172:175]
ds_read_b64_tr_b16 v[110:111], v57 offset:25344
v_mfma_f32_16x16x32_bf16 v[240:243], v[120:123], a[32:35], v[240:243]
s_add_u32 m0, s95, 135168
s_mov_b32 s92, 384
v_mfma_f32_16x16x32_bf16 v[244:247], v[120:123], a[36:39], v[244:247]
buffer_load_dwordx4 v46, s[12:15], s92 offen offset:0 lds
v_mfma_f32_16x16x32_bf16 v[248:251], v[120:123], a[40:43], v[248:251]
v_mfma_f32_16x16x32_bf16 v[252:255], v[120:123], a[44:47], v[252:255]
buffer_load_dwordx4 v47, s[12:15], s92 offen offset:1024 lds
v_mfma_f32_16x16x32_bf16 v[176:179], v[124:127], a[32:35], v[176:179]
v_mfma_f32_16x16x32_bf16 v[180:183], v[124:127], a[36:39], v[180:183]
buffer_load_dwordx4 v48, s[12:15], s92 offen offset:2048 lds
v_mfma_f32_16x16x32_bf16 v[184:187], v[124:127], a[40:43], v[184:187]
v_mfma_f32_16x16x32_bf16 v[188:191], v[124:127], a[44:47], v[188:191]
buffer_load_dwordx4 v49, s[12:15], s92 offen offset:3072 lds
s_waitcnt vmcnt(14) lgkmcnt(0)
s_barrier
v_mfma_f32_16x16x32_bf16 v[192:195], v[96:99], a[48:51], v[192:195]
ds_read_b64_tr_b16 v[112:113], v56 offset:25600
v_mfma_f32_16x16x32_bf16 v[196:199], v[96:99], a[52:55], v[196:199]
ds_read_b64_tr_b16 v[114:115], v56 offset:25856
v_mfma_f32_16x16x32_bf16 v[200:203], v[96:99], a[56:59], v[200:203]
ds_read_b64_tr_b16 v[116:117], v57 offset:25600
v_mfma_f32_16x16x32_bf16 v[204:207], v[96:99], a[60:63], v[204:207]
ds_read_b64_tr_b16 v[118:119], v57 offset:25856
v_mfma_f32_16x16x32_bf16 v[128:131], v[100:103], a[48:51], v[128:131]
ds_read_b64_tr_b16 v[120:121], v56 offset:26112
v_mfma_f32_16x16x32_bf16 v[132:135], v[100:103], a[52:55], v[132:135]
ds_read_b64_tr_b16 v[122:123], v56 offset:26368
v_mfma_f32_16x16x32_bf16 v[136:139], v[100:103], a[56:59], v[136:139]
ds_read_b64_tr_b16 v[124:125], v57 offset:26112
v_mfma_f32_16x16x32_bf16 v[140:143], v[100:103], a[60:63], v[140:143]
ds_read_b64_tr_b16 v[126:127], v57 offset:26368
v_mfma_f32_16x16x32_bf16 v[208:211], v[104:107], a[48:51], v[208:211]
ds_read_b128 a[64:67], v40 offset:32768
v_mfma_f32_16x16x32_bf16 v[212:215], v[104:107], a[52:55], v[212:215]
ds_read_b128 a[68:71], v40 offset:34816
v_mfma_f32_16x16x32_bf16 v[216:219], v[104:107], a[56:59], v[216:219]
ds_read_b128 a[72:75], v40 offset:36864
v_mfma_f32_16x16x32_bf16 v[220:223], v[104:107], a[60:63], v[220:223]
ds_read_b128 a[76:79], v40 offset:38912
s_add_u32 m0, s50, 0
s_mov_b32 s93, 2097152
v_mfma_f32_16x16x32_bf16 v[144:147], v[108:111], a[48:51], v[144:147]
buffer_load_dwordx4 v4, s[16:19], s93 offen lds
v_mfma_f32_16x16x32_bf16 v[148:151], v[108:111], a[52:55], v[148:151]
buffer_load_dwordx4 v5, s[16:19], s93 offen offset:2048 lds
s_add_u32 m0, s50, 8192
s_mov_b32 s93, 2359296
v_mfma_f32_16x16x32_bf16 v[152:155], v[108:111], a[56:59], v[152:155]
buffer_load_dwordx4 v4, s[16:19], s93 offen lds
v_mfma_f32_16x16x32_bf16 v[156:159], v[108:111], a[60:63], v[156:159]
buffer_load_dwordx4 v5, s[16:19], s93 offen offset:2048 lds
s_waitcnt lgkmcnt(4)
v_mfma_f32_16x16x32_bf16 v[224:227], v[112:115], a[48:51], v[224:227]
ds_read_b64_tr_b16 v[96:97], v56 offset:32768
v_mfma_f32_16x16x32_bf16 v[228:231], v[112:115], a[52:55], v[228:231]
ds_read_b64_tr_b16 v[98:99], v56 offset:33024
v_mfma_f32_16x16x32_bf16 v[232:235], v[112:115], a[56:59], v[232:235]
ds_read_b64_tr_b16 v[100:101], v57 offset:32768
v_mfma_f32_16x16x32_bf16 v[236:239], v[112:115], a[60:63], v[236:239]
ds_read_b64_tr_b16 v[102:103], v57 offset:33024
v_mfma_f32_16x16x32_bf16 v[160:163], v[116:119], a[48:51], v[160:163]
ds_read_b64_tr_b16 v[104:105], v56 offset:33280
v_mfma_f32_16x16x32_bf16 v[164:167], v[116:119], a[52:55], v[164:167]
ds_read_b64_tr_b16 v[106:107], v56 offset:33536
v_mfma_f32_16x16x32_bf16 v[168:171], v[116:119], a[56:59], v[168:171]
ds_read_b64_tr_b16 v[108:109], v57 offset:33280
v_mfma_f32_16x16x32_bf16 v[172:175], v[116:119], a[60:63], v[172:175]
ds_read_b64_tr_b16 v[110:111], v57 offset:33536
v_mfma_f32_16x16x32_bf16 v[240:243], v[120:123], a[48:51], v[240:243]
ds_read_b128 a[80:83], v41 offset:32768
v_mfma_f32_16x16x32_bf16 v[244:247], v[120:123], a[52:55], v[244:247]
ds_read_b128 a[84:87], v41 offset:34816
v_mfma_f32_16x16x32_bf16 v[248:251], v[120:123], a[56:59], v[248:251]
ds_read_b128 a[88:91], v41 offset:36864
v_mfma_f32_16x16x32_bf16 v[252:255], v[120:123], a[60:63], v[252:255]
ds_read_b128 a[92:95], v41 offset:38912
s_add_u32 m0, s95, 65536
s_mov_b32 s92, 512
v_mfma_f32_16x16x32_bf16 v[176:179], v[124:127], a[48:51], v[176:179]
buffer_load_dwordx4 v42, s[12:15], s92 offen offset:0 lds
v_mfma_f32_16x16x32_bf16 v[180:183], v[124:127], a[52:55], v[180:183]
buffer_load_dwordx4 v43, s[12:15], s92 offen offset:1024 lds
v_mfma_f32_16x16x32_bf16 v[184:187], v[124:127], a[56:59], v[184:187]
buffer_load_dwordx4 v44, s[12:15], s92 offen offset:2048 lds
v_mfma_f32_16x16x32_bf16 v[188:191], v[124:127], a[60:63], v[188:191]
buffer_load_dwordx4 v45, s[12:15], s92 offen offset:3072 lds
s_waitcnt vmcnt(20) lgkmcnt(0)
s_barrier
v_mfma_f32_16x16x32_bf16 v[192:195], v[96:99], a[64:67], v[192:195]
ds_read_b64_tr_b16 v[112:113], v56 offset:33792
v_mfma_f32_16x16x32_bf16 v[196:199], v[96:99], a[68:71], v[196:199]
ds_read_b64_tr_b16 v[114:115], v56 offset:34048
v_mfma_f32_16x16x32_bf16 v[200:203], v[96:99], a[72:75], v[200:203]
ds_read_b64_tr_b16 v[116:117], v57 offset:33792
v_mfma_f32_16x16x32_bf16 v[204:207], v[96:99], a[76:79], v[204:207]
ds_read_b64_tr_b16 v[118:119], v57 offset:34048
v_mfma_f32_16x16x32_bf16 v[128:131], v[100:103], a[64:67], v[128:131]
ds_read_b64_tr_b16 v[120:121], v56 offset:34304
v_mfma_f32_16x16x32_bf16 v[132:135], v[100:103], a[68:71], v[132:135]
ds_read_b64_tr_b16 v[122:123], v56 offset:34560
v_mfma_f32_16x16x32_bf16 v[136:139], v[100:103], a[72:75], v[136:139]
ds_read_b64_tr_b16 v[124:125], v57 offset:34304
v_mfma_f32_16x16x32_bf16 v[140:143], v[100:103], a[76:79], v[140:143]
ds_read_b64_tr_b16 v[126:127], v57 offset:34560
v_mfma_f32_16x16x32_bf16 v[208:211], v[104:107], a[64:67], v[208:211]
s_add_u32 m0, s95, 69632
s_mov_b32 s92, 512
v_mfma_f32_16x16x32_bf16 v[212:215], v[104:107], a[68:71], v[212:215]
buffer_load_dwordx4 v46, s[12:15], s92 offen offset:0 lds
v_mfma_f32_16x16x32_bf16 v[216:219], v[104:107], a[72:75], v[216:219]
v_mfma_f32_16x16x32_bf16 v[220:223], v[104:107], a[76:79], v[220:223]
buffer_load_dwordx4 v47, s[12:15], s92 offen offset:1024 lds
v_mfma_f32_16x16x32_bf16 v[144:147], v[108:111], a[64:67], v[144:147]
v_mfma_f32_16x16x32_bf16 v[148:151], v[108:111], a[68:71], v[148:151]
buffer_load_dwordx4 v48, s[12:15], s92 offen offset:2048 lds
v_mfma_f32_16x16x32_bf16 v[152:155], v[108:111], a[72:75], v[152:155]
v_mfma_f32_16x16x32_bf16 v[156:159], v[108:111], a[76:79], v[156:159]
buffer_load_dwordx4 v49, s[12:15], s92 offen offset:3072 lds
s_waitcnt lgkmcnt(0)
v_mfma_f32_16x16x32_bf16 v[224:227], v[112:115], a[64:67], v[224:227]
ds_read_b64_tr_b16 v[96:97], v56 offset:40960
v_mfma_f32_16x16x32_bf16 v[228:231], v[112:115], a[68:71], v[228:231]
ds_read_b64_tr_b16 v[98:99], v56 offset:41216
v_mfma_f32_16x16x32_bf16 v[232:235], v[112:115], a[72:75], v[232:235]
ds_read_b64_tr_b16 v[100:101], v57 offset:40960
v_mfma_f32_16x16x32_bf16 v[236:239], v[112:115], a[76:79], v[236:239]
ds_read_b64_tr_b16 v[102:103], v57 offset:41216
v_mfma_f32_16x16x32_bf16 v[160:163], v[116:119], a[64:67], v[160:163]
ds_read_b64_tr_b16 v[104:105], v56 offset:41472
v_mfma_f32_16x16x32_bf16 v[164:167], v[116:119], a[68:71], v[164:167]
ds_read_b64_tr_b16 v[106:107], v56 offset:41728
v_mfma_f32_16x16x32_bf16 v[168:171], v[116:119], a[72:75], v[168:171]
ds_read_b64_tr_b16 v[108:109], v57 offset:41472
v_mfma_f32_16x16x32_bf16 v[172:175], v[116:119], a[76:79], v[172:175]
ds_read_b64_tr_b16 v[110:111], v57 offset:41728
v_mfma_f32_16x16x32_bf16 v[240:243], v[120:123], a[64:67], v[240:243]
s_add_u32 m0, s50, 16384
s_mov_b32 s93, 2621440
v_mfma_f32_16x16x32_bf16 v[244:247], v[120:123], a[68:71], v[244:247]
buffer_load_dwordx4 v4, s[16:19], s93 offen lds
v_mfma_f32_16x16x32_bf16 v[248:251], v[120:123], a[72:75], v[248:251]
v_mfma_f32_16x16x32_bf16 v[252:255], v[120:123], a[76:79], v[252:255]
buffer_load_dwordx4 v5, s[16:19], s93 offen offset:2048 lds
v_mfma_f32_16x16x32_bf16 v[176:179], v[124:127], a[64:67], v[176:179]
s_add_u32 m0, s50, 24576
s_mov_b32 s93, 2883584
v_mfma_f32_16x16x32_bf16 v[180:183], v[124:127], a[68:71], v[180:183]
buffer_load_dwordx4 v4, s[16:19], s93 offen lds
v_mfma_f32_16x16x32_bf16 v[184:187], v[124:127], a[72:75], v[184:187]
v_mfma_f32_16x16x32_bf16 v[188:191], v[124:127], a[76:79], v[188:191]
buffer_load_dwordx4 v5, s[16:19], s93 offen offset:2048 lds
s_waitcnt vmcnt(16) lgkmcnt(0)
s_barrier
v_mfma_f32_16x16x32_bf16 v[192:195], v[96:99], a[80:83], v[192:195]
ds_read_b64_tr_b16 v[112:113], v56 offset:41984
v_mfma_f32_16x16x32_bf16 v[196:199], v[96:99], a[84:87], v[196:199]
ds_read_b64_tr_b16 v[114:115], v56 offset:42240
v_mfma_f32_16x16x32_bf16 v[200:203], v[96:99], a[88:91], v[200:203]
ds_read_b64_tr_b16 v[116:117], v57 offset:41984
v_mfma_f32_16x16x32_bf16 v[204:207], v[96:99], a[92:95], v[204:207]
ds_read_b64_tr_b16 v[118:119], v57 offset:42240
v_mfma_f32_16x16x32_bf16 v[128:131], v[100:103], a[80:83], v[128:131]
ds_read_b64_tr_b16 v[120:121], v56 offset:42496
v_mfma_f32_16x16x32_bf16 v[132:135], v[100:103], a[84:87], v[132:135]
ds_read_b64_tr_b16 v[122:123], v56 offset:42752
v_mfma_f32_16x16x32_bf16 v[136:139], v[100:103], a[88:91], v[136:139]
ds_read_b64_tr_b16 v[124:125], v57 offset:42496
v_mfma_f32_16x16x32_bf16 v[140:143], v[100:103], a[92:95], v[140:143]
ds_read_b64_tr_b16 v[126:127], v57 offset:42752
v_mfma_f32_16x16x32_bf16 v[208:211], v[104:107], a[80:83], v[208:211]
ds_read_b128 a[96:99], v24 offset:0
v_mfma_f32_16x16x32_bf16 v[212:215], v[104:107], a[84:87], v[212:215]
ds_read_b128 a[100:103], v24 offset:2048
)ASM"
R"ASM(
v_mfma_f32_16x16x32_bf16 v[216:219], v[104:107], a[88:91], v[216:219]
ds_read_b128 a[104:107], v24 offset:4096
v_mfma_f32_16x16x32_bf16 v[220:223], v[104:107], a[92:95], v[220:223]
ds_read_b128 a[108:111], v24 offset:6144
s_add_u32 m0, s95, 98304
s_mov_b32 s92, 640
v_mfma_f32_16x16x32_bf16 v[144:147], v[108:111], a[80:83], v[144:147]
buffer_load_dwordx4 v42, s[12:15], s92 offen offset:0 lds
v_mfma_f32_16x16x32_bf16 v[148:151], v[108:111], a[84:87], v[148:151]
buffer_load_dwordx4 v43, s[12:15], s92 offen offset:1024 lds
v_mfma_f32_16x16x32_bf16 v[152:155], v[108:111], a[88:91], v[152:155]
buffer_load_dwordx4 v44, s[12:15], s92 offen offset:2048 lds
v_mfma_f32_16x16x32_bf16 v[156:159], v[108:111], a[92:95], v[156:159]
buffer_load_dwordx4 v45, s[12:15], s92 offen offset:3072 lds
s_waitcnt lgkmcnt(4)
v_mfma_f32_16x16x32_bf16 v[224:227], v[112:115], a[80:83], v[224:227]
ds_read_b64_tr_b16 v[96:97], v56 offset:49152
v_mfma_f32_16x16x32_bf16 v[228:231], v[112:115], a[84:87], v[228:231]
ds_read_b64_tr_b16 v[98:99], v56 offset:49408
v_mfma_f32_16x16x32_bf16 v[232:235], v[112:115], a[88:91], v[232:235]
ds_read_b64_tr_b16 v[100:101], v57 offset:49152
v_mfma_f32_16x16x32_bf16 v[236:239], v[112:115], a[92:95], v[236:239]
ds_read_b64_tr_b16 v[102:103], v57 offset:49408
v_mfma_f32_16x16x32_bf16 v[160:163], v[116:119], a[80:83], v[160:163]
ds_read_b64_tr_b16 v[104:105], v56 offset:49664
v_mfma_f32_16x16x32_bf16 v[164:167], v[116:119], a[84:87], v[164:167]
ds_read_b64_tr_b16 v[106:107], v56 offset:49920
v_mfma_f32_16x16x32_bf16 v[168:171], v[116:119], a[88:91], v[168:171]
ds_read_b64_tr_b16 v[108:109], v57 offset:49664
v_mfma_f32_16x16x32_bf16 v[172:175], v[116:119], a[92:95], v[172:175]
ds_read_b64_tr_b16 v[110:111], v57 offset:49920
v_mfma_f32_16x16x32_bf16 v[240:243], v[120:123], a[80:83], v[240:243]
ds_read_b128 a[112:115], v25 offset:0
v_mfma_f32_16x16x32_bf16 v[244:247], v[120:123], a[84:87], v[244:247]
ds_read_b128 a[116:119], v25 offset:2048
v_mfma_f32_16x16x32_bf16 v[248:251], v[120:123], a[88:91], v[248:251]
ds_read_b128 a[120:123], v25 offset:4096
v_mfma_f32_16x16x32_bf16 v[252:255], v[120:123], a[92:95], v[252:255]
ds_read_b128 a[124:127], v25 offset:6144
s_add_u32 m0, s95, 102400
s_mov_b32 s92, 640
v_mfma_f32_16x16x32_bf16 v[176:179], v[124:127], a[80:83], v[176:179]
buffer_load_dwordx4 v46, s[12:15], s92 offen offset:0 lds
v_mfma_f32_16x16x32_bf16 v[180:183], v[124:127], a[84:87], v[180:183]
buffer_load_dwordx4 v47, s[12:15], s92 offen offset:1024 lds
v_mfma_f32_16x16x32_bf16 v[184:187], v[124:127], a[88:91], v[184:187]
buffer_load_dwordx4 v48, s[12:15], s92 offen offset:2048 lds
v_mfma_f32_16x16x32_bf16 v[188:191], v[124:127], a[92:95], v[188:191]
buffer_load_dwordx4 v49, s[12:15], s92 offen offset:3072 lds
s_waitcnt vmcnt(32) lgkmcnt(0)
s_barrier
v_mfma_f32_16x16x32_bf16 v[192:195], v[96:99], a[96:99], v[192:195]
ds_read_b64_tr_b16 v[112:113], v56 offset:50176
v_mfma_f32_16x16x32_bf16 v[196:199], v[96:99], a[100:103], v[196:199]
ds_read_b64_tr_b16 v[114:115], v56 offset:50432
v_mfma_f32_16x16x32_bf16 v[200:203], v[96:99], a[104:107], v[200:203]
ds_read_b64_tr_b16 v[116:117], v57 offset:50176
v_mfma_f32_16x16x32_bf16 v[204:207], v[96:99], a[108:111], v[204:207]
ds_read_b64_tr_b16 v[118:119], v57 offset:50432
v_mfma_f32_16x16x32_bf16 v[128:131], v[100:103], a[96:99], v[128:131]
ds_read_b64_tr_b16 v[120:121], v56 offset:50688
v_mfma_f32_16x16x32_bf16 v[132:135], v[100:103], a[100:103], v[132:135]
ds_read_b64_tr_b16 v[122:123], v56 offset:50944
v_mfma_f32_16x16x32_bf16 v[136:139], v[100:103], a[104:107], v[136:139]
ds_read_b64_tr_b16 v[124:125], v57 offset:50688
v_mfma_f32_16x16x32_bf16 v[140:143], v[100:103], a[108:111], v[140:143]
ds_read_b64_tr_b16 v[126:127], v57 offset:50944
v_mfma_f32_16x16x32_bf16 v[208:211], v[104:107], a[96:99], v[208:211]
s_add_u32 m0, s50, 32768
s_mov_b32 s93, 3145728
v_mfma_f32_16x16x32_bf16 v[212:215], v[104:107], a[100:103], v[212:215]
buffer_load_dwordx4 v4, s[16:19], s93 offen lds
v_mfma_f32_16x16x32_bf16 v[216:219], v[104:107], a[104:107], v[216:219]
v_mfma_f32_16x16x32_bf16 v[220:223], v[104:107], a[108:111], v[220:223]
buffer_load_dwordx4 v5, s[16:19], s93 offen offset:2048 lds
v_mfma_f32_16x16x32_bf16 v[144:147], v[108:111], a[96:99], v[144:147]
s_add_u32 m0, s50, 40960
s_mov_b32 s93, 3407872
v_mfma_f32_16x16x32_bf16 v[148:151], v[108:111], a[100:103], v[148:151]
buffer_load_dwordx4 v4, s[16:19], s93 offen lds
v_mfma_f32_16x16x32_bf16 v[152:155], v[108:111], a[104:107], v[152:155]
v_mfma_f32_16x16x32_bf16 v[156:159], v[108:111], a[108:111], v[156:159]
buffer_load_dwordx4 v5, s[16:19], s93 offen offset:2048 lds
s_waitcnt lgkmcnt(0)
v_mfma_f32_16x16x32_bf16 v[224:227], v[112:115], a[96:99], v[224:227]
ds_read_b64_tr_b16 v[96:97], v56 offset:57344
v_mfma_f32_16x16x32_bf16 v[228:231], v[112:115], a[100:103], v[228:231]
ds_read_b64_tr_b16 v[98:99], v56 offset:57600
v_mfma_f32_16x16x32_bf16 v[232:235], v[112:115], a[104:107], v[232:235]
ds_read_b64_tr_b16 v[100:101], v57 offset:57344
v_mfma_f32_16x16x32_bf16 v[236:239], v[112:115], a[108:111], v[236:239]
ds_read_b64_tr_b16 v[102:103], v57 offset:57600
v_mfma_f32_16x16x32_bf16 v[160:163], v[116:119], a[96:99], v[160:163]
ds_read_b64_tr_b16 v[104:105], v56 offset:57856
v_mfma_f32_16x16x32_bf16 v[164:167], v[116:119], a[100:103], v[164:167]
ds_read_b64_tr_b16 v[106:107], v56 offset:58112
v_mfma_f32_16x16x32_bf16 v[168:171], v[116:119], a[104:107], v[168:171]
ds_read_b64_tr_b16 v[108:109], v57 offset:57856
v_mfma_f32_16x16x32_bf16 v[172:175], v[116:119], a[108:111], v[172:175]
ds_read_b64_tr_b16 v[110:111], v57 offset:58112
v_mfma_f32_16x16x32_bf16 v[240:243], v[120:123], a[96:99], v[240:243]
s_add_u32 m0, s95, 131072
s_mov_b32 s92, 768
v_mfma_f32_16x16x32_bf16 v[244:247], v[120:123], a[100:103], v[244:247]
buffer_load_dwordx4 v42, s[12:15], s92 offen offset:0 lds
v_mfma_f32_16x16x32_bf16 v[248:251], v[120:123], a[104:107], v[248:251]
v_mfma_f32_16x16x32_bf16 v[252:255], v[120:123], a[108:111], v[252:255]
buffer_load_dwordx4 v43, s[12:15], s92 offen offset:1024 lds
v_mfma_f32_16x16x32_bf16 v[176:179], v[124:127], a[96:99], v[176:179]
v_mfma_f32_16x16x32_bf16 v[180:183], v[124:127], a[100:103], v[180:183]
buffer_load_dwordx4 v44, s[12:15], s92 offen offset:2048 lds
v_mfma_f32_16x16x32_bf16 v[184:187], v[124:127], a[104:107], v[184:187]
v_mfma_f32_16x16x32_bf16 v[188:191], v[124:127], a[108:111], v[188:191]
buffer_load_dwordx4 v45, s[12:15], s92 offen offset:3072 lds
s_waitcnt vmcnt(20) lgkmcnt(0)
s_barrier
v_mfma_f32_16x16x32_bf16 v[192:195], v[96:99], a[112:115], v[192:195]
ds_read_b64_tr_b16 v[112:113], v56 offset:58368
v_mfma_f32_16x16x32_bf16 v[196:199], v[96:99], a[116:119], v[196:199]
ds_read_b64_tr_b16 v[114:115], v56 offset:58624
v_mfma_f32_16x16x32_bf16 v[200:203], v[96:99], a[120:123], v[200:203]
ds_read_b64_tr_b16 v[116:117], v57 offset:58368
v_mfma_f32_16x16x32_bf16 v[204:207], v[96:99], a[124:127], v[204:207]
ds_read_b64_tr_b16 v[118:119], v57 offset:58624
v_mfma_f32_16x16x32_bf16 v[128:131], v[100:103], a[112:115], v[128:131]
ds_read_b64_tr_b16 v[120:121], v56 offset:58880
v_mfma_f32_16x16x32_bf16 v[132:135], v[100:103], a[116:119], v[132:135]
ds_read_b64_tr_b16 v[122:123], v56 offset:59136
v_mfma_f32_16x16x32_bf16 v[136:139], v[100:103], a[120:123], v[136:139]
ds_read_b64_tr_b16 v[124:125], v57 offset:58880
v_mfma_f32_16x16x32_bf16 v[140:143], v[100:103], a[124:127], v[140:143]
ds_read_b64_tr_b16 v[126:127], v57 offset:59136
v_mfma_f32_16x16x32_bf16 v[208:211], v[104:107], a[112:115], v[208:211]
ds_read_b128 a[128:131], v40 offset:0
v_mfma_f32_16x16x32_bf16 v[212:215], v[104:107], a[116:119], v[212:215]
ds_read_b128 a[132:135], v40 offset:2048
v_mfma_f32_16x16x32_bf16 v[216:219], v[104:107], a[120:123], v[216:219]
ds_read_b128 a[136:139], v40 offset:4096
v_mfma_f32_16x16x32_bf16 v[220:223], v[104:107], a[124:127], v[220:223]
ds_read_b128 a[140:143], v40 offset:6144
s_add_u32 m0, s95, 135168
s_mov_b32 s92, 768
v_mfma_f32_16x16x32_bf16 v[144:147], v[108:111], a[112:115], v[144:147]
buffer_load_dwordx4 v46, s[12:15], s92 offen offset:0 lds
v_mfma_f32_16x16x32_bf16 v[148:151], v[108:111], a[116:119], v[148:151]
buffer_load_dwordx4 v47, s[12:15], s92 offen offset:1024 lds
v_mfma_f32_16x16x32_bf16 v[152:155], v[108:111], a[120:123], v[152:155]
buffer_load_dwordx4 v48, s[12:15], s92 offen offset:2048 lds
v_mfma_f32_16x16x32_bf16 v[156:159], v[108:111], a[124:127], v[156:159]
buffer_load_dwordx4 v49, s[12:15], s92 offen offset:3072 lds
s_waitcnt lgkmcnt(4)
v_mfma_f32_16x16x32_bf16 v[224:227], v[112:115], a[112:115], v[224:227]
ds_read_b64_tr_b16 v[96:97], v56 offset:0
v_mfma_f32_16x16x32_bf16 v[228:231], v[112:115], a[116:119], v[228:231]
ds_read_b64_tr_b16 v[98:99], v56 offset:256
v_mfma_f32_16x16x32_bf16 v[232:235], v[112:115], a[120:123], v[232:235]
ds_read_b64_tr_b16 v[100:101], v57 offset:0
v_mfma_f32_16x16x32_bf16 v[236:239], v[112:115], a[124:127], v[236:239]
ds_read_b64_tr_b16 v[102:103], v57 offset:256
v_mfma_f32_16x16x32_bf16 v[160:163], v[116:119], a[112:115], v[160:163]
ds_read_b64_tr_b16 v[104:105], v56 offset:512
v_mfma_f32_16x16x32_bf16 v[164:167], v[116:119], a[116:119], v[164:167]
ds_read_b64_tr_b16 v[106:107], v56 offset:768
v_mfma_f32_16x16x32_bf16 v[168:171], v[116:119], a[120:123], v[168:171]
ds_read_b64_tr_b16 v[108:109], v57 offset:512
v_mfma_f32_16x16x32_bf16 v[172:175], v[116:119], a[124:127], v[172:175]
ds_read_b64_tr_b16 v[110:111], v57 offset:768
v_mfma_f32_16x16x32_bf16 v[240:243], v[120:123], a[112:115], v[240:243]
ds_read_b128 a[144:147], v41 offset:0
v_mfma_f32_16x16x32_bf16 v[244:247], v[120:123], a[116:119], v[244:247]
ds_read_b128 a[148:151], v41 offset:2048
v_mfma_f32_16x16x32_bf16 v[248:251], v[120:123], a[120:123], v[248:251]
ds_read_b128 a[152:155], v41 offset:4096
v_mfma_f32_16x16x32_bf16 v[252:255], v[120:123], a[124:127], v[252:255]
ds_read_b128 a[156:159], v41 offset:6144
s_add_u32 m0, s50, 49152
s_mov_b32 s93, 3670016
v_mfma_f32_16x16x32_bf16 v[176:179], v[124:127], a[112:115], v[176:179]
buffer_load_dwordx4 v4, s[16:19], s93 offen lds
v_mfma_f32_16x16x32_bf16 v[180:183], v[124:127], a[116:119], v[180:183]
buffer_load_dwordx4 v5, s[16:19], s93 offen offset:2048 lds
v_mfma_f32_16x16x32_bf16 v[184:187], v[124:127], a[120:123], v[184:187]
v_mfma_f32_16x16x32_bf16 v[188:191], v[124:127], a[124:127], v[188:191]
s_waitcnt vmcnt(34) lgkmcnt(0)
s_barrier
v_mfma_f32_16x16x32_bf16 v[192:195], v[96:99], a[128:131], v[192:195]
ds_read_b64_tr_b16 v[112:113], v56 offset:1024
v_mfma_f32_16x16x32_bf16 v[196:199], v[96:99], a[132:135], v[196:199]
ds_read_b64_tr_b16 v[114:115], v56 offset:1280
v_mfma_f32_16x16x32_bf16 v[200:203], v[96:99], a[136:139], v[200:203]
ds_read_b64_tr_b16 v[116:117], v57 offset:1024
v_mfma_f32_16x16x32_bf16 v[204:207], v[96:99], a[140:143], v[204:207]
ds_read_b64_tr_b16 v[118:119], v57 offset:1280
v_mfma_f32_16x16x32_bf16 v[128:131], v[100:103], a[128:131], v[128:131]
ds_read_b64_tr_b16 v[120:121], v56 offset:1536
v_mfma_f32_16x16x32_bf16 v[132:135], v[100:103], a[132:135], v[132:135]
ds_read_b64_tr_b16 v[122:123], v56 offset:1792
v_mfma_f32_16x16x32_bf16 v[136:139], v[100:103], a[136:139], v[136:139]
ds_read_b64_tr_b16 v[124:125], v57 offset:1536
v_mfma_f32_16x16x32_bf16 v[140:143], v[100:103], a[140:143], v[140:143]
)ASM"
R"ASM(
ds_read_b64_tr_b16 v[126:127], v57 offset:1792
v_mfma_f32_16x16x32_bf16 v[208:211], v[104:107], a[128:131], v[208:211]
s_add_u32 m0, s50, 57344
s_mov_b32 s93, 3932160
v_mfma_f32_16x16x32_bf16 v[212:215], v[104:107], a[132:135], v[212:215]
buffer_load_dwordx4 v4, s[16:19], s93 offen lds
v_mfma_f32_16x16x32_bf16 v[216:219], v[104:107], a[136:139], v[216:219]
v_mfma_f32_16x16x32_bf16 v[220:223], v[104:107], a[140:143], v[220:223]
buffer_load_dwordx4 v5, s[16:19], s93 offen offset:2048 lds
v_mfma_f32_16x16x32_bf16 v[144:147], v[108:111], a[128:131], v[144:147]
s_add_u32 m0, s95, 65536
s_mov_b32 s92, 896
v_mfma_f32_16x16x32_bf16 v[148:151], v[108:111], a[132:135], v[148:151]
buffer_load_dwordx4 v42, s[12:15], s92 offen offset:0 lds
v_mfma_f32_16x16x32_bf16 v[152:155], v[108:111], a[136:139], v[152:155]
v_mfma_f32_16x16x32_bf16 v[156:159], v[108:111], a[140:143], v[156:159]
buffer_load_dwordx4 v43, s[12:15], s92 offen offset:1024 lds
s_waitcnt lgkmcnt(0)
v_mfma_f32_16x16x32_bf16 v[224:227], v[112:115], a[128:131], v[224:227]
ds_read_b64_tr_b16 v[96:97], v56 offset:8192
v_mfma_f32_16x16x32_bf16 v[228:231], v[112:115], a[132:135], v[228:231]
ds_read_b64_tr_b16 v[98:99], v56 offset:8448
v_mfma_f32_16x16x32_bf16 v[232:235], v[112:115], a[136:139], v[232:235]
ds_read_b64_tr_b16 v[100:101], v57 offset:8192
v_mfma_f32_16x16x32_bf16 v[236:239], v[112:115], a[140:143], v[236:239]
ds_read_b64_tr_b16 v[102:103], v57 offset:8448
v_mfma_f32_16x16x32_bf16 v[160:163], v[116:119], a[128:131], v[160:163]
ds_read_b64_tr_b16 v[104:105], v56 offset:8704
v_mfma_f32_16x16x32_bf16 v[164:167], v[116:119], a[132:135], v[164:167]
ds_read_b64_tr_b16 v[106:107], v56 offset:8960
v_mfma_f32_16x16x32_bf16 v[168:171], v[116:119], a[136:139], v[168:171]
ds_read_b64_tr_b16 v[108:109], v57 offset:8704
v_mfma_f32_16x16x32_bf16 v[172:175], v[116:119], a[140:143], v[172:175]
ds_read_b64_tr_b16 v[110:111], v57 offset:8960
v_mfma_f32_16x16x32_bf16 v[240:243], v[120:123], a[128:131], v[240:243]
v_mfma_f32_16x16x32_bf16 v[244:247], v[120:123], a[132:135], v[244:247]
buffer_load_dwordx4 v44, s[12:15], s92 offen offset:2048 lds
v_mfma_f32_16x16x32_bf16 v[248:251], v[120:123], a[136:139], v[248:251]
v_mfma_f32_16x16x32_bf16 v[252:255], v[120:123], a[140:143], v[252:255]
buffer_load_dwordx4 v45, s[12:15], s92 offen offset:3072 lds
v_mfma_f32_16x16x32_bf16 v[176:179], v[124:127], a[128:131], v[176:179]
s_add_u32 m0, s95, 69632
s_mov_b32 s92, 896
v_mfma_f32_16x16x32_bf16 v[180:183], v[124:127], a[132:135], v[180:183]
buffer_load_dwordx4 v46, s[12:15], s92 offen offset:0 lds
v_mfma_f32_16x16x32_bf16 v[184:187], v[124:127], a[136:139], v[184:187]
v_mfma_f32_16x16x32_bf16 v[188:191], v[124:127], a[140:143], v[188:191]
buffer_load_dwordx4 v47, s[12:15], s92 offen offset:1024 lds
s_waitcnt vmcnt(22) lgkmcnt(0)
s_barrier
v_mfma_f32_16x16x32_bf16 v[192:195], v[96:99], a[144:147], v[192:195]
ds_read_b64_tr_b16 v[112:113], v56 offset:9216
v_mfma_f32_16x16x32_bf16 v[196:199], v[96:99], a[148:151], v[196:199]
ds_read_b64_tr_b16 v[114:115], v56 offset:9472
v_mfma_f32_16x16x32_bf16 v[200:203], v[96:99], a[152:155], v[200:203]
ds_read_b64_tr_b16 v[116:117], v57 offset:9216
v_mfma_f32_16x16x32_bf16 v[204:207], v[96:99], a[156:159], v[204:207]
ds_read_b64_tr_b16 v[118:119], v57 offset:9472
v_mfma_f32_16x16x32_bf16 v[128:131], v[100:103], a[144:147], v[128:131]
ds_read_b64_tr_b16 v[120:121], v56 offset:9728
v_mfma_f32_16x16x32_bf16 v[132:135], v[100:103], a[148:151], v[132:135]
ds_read_b64_tr_b16 v[122:123], v56 offset:9984
v_mfma_f32_16x16x32_bf16 v[136:139], v[100:103], a[152:155], v[136:139]
ds_read_b64_tr_b16 v[124:125], v57 offset:9728
v_mfma_f32_16x16x32_bf16 v[140:143], v[100:103], a[156:159], v[140:143]
ds_read_b64_tr_b16 v[126:127], v57 offset:9984
v_mfma_f32_16x16x32_bf16 v[208:211], v[104:107], a[144:147], v[208:211]
ds_read_b128 a[160:163], v40 offset:32768
v_mfma_f32_16x16x32_bf16 v[212:215], v[104:107], a[148:151], v[212:215]
ds_read_b128 a[164:167], v40 offset:34816
v_mfma_f32_16x16x32_bf16 v[216:219], v[104:107], a[152:155], v[216:219]
ds_read_b128 a[168:171], v40 offset:36864
v_mfma_f32_16x16x32_bf16 v[220:223], v[104:107], a[156:159], v[220:223]
ds_read_b128 a[172:175], v40 offset:38912
v_mfma_f32_16x16x32_bf16 v[144:147], v[108:111], a[144:147], v[144:147]
buffer_load_dwordx4 v48, s[12:15], s92 offen offset:2048 lds
v_mfma_f32_16x16x32_bf16 v[148:151], v[108:111], a[148:151], v[148:151]
buffer_load_dwordx4 v49, s[12:15], s92 offen offset:3072 lds
s_add_u32 m0, s50, 0
s_mov_b32 s93, 0
v_mfma_f32_16x16x32_bf16 v[152:155], v[108:111], a[152:155], v[152:155]
buffer_load_dwordx4 v4, s[20:23], s93 offen lds
v_mfma_f32_16x16x32_bf16 v[156:159], v[108:111], a[156:159], v[156:159]
buffer_load_dwordx4 v5, s[20:23], s93 offen offset:2048 lds
s_waitcnt lgkmcnt(4)
v_mfma_f32_16x16x32_bf16 v[224:227], v[112:115], a[144:147], v[224:227]
ds_read_b64_tr_b16 v[96:97], v56 offset:16384
v_mfma_f32_16x16x32_bf16 v[228:231], v[112:115], a[148:151], v[228:231]
ds_read_b64_tr_b16 v[98:99], v56 offset:16640
v_mfma_f32_16x16x32_bf16 v[232:235], v[112:115], a[152:155], v[232:235]
ds_read_b64_tr_b16 v[100:101], v57 offset:16384
v_mfma_f32_16x16x32_bf16 v[236:239], v[112:115], a[156:159], v[236:239]
ds_read_b64_tr_b16 v[102:103], v57 offset:16640
v_mfma_f32_16x16x32_bf16 v[160:163], v[116:119], a[144:147], v[160:163]
ds_read_b64_tr_b16 v[104:105], v56 offset:16896
v_mfma_f32_16x16x32_bf16 v[164:167], v[116:119], a[148:151], v[164:167]
ds_read_b64_tr_b16 v[106:107], v56 offset:17152
v_mfma_f32_16x16x32_bf16 v[168:171], v[116:119], a[152:155], v[168:171]
ds_read_b64_tr_b16 v[108:109], v57 offset:16896
v_mfma_f32_16x16x32_bf16 v[172:175], v[116:119], a[156:159], v[172:175]
ds_read_b64_tr_b16 v[110:111], v57 offset:17152
v_mfma_f32_16x16x32_bf16 v[240:243], v[120:123], a[144:147], v[240:243]
ds_read_b128 a[176:179], v41 offset:32768
v_mfma_f32_16x16x32_bf16 v[244:247], v[120:123], a[148:151], v[244:247]
ds_read_b128 a[180:183], v41 offset:34816
v_mfma_f32_16x16x32_bf16 v[248:251], v[120:123], a[152:155], v[248:251]
ds_read_b128 a[184:187], v41 offset:36864
v_mfma_f32_16x16x32_bf16 v[252:255], v[120:123], a[156:159], v[252:255]
ds_read_b128 a[188:191], v41 offset:38912
v_mfma_f32_16x16x32_bf16 v[176:179], v[124:127], a[144:147], v[176:179]
v_mfma_f32_16x16x32_bf16 v[180:183], v[124:127], a[148:151], v[180:183]
v_mfma_f32_16x16x32_bf16 v[184:187], v[124:127], a[152:155], v[184:187]
v_mfma_f32_16x16x32_bf16 v[188:191], v[124:127], a[156:159], v[188:191]
s_waitcnt vmcnt(34) lgkmcnt(0)
s_barrier
v_mfma_f32_16x16x32_bf16 v[192:195], v[96:99], a[160:163], v[192:195]
ds_read_b64_tr_b16 v[112:113], v56 offset:17408
v_mfma_f32_16x16x32_bf16 v[196:199], v[96:99], a[164:167], v[196:199]
ds_read_b64_tr_b16 v[114:115], v56 offset:17664
v_mfma_f32_16x16x32_bf16 v[200:203], v[96:99], a[168:171], v[200:203]
ds_read_b64_tr_b16 v[116:117], v57 offset:17408
v_mfma_f32_16x16x32_bf16 v[204:207], v[96:99], a[172:175], v[204:207]
ds_read_b64_tr_b16 v[118:119], v57 offset:17664
v_mfma_f32_16x16x32_bf16 v[128:131], v[100:103], a[160:163], v[128:131]
ds_read_b64_tr_b16 v[120:121], v56 offset:17920
v_mfma_f32_16x16x32_bf16 v[132:135], v[100:103], a[164:167], v[132:135]
ds_read_b64_tr_b16 v[122:123], v56 offset:18176
v_mfma_f32_16x16x32_bf16 v[136:139], v[100:103], a[168:171], v[136:139]
ds_read_b64_tr_b16 v[124:125], v57 offset:17920
v_mfma_f32_16x16x32_bf16 v[140:143], v[100:103], a[172:175], v[140:143]
ds_read_b64_tr_b16 v[126:127], v57 offset:18176
v_mfma_f32_16x16x32_bf16 v[208:211], v[104:107], a[160:163], v[208:211]
s_add_u32 m0, s50, 8192
s_mov_b32 s93, 262144
buffer_load_dword v63, v53, s[28:31], 0 offen
v_mfma_f32_16x16x32_bf16 v[212:215], v[104:107], a[164:167], v[212:215]
buffer_load_dwordx4 v4, s[20:23], s93 offen lds
v_mfma_f32_16x16x32_bf16 v[216:219], v[104:107], a[168:171], v[216:219]
buffer_load_dword v62, v54, s[32:35], 0 offen
v_mfma_f32_16x16x32_bf16 v[220:223], v[104:107], a[172:175], v[220:223]
buffer_load_dwordx4 v5, s[20:23], s93 offen offset:2048 lds
v_mfma_f32_16x16x32_bf16 v[144:147], v[108:111], a[160:163], v[144:147]
v_mfma_f32_16x16x32_bf16 v[148:151], v[108:111], a[164:167], v[148:151]
v_mfma_f32_16x16x32_bf16 v[152:155], v[108:111], a[168:171], v[152:155]
v_mfma_f32_16x16x32_bf16 v[156:159], v[108:111], a[172:175], v[156:159]
s_waitcnt lgkmcnt(0)
v_mfma_f32_16x16x32_bf16 v[224:227], v[112:115], a[160:163], v[224:227]
ds_read_b64_tr_b16 v[96:97], v56 offset:24576
v_mfma_f32_16x16x32_bf16 v[228:231], v[112:115], a[164:167], v[228:231]
ds_read_b64_tr_b16 v[98:99], v56 offset:24832
v_mfma_f32_16x16x32_bf16 v[232:235], v[112:115], a[168:171], v[232:235]
ds_read_b64_tr_b16 v[100:101], v57 offset:24576
v_mfma_f32_16x16x32_bf16 v[236:239], v[112:115], a[172:175], v[236:239]
ds_read_b64_tr_b16 v[102:103], v57 offset:24832
v_mfma_f32_16x16x32_bf16 v[160:163], v[116:119], a[160:163], v[160:163]
ds_read_b64_tr_b16 v[104:105], v56 offset:25088
v_mfma_f32_16x16x32_bf16 v[164:167], v[116:119], a[164:167], v[164:167]
ds_read_b64_tr_b16 v[106:107], v56 offset:25344
v_mfma_f32_16x16x32_bf16 v[168:171], v[116:119], a[168:171], v[168:171]
ds_read_b64_tr_b16 v[108:109], v57 offset:25088
v_mfma_f32_16x16x32_bf16 v[172:175], v[116:119], a[172:175], v[172:175]
ds_read_b64_tr_b16 v[110:111], v57 offset:25344
v_mfma_f32_16x16x32_bf16 v[240:243], v[120:123], a[160:163], v[240:243]
v_mfma_f32_16x16x32_bf16 v[244:247], v[120:123], a[164:167], v[244:247]
v_mfma_f32_16x16x32_bf16 v[248:251], v[120:123], a[168:171], v[248:251]
v_mfma_f32_16x16x32_bf16 v[252:255], v[120:123], a[172:175], v[252:255]
v_mfma_f32_16x16x32_bf16 v[176:179], v[124:127], a[160:163], v[176:179]
v_mfma_f32_16x16x32_bf16 v[180:183], v[124:127], a[164:167], v[180:183]
v_mfma_f32_16x16x32_bf16 v[184:187], v[124:127], a[168:171], v[184:187]
v_mfma_f32_16x16x32_bf16 v[188:191], v[124:127], a[172:175], v[188:191]
s_waitcnt vmcnt(18) lgkmcnt(0)
s_barrier
v_mfma_f32_16x16x32_bf16 v[192:195], v[96:99], a[176:179], v[192:195]
ds_read_b64_tr_b16 v[112:113], v56 offset:25600
v_mfma_f32_16x16x32_bf16 v[196:199], v[96:99], a[180:183], v[196:199]
ds_read_b64_tr_b16 v[114:115], v56 offset:25856
v_mfma_f32_16x16x32_bf16 v[200:203], v[96:99], a[184:187], v[200:203]
ds_read_b64_tr_b16 v[116:117], v57 offset:25600
v_mfma_f32_16x16x32_bf16 v[204:207], v[96:99], a[188:191], v[204:207]
ds_read_b64_tr_b16 v[118:119], v57 offset:25856
v_mfma_f32_16x16x32_bf16 v[128:131], v[100:103], a[176:179], v[128:131]
ds_read_b64_tr_b16 v[120:121], v56 offset:26112
v_mfma_f32_16x16x32_bf16 v[132:135], v[100:103], a[180:183], v[132:135]
ds_read_b64_tr_b16 v[122:123], v56 offset:26368
v_mfma_f32_16x16x32_bf16 v[136:139], v[100:103], a[184:187], v[136:139]
ds_read_b64_tr_b16 v[124:125], v57 offset:26112
v_mfma_f32_16x16x32_bf16 v[140:143], v[100:103], a[188:191], v[140:143]
ds_read_b64_tr_b16 v[126:127], v57 offset:26368
v_mfma_f32_16x16x32_bf16 v[208:211], v[104:107], a[176:179], v[208:211]
ds_read_b128 a[192:195], v24 offset:0
v_mfma_f32_16x16x32_bf16 v[212:215], v[104:107], a[180:183], v[212:215]
ds_read_b128 a[196:199], v24 offset:2048
v_mfma_f32_16x16x32_bf16 v[216:219], v[104:107], a[184:187], v[216:219]
ds_read_b128 a[200:203], v24 offset:4096
v_mfma_f32_16x16x32_bf16 v[220:223], v[104:107], a[188:191], v[220:223]
ds_read_b128 a[204:207], v24 offset:6144
s_add_u32 m0, s50, 16384
s_mov_b32 s93, 524288
v_mfma_f32_16x16x32_bf16 v[144:147], v[108:111], a[176:179], v[144:147]
buffer_load_dwordx4 v4, s[20:23], s93 offen lds
)ASM"
R"ASM(
v_mfma_f32_16x16x32_bf16 v[148:151], v[108:111], a[180:183], v[148:151]
buffer_load_dwordx4 v5, s[20:23], s93 offen offset:2048 lds
s_add_u32 m0, s50, 98304
s_mov_b32 s93, 2097152
v_mfma_f32_16x16x32_bf16 v[152:155], v[108:111], a[184:187], v[152:155]
buffer_load_dwordx4 v4, s[20:23], s93 offen lds
v_mfma_f32_16x16x32_bf16 v[156:159], v[108:111], a[188:191], v[156:159]
buffer_load_dwordx4 v5, s[20:23], s93 offen offset:2048 lds
s_waitcnt lgkmcnt(4)
v_mfma_f32_16x16x32_bf16 v[224:227], v[112:115], a[176:179], v[224:227]
ds_read_b64_tr_b16 v[96:97], v56 offset:32768
v_mfma_f32_16x16x32_bf16 v[228:231], v[112:115], a[180:183], v[228:231]
ds_read_b64_tr_b16 v[98:99], v56 offset:33024
v_mfma_f32_16x16x32_bf16 v[232:235], v[112:115], a[184:187], v[232:235]
ds_read_b64_tr_b16 v[100:101], v57 offset:32768
v_mfma_f32_16x16x32_bf16 v[236:239], v[112:115], a[188:191], v[236:239]
ds_read_b64_tr_b16 v[102:103], v57 offset:33024
v_mfma_f32_16x16x32_bf16 v[160:163], v[116:119], a[176:179], v[160:163]
ds_read_b64_tr_b16 v[104:105], v56 offset:33280
v_mfma_f32_16x16x32_bf16 v[164:167], v[116:119], a[180:183], v[164:167]
ds_read_b64_tr_b16 v[106:107], v56 offset:33536
v_mfma_f32_16x16x32_bf16 v[168:171], v[116:119], a[184:187], v[168:171]
ds_read_b64_tr_b16 v[108:109], v57 offset:33280
v_mfma_f32_16x16x32_bf16 v[172:175], v[116:119], a[188:191], v[172:175]
ds_read_b64_tr_b16 v[110:111], v57 offset:33536
v_mfma_f32_16x16x32_bf16 v[240:243], v[120:123], a[176:179], v[240:243]
ds_read_b128 a[208:211], v25 offset:0
v_mfma_f32_16x16x32_bf16 v[244:247], v[120:123], a[180:183], v[244:247]
ds_read_b128 a[212:215], v25 offset:2048
v_mfma_f32_16x16x32_bf16 v[248:251], v[120:123], a[184:187], v[248:251]
ds_read_b128 a[216:219], v25 offset:4096
v_mfma_f32_16x16x32_bf16 v[252:255], v[120:123], a[188:191], v[252:255]
ds_read_b128 a[220:223], v25 offset:6144
s_add_u32 m0, s50, 106496
s_mov_b32 s93, 2359296
v_mfma_f32_16x16x32_bf16 v[176:179], v[124:127], a[176:179], v[176:179]
buffer_load_dwordx4 v4, s[20:23], s93 offen lds
v_mfma_f32_16x16x32_bf16 v[180:183], v[124:127], a[180:183], v[180:183]
buffer_load_dwordx4 v5, s[20:23], s93 offen offset:2048 lds
s_add_u32 m0, s50, 114688
s_mov_b32 s93, 2621440
v_mfma_f32_16x16x32_bf16 v[184:187], v[124:127], a[184:187], v[184:187]
buffer_load_dwordx4 v4, s[20:23], s93 offen lds
v_mfma_f32_16x16x32_bf16 v[188:191], v[124:127], a[188:191], v[188:191]
buffer_load_dwordx4 v5, s[20:23], s93 offen offset:2048 lds
s_waitcnt vmcnt(34) lgkmcnt(0)
s_barrier
v_mfma_f32_16x16x32_bf16 v[192:195], v[96:99], a[192:195], v[192:195]
ds_read_b64_tr_b16 v[112:113], v56 offset:33792
v_mfma_f32_16x16x32_bf16 v[196:199], v[96:99], a[196:199], v[196:199]
ds_read_b64_tr_b16 v[114:115], v56 offset:34048
v_mfma_f32_16x16x32_bf16 v[200:203], v[96:99], a[200:203], v[200:203]
ds_read_b64_tr_b16 v[116:117], v57 offset:33792
v_mfma_f32_16x16x32_bf16 v[204:207], v[96:99], a[204:207], v[204:207]
ds_read_b64_tr_b16 v[118:119], v57 offset:34048
v_mfma_f32_16x16x32_bf16 v[128:131], v[100:103], a[192:195], v[128:131]
ds_read_b64_tr_b16 v[120:121], v56 offset:34304
v_mfma_f32_16x16x32_bf16 v[132:135], v[100:103], a[196:199], v[132:135]
ds_read_b64_tr_b16 v[122:123], v56 offset:34560
v_mfma_f32_16x16x32_bf16 v[136:139], v[100:103], a[200:203], v[136:139]
ds_read_b64_tr_b16 v[124:125], v57 offset:34304
v_mfma_f32_16x16x32_bf16 v[140:143], v[100:103], a[204:207], v[140:143]
ds_read_b64_tr_b16 v[126:127], v57 offset:34560
v_mfma_f32_16x16x32_bf16 v[208:211], v[104:107], a[192:195], v[208:211]
s_add_u32 m0, s50, 122880
s_mov_b32 s93, 2883584
v_mfma_f32_16x16x32_bf16 v[212:215], v[104:107], a[196:199], v[212:215]
buffer_load_dwordx4 v4, s[20:23], s93 offen lds
v_mfma_f32_16x16x32_bf16 v[216:219], v[104:107], a[200:203], v[216:219]
v_mfma_f32_16x16x32_bf16 v[220:223], v[104:107], a[204:207], v[220:223]
buffer_load_dwordx4 v5, s[20:23], s93 offen offset:2048 lds
v_mfma_f32_16x16x32_bf16 v[144:147], v[108:111], a[192:195], v[144:147]
s_add_u32 m0, s50, 24576
s_mov_b32 s93, 786432
v_mfma_f32_16x16x32_bf16 v[148:151], v[108:111], a[196:199], v[148:151]
buffer_load_dwordx4 v4, s[20:23], s93 offen lds
v_mfma_f32_16x16x32_bf16 v[152:155], v[108:111], a[200:203], v[152:155]
v_mfma_f32_16x16x32_bf16 v[156:159], v[108:111], a[204:207], v[156:159]
buffer_load_dwordx4 v5, s[20:23], s93 offen offset:2048 lds
s_waitcnt lgkmcnt(0)
v_mfma_f32_16x16x32_bf16 v[224:227], v[112:115], a[192:195], v[224:227]
ds_read_b64_tr_b16 v[96:97], v56 offset:40960
v_mfma_f32_16x16x32_bf16 v[228:231], v[112:115], a[196:199], v[228:231]
ds_read_b64_tr_b16 v[98:99], v56 offset:41216
v_mfma_f32_16x16x32_bf16 v[232:235], v[112:115], a[200:203], v[232:235]
ds_read_b64_tr_b16 v[100:101], v57 offset:40960
v_mfma_f32_16x16x32_bf16 v[236:239], v[112:115], a[204:207], v[236:239]
ds_read_b64_tr_b16 v[102:103], v57 offset:41216
v_mfma_f32_16x16x32_bf16 v[160:163], v[116:119], a[192:195], v[160:163]
ds_read_b64_tr_b16 v[104:105], v56 offset:41472
v_mfma_f32_16x16x32_bf16 v[164:167], v[116:119], a[196:199], v[164:167]
ds_read_b64_tr_b16 v[106:107], v56 offset:41728
v_mfma_f32_16x16x32_bf16 v[168:171], v[116:119], a[200:203], v[168:171]
ds_read_b64_tr_b16 v[108:109], v57 offset:41472
v_mfma_f32_16x16x32_bf16 v[172:175], v[116:119], a[204:207], v[172:175]
ds_read_b64_tr_b16 v[110:111], v57 offset:41728
v_mfma_f32_16x16x32_bf16 v[240:243], v[120:123], a[192:195], v[240:243]
v_mfma_f32_16x16x32_bf16 v[244:247], v[120:123], a[196:199], v[244:247]
v_mfma_f32_16x16x32_bf16 v[248:251], v[120:123], a[200:203], v[248:251]
v_mfma_f32_16x16x32_bf16 v[252:255], v[120:123], a[204:207], v[252:255]
v_mfma_f32_16x16x32_bf16 v[176:179], v[124:127], a[192:195], v[176:179]
v_mfma_f32_16x16x32_bf16 v[180:183], v[124:127], a[196:199], v[180:183]
v_mfma_f32_16x16x32_bf16 v[184:187], v[124:127], a[200:203], v[184:187]
v_mfma_f32_16x16x32_bf16 v[188:191], v[124:127], a[204:207], v[188:191]
s_waitcnt vmcnt(18) lgkmcnt(0)
s_barrier
v_mfma_f32_16x16x32_bf16 v[192:195], v[96:99], a[208:211], v[192:195]
ds_read_b64_tr_b16 v[112:113], v56 offset:41984
v_mfma_f32_16x16x32_bf16 v[196:199], v[96:99], a[212:215], v[196:199]
ds_read_b64_tr_b16 v[114:115], v56 offset:42240
v_mfma_f32_16x16x32_bf16 v[200:203], v[96:99], a[216:219], v[200:203]
ds_read_b64_tr_b16 v[116:117], v57 offset:41984
v_mfma_f32_16x16x32_bf16 v[204:207], v[96:99], a[220:223], v[204:207]
ds_read_b64_tr_b16 v[118:119], v57 offset:42240
v_mfma_f32_16x16x32_bf16 v[128:131], v[100:103], a[208:211], v[128:131]
ds_read_b64_tr_b16 v[120:121], v56 offset:42496
v_mfma_f32_16x16x32_bf16 v[132:135], v[100:103], a[212:215], v[132:135]
ds_read_b64_tr_b16 v[122:123], v56 offset:42752
v_mfma_f32_16x16x32_bf16 v[136:139], v[100:103], a[216:219], v[136:139]
ds_read_b64_tr_b16 v[124:125], v57 offset:42496
v_mfma_f32_16x16x32_bf16 v[140:143], v[100:103], a[220:223], v[140:143]
ds_read_b64_tr_b16 v[126:127], v57 offset:42752
v_mfma_f32_16x16x32_bf16 v[208:211], v[104:107], a[208:211], v[208:211]
ds_read_b128 a[224:227], v40 offset:0
v_mfma_f32_16x16x32_bf16 v[212:215], v[104:107], a[212:215], v[212:215]
ds_read_b128 a[228:231], v40 offset:2048
v_mfma_f32_16x16x32_bf16 v[216:219], v[104:107], a[216:219], v[216:219]
ds_read_b128 a[232:235], v40 offset:4096
v_mfma_f32_16x16x32_bf16 v[220:223], v[104:107], a[220:223], v[220:223]
ds_read_b128 a[236:239], v40 offset:6144
s_add_u32 m0, s50, 32768
s_mov_b32 s93, 1048576
v_mfma_f32_16x16x32_bf16 v[144:147], v[108:111], a[208:211], v[144:147]
buffer_load_dwordx4 v4, s[20:23], s93 offen lds
v_mfma_f32_16x16x32_bf16 v[148:151], v[108:111], a[212:215], v[148:151]
buffer_load_dwordx4 v5, s[20:23], s93 offen offset:2048 lds
s_add_u32 m0, s50, 131072
s_mov_b32 s93, 3145728
v_mfma_f32_16x16x32_bf16 v[152:155], v[108:111], a[216:219], v[152:155]
buffer_load_dwordx4 v4, s[20:23], s93 offen lds
v_mfma_f32_16x16x32_bf16 v[156:159], v[108:111], a[220:223], v[156:159]
buffer_load_dwordx4 v5, s[20:23], s93 offen offset:2048 lds
s_waitcnt lgkmcnt(4)
v_mfma_f32_16x16x32_bf16 v[224:227], v[112:115], a[208:211], v[224:227]
ds_read_b64_tr_b16 v[96:97], v56 offset:49152
v_mfma_f32_16x16x32_bf16 v[228:231], v[112:115], a[212:215], v[228:231]
ds_read_b64_tr_b16 v[98:99], v56 offset:49408
v_mfma_f32_16x16x32_bf16 v[232:235], v[112:115], a[216:219], v[232:235]
ds_read_b64_tr_b16 v[100:101], v57 offset:49152
v_mfma_f32_16x16x32_bf16 v[236:239], v[112:115], a[220:223], v[236:239]
ds_read_b64_tr_b16 v[102:103], v57 offset:49408
v_mfma_f32_16x16x32_bf16 v[160:163], v[116:119], a[208:211], v[160:163]
ds_read_b64_tr_b16 v[104:105], v56 offset:49664
v_mfma_f32_16x16x32_bf16 v[164:167], v[116:119], a[212:215], v[164:167]
ds_read_b64_tr_b16 v[106:107], v56 offset:49920
v_mfma_f32_16x16x32_bf16 v[168:171], v[116:119], a[216:219], v[168:171]
ds_read_b64_tr_b16 v[108:109], v57 offset:49664
v_mfma_f32_16x16x32_bf16 v[172:175], v[116:119], a[220:223], v[172:175]
ds_read_b64_tr_b16 v[110:111], v57 offset:49920
v_mfma_f32_16x16x32_bf16 v[240:243], v[120:123], a[208:211], v[240:243]
ds_read_b128 a[240:243], v41 offset:0
v_mfma_f32_16x16x32_bf16 v[244:247], v[120:123], a[212:215], v[244:247]
ds_read_b128 a[244:247], v41 offset:2048
v_mfma_f32_16x16x32_bf16 v[248:251], v[120:123], a[216:219], v[248:251]
ds_read_b128 a[248:251], v41 offset:4096
v_mfma_f32_16x16x32_bf16 v[252:255], v[120:123], a[220:223], v[252:255]
ds_read_b128 a[252:255], v41 offset:6144
s_add_u32 m0, s50, 139264
s_mov_b32 s93, 3407872
v_mfma_f32_16x16x32_bf16 v[176:179], v[124:127], a[208:211], v[176:179]
buffer_load_dwordx4 v4, s[20:23], s93 offen lds
v_mfma_f32_16x16x32_bf16 v[180:183], v[124:127], a[212:215], v[180:183]
buffer_load_dwordx4 v5, s[20:23], s93 offen offset:2048 lds
s_add_u32 m0, s50, 147456
s_mov_b32 s93, 3670016
v_mfma_f32_16x16x32_bf16 v[184:187], v[124:127], a[216:219], v[184:187]
buffer_load_dwordx4 v4, s[20:23], s93 offen lds
v_mfma_f32_16x16x32_bf16 v[188:191], v[124:127], a[220:223], v[188:191]
buffer_load_dwordx4 v5, s[20:23], s93 offen offset:2048 lds
s_waitcnt vmcnt(34) lgkmcnt(0)
s_barrier
v_mfma_f32_16x16x32_bf16 v[192:195], v[96:99], a[224:227], v[192:195]
ds_read_b64_tr_b16 v[112:113], v56 offset:50176
v_mfma_f32_16x16x32_bf16 v[196:199], v[96:99], a[228:231], v[196:199]
ds_read_b64_tr_b16 v[114:115], v56 offset:50432
v_mfma_f32_16x16x32_bf16 v[200:203], v[96:99], a[232:235], v[200:203]
ds_read_b64_tr_b16 v[116:117], v57 offset:50176
v_mfma_f32_16x16x32_bf16 v[204:207], v[96:99], a[236:239], v[204:207]
ds_read_b64_tr_b16 v[118:119], v57 offset:50432
v_mfma_f32_16x16x32_bf16 v[128:131], v[100:103], a[224:227], v[128:131]
ds_read_b64_tr_b16 v[120:121], v56 offset:50688
v_mfma_f32_16x16x32_bf16 v[132:135], v[100:103], a[228:231], v[132:135]
ds_read_b64_tr_b16 v[122:123], v56 offset:50944
v_mfma_f32_16x16x32_bf16 v[136:139], v[100:103], a[232:235], v[136:139]
ds_read_b64_tr_b16 v[124:125], v57 offset:50688
v_mfma_f32_16x16x32_bf16 v[140:143], v[100:103], a[236:239], v[140:143]
ds_read_b64_tr_b16 v[126:127], v57 offset:50944
v_mfma_f32_16x16x32_bf16 v[208:211], v[104:107], a[224:227], v[208:211]
s_add_u32 m0, s50, 155648
s_mov_b32 s93, 3932160
)ASM"
R"ASM(
buffer_load_dwordx4 v[64:67], v50, s[24:27], 0 offen
v_mfma_f32_16x16x32_bf16 v[212:215], v[104:107], a[228:231], v[212:215]
buffer_load_dwordx4 v4, s[20:23], s93 offen lds
v_mfma_f32_16x16x32_bf16 v[216:219], v[104:107], a[232:235], v[216:219]
buffer_load_dwordx4 v[68:71], v50, s[24:27], 0 offen offset:512
v_mfma_f32_16x16x32_bf16 v[220:223], v[104:107], a[236:239], v[220:223]
buffer_load_dwordx4 v5, s[20:23], s93 offen offset:2048 lds
v_mfma_f32_16x16x32_bf16 v[144:147], v[108:111], a[224:227], v[144:147]
s_add_u32 m0, s50, 40960
s_mov_b32 s93, 1310720
v_mfma_f32_16x16x32_bf16 v[148:151], v[108:111], a[228:231], v[148:151]
buffer_load_dwordx4 v4, s[20:23], s93 offen lds
v_mfma_f32_16x16x32_bf16 v[152:155], v[108:111], a[232:235], v[152:155]
v_mfma_f32_16x16x32_bf16 v[156:159], v[108:111], a[236:239], v[156:159]
buffer_load_dwordx4 v5, s[20:23], s93 offen offset:2048 lds
s_waitcnt lgkmcnt(0)
v_mfma_f32_16x16x32_bf16 v[224:227], v[112:115], a[224:227], v[224:227]
ds_read_b64_tr_b16 v[96:97], v56 offset:57344
v_mfma_f32_16x16x32_bf16 v[228:231], v[112:115], a[228:231], v[228:231]
ds_read_b64_tr_b16 v[98:99], v56 offset:57600
v_mfma_f32_16x16x32_bf16 v[232:235], v[112:115], a[232:235], v[232:235]
ds_read_b64_tr_b16 v[100:101], v57 offset:57344
v_mfma_f32_16x16x32_bf16 v[236:239], v[112:115], a[236:239], v[236:239]
ds_read_b64_tr_b16 v[102:103], v57 offset:57600
v_mfma_f32_16x16x32_bf16 v[160:163], v[116:119], a[224:227], v[160:163]
ds_read_b64_tr_b16 v[104:105], v56 offset:57856
v_mfma_f32_16x16x32_bf16 v[164:167], v[116:119], a[228:231], v[164:167]
ds_read_b64_tr_b16 v[106:107], v56 offset:58112
v_mfma_f32_16x16x32_bf16 v[168:171], v[116:119], a[232:235], v[168:171]
ds_read_b64_tr_b16 v[108:109], v57 offset:57856
v_mfma_f32_16x16x32_bf16 v[172:175], v[116:119], a[236:239], v[172:175]
ds_read_b64_tr_b16 v[110:111], v57 offset:58112
v_mfma_f32_16x16x32_bf16 v[240:243], v[120:123], a[224:227], v[240:243]
v_mfma_f32_16x16x32_bf16 v[244:247], v[120:123], a[228:231], v[244:247]
v_mfma_f32_16x16x32_bf16 v[248:251], v[120:123], a[232:235], v[248:251]
v_mfma_f32_16x16x32_bf16 v[252:255], v[120:123], a[236:239], v[252:255]
v_mfma_f32_16x16x32_bf16 v[176:179], v[124:127], a[224:227], v[176:179]
v_mfma_f32_16x16x32_bf16 v[180:183], v[124:127], a[228:231], v[180:183]
v_mfma_f32_16x16x32_bf16 v[184:187], v[124:127], a[232:235], v[184:187]
v_mfma_f32_16x16x32_bf16 v[188:191], v[124:127], a[236:239], v[188:191]
s_waitcnt vmcnt(30) lgkmcnt(0)
s_barrier
v_mfma_f32_16x16x32_bf16 v[192:195], v[96:99], a[240:243], v[192:195]
ds_read_b64_tr_b16 v[112:113], v56 offset:58368
v_mfma_f32_16x16x32_bf16 v[196:199], v[96:99], a[244:247], v[196:199]
ds_read_b64_tr_b16 v[114:115], v56 offset:58624
v_mfma_f32_16x16x32_bf16 v[200:203], v[96:99], a[248:251], v[200:203]
ds_read_b64_tr_b16 v[116:117], v57 offset:58368
v_mfma_f32_16x16x32_bf16 v[204:207], v[96:99], a[252:255], v[204:207]
ds_read_b64_tr_b16 v[118:119], v57 offset:58624
v_mfma_f32_16x16x32_bf16 v[128:131], v[100:103], a[240:243], v[128:131]
ds_read_b64_tr_b16 v[120:121], v56 offset:58880
v_mfma_f32_16x16x32_bf16 v[132:135], v[100:103], a[244:247], v[132:135]
ds_read_b64_tr_b16 v[122:123], v56 offset:59136
v_mfma_f32_16x16x32_bf16 v[136:139], v[100:103], a[248:251], v[136:139]
ds_read_b64_tr_b16 v[124:125], v57 offset:58880
v_mfma_f32_16x16x32_bf16 v[140:143], v[100:103], a[252:255], v[140:143]
ds_read_b64_tr_b16 v[126:127], v57 offset:59136
v_mfma_f32_16x16x32_bf16 v[208:211], v[104:107], a[240:243], v[208:211]
s_add_u32 m0, s50, 49152
s_mov_b32 s93, 1572864
v_mfma_f32_16x16x32_bf16 v[212:215], v[104:107], a[244:247], v[212:215]
buffer_load_dwordx4 v4, s[20:23], s93 offen lds
v_mfma_f32_16x16x32_bf16 v[216:219], v[104:107], a[248:251], v[216:219]
v_mfma_f32_16x16x32_bf16 v[220:223], v[104:107], a[252:255], v[220:223]
buffer_load_dwordx4 v5, s[20:23], s93 offen offset:2048 lds
v_mfma_f32_16x16x32_bf16 v[144:147], v[108:111], a[240:243], v[144:147]
v_mfma_f32_16x16x32_bf16 v[148:151], v[108:111], a[244:247], v[148:151]
v_mfma_f32_16x16x32_bf16 v[152:155], v[108:111], a[248:251], v[152:155]
v_mfma_f32_16x16x32_bf16 v[156:159], v[108:111], a[252:255], v[156:159]
s_waitcnt lgkmcnt(0)
v_mfma_f32_16x16x32_bf16 v[224:227], v[112:115], a[240:243], v[224:227]
ds_read_b64_tr_b16 v[96:97], v56 offset:0
v_mfma_f32_16x16x32_bf16 v[228:231], v[112:115], a[244:247], v[228:231]
ds_read_b64_tr_b16 v[98:99], v56 offset:256
v_mfma_f32_16x16x32_bf16 v[232:235], v[112:115], a[248:251], v[232:235]
ds_read_b64_tr_b16 v[100:101], v57 offset:0
v_cvt_pk_bf16_f32 v192, v192, v193
v_cvt_pk_bf16_f32 v193, v194, v195
v_cvt_pk_bf16_f32 v194, v128, v129
v_mfma_f32_16x16x32_bf16 v[236:239], v[112:115], a[252:255], v[236:239]
ds_read_b64_tr_b16 v[102:103], v57 offset:256
v_cvt_pk_bf16_f32 v195, v130, v131
v_cvt_pk_bf16_f32 v196, v196, v197
v_cvt_pk_bf16_f32 v197, v198, v199
v_mfma_f32_16x16x32_bf16 v[160:163], v[116:119], a[240:243], v[160:163]
ds_read_b64_tr_b16 v[104:105], v56 offset:512
v_cvt_pk_bf16_f32 v198, v132, v133
v_cvt_pk_bf16_f32 v199, v134, v135
v_cvt_pk_bf16_f32 v200, v200, v201
v_mfma_f32_16x16x32_bf16 v[164:167], v[116:119], a[244:247], v[164:167]
ds_read_b64_tr_b16 v[106:107], v56 offset:768
v_cvt_pk_bf16_f32 v201, v202, v203
v_cvt_pk_bf16_f32 v202, v136, v137
v_cvt_pk_bf16_f32 v203, v138, v139
v_mfma_f32_16x16x32_bf16 v[168:171], v[116:119], a[248:251], v[168:171]
ds_read_b64_tr_b16 v[108:109], v57 offset:512
v_cvt_pk_bf16_f32 v204, v204, v205
v_cvt_pk_bf16_f32 v205, v206, v207
v_cvt_pk_bf16_f32 v206, v140, v141
v_mfma_f32_16x16x32_bf16 v[172:175], v[116:119], a[252:255], v[172:175]
ds_read_b64_tr_b16 v[110:111], v57 offset:768
v_cvt_pk_bf16_f32 v207, v142, v143
v_cvt_pk_bf16_f32 v208, v208, v209
v_cvt_pk_bf16_f32 v209, v210, v211
v_mfma_f32_16x16x32_bf16 v[240:243], v[120:123], a[240:243], v[240:243]
v_cvt_pk_bf16_f32 v210, v144, v145
v_cvt_pk_bf16_f32 v211, v146, v147
v_cvt_pk_bf16_f32 v212, v212, v213
v_mfma_f32_16x16x32_bf16 v[244:247], v[120:123], a[244:247], v[244:247]
v_cvt_pk_bf16_f32 v213, v214, v215
v_cvt_pk_bf16_f32 v214, v148, v149
v_cvt_pk_bf16_f32 v215, v150, v151
v_mfma_f32_16x16x32_bf16 v[248:251], v[120:123], a[248:251], v[248:251]
v_cvt_pk_bf16_f32 v216, v216, v217
v_cvt_pk_bf16_f32 v217, v218, v219
v_cvt_pk_bf16_f32 v218, v152, v153
v_mfma_f32_16x16x32_bf16 v[252:255], v[120:123], a[252:255], v[252:255]
v_cvt_pk_bf16_f32 v219, v154, v155
v_cvt_pk_bf16_f32 v220, v220, v221
v_cvt_pk_bf16_f32 v221, v222, v223
v_mfma_f32_16x16x32_bf16 v[176:179], v[124:127], a[240:243], v[176:179]
v_cvt_pk_bf16_f32 v222, v156, v157
v_cvt_pk_bf16_f32 v223, v158, v159
v_mfma_f32_16x16x32_bf16 v[180:183], v[124:127], a[244:247], v[180:183]
v_mfma_f32_16x16x32_bf16 v[184:187], v[124:127], a[248:251], v[184:187]
v_mfma_f32_16x16x32_bf16 v[188:191], v[124:127], a[252:255], v[188:191]
s_nop 3
v_cvt_pk_bf16_f32 v224, v224, v225
v_cvt_pk_bf16_f32 v225, v226, v227
v_cvt_pk_bf16_f32 v226, v160, v161
v_cvt_pk_bf16_f32 v227, v162, v163
v_cvt_pk_bf16_f32 v228, v228, v229
v_cvt_pk_bf16_f32 v229, v230, v231
v_cvt_pk_bf16_f32 v230, v164, v165
v_cvt_pk_bf16_f32 v231, v166, v167
v_cvt_pk_bf16_f32 v232, v232, v233
v_cvt_pk_bf16_f32 v233, v234, v235
v_cvt_pk_bf16_f32 v234, v168, v169
v_cvt_pk_bf16_f32 v235, v170, v171
v_cvt_pk_bf16_f32 v236, v236, v237
v_cvt_pk_bf16_f32 v237, v238, v239
v_cvt_pk_bf16_f32 v238, v172, v173
v_cvt_pk_bf16_f32 v239, v174, v175
v_cvt_pk_bf16_f32 v240, v240, v241
v_cvt_pk_bf16_f32 v241, v242, v243
v_cvt_pk_bf16_f32 v242, v176, v177
v_cvt_pk_bf16_f32 v243, v178, v179
v_cvt_pk_bf16_f32 v244, v244, v245
v_cvt_pk_bf16_f32 v245, v246, v247
v_cvt_pk_bf16_f32 v246, v180, v181
v_cvt_pk_bf16_f32 v247, v182, v183
v_cvt_pk_bf16_f32 v248, v248, v249
v_cvt_pk_bf16_f32 v249, v250, v251
v_cvt_pk_bf16_f32 v250, v184, v185
v_cvt_pk_bf16_f32 v251, v186, v187
v_cvt_pk_bf16_f32 v252, v252, v253
v_cvt_pk_bf16_f32 v253, v254, v255
v_cvt_pk_bf16_f32 v254, v188, v189
v_cvt_pk_bf16_f32 v255, v190, v191
v_readfirstlane_b32 s100, v74
v_readfirstlane_b32 s101, v75
s_add_u32 s14, s82, 16777216
s_waitcnt vmcnt(5) lgkmcnt(0)
s_barrier
s_add_u32 s100, s100, 8
s_add_u32 s101, s101, 8
v_mfma_f32_16x16x32_bf16 v[128:131], a[0:3], v[96:99], 0
ds_read_b64_tr_b16 v[112:113], v56 offset:8192
v_lshlrev_b32 v80, 16, v64
v_mfma_f32_16x16x32_bf16 v[132:135], a[4:7], v[96:99], 0
ds_read_b64_tr_b16 v[114:115], v56 offset:8448
v_and_b32 v81, 0xffff0000, v64
v_mfma_f32_16x16x32_bf16 v[136:139], a[8:11], v[96:99], 0
ds_read_b64_tr_b16 v[116:117], v57 offset:8192
v_lshlrev_b32 v82, 16, v65
v_mfma_f32_16x16x32_bf16 v[140:143], a[12:15], v[96:99], 0
ds_read_b64_tr_b16 v[118:119], v57 offset:8448
v_and_b32 v83, 0xffff0000, v65
v_mfma_f32_16x16x32_bf16 v[144:147], a[0:3], v[100:103], 0
ds_read_b64_tr_b16 v[120:121], v56 offset:8704
v_lshlrev_b32 v84, 16, v66
v_mfma_f32_16x16x32_bf16 v[148:151], a[4:7], v[100:103], 0
ds_read_b64_tr_b16 v[122:123], v56 offset:8960
v_and_b32 v85, 0xffff0000, v66
v_mfma_f32_16x16x32_bf16 v[152:155], a[8:11], v[100:103], 0
ds_read_b64_tr_b16 v[124:125], v57 offset:8704
v_lshlrev_b32 v86, 16, v67
v_mfma_f32_16x16x32_bf16 v[156:159], a[12:15], v[100:103], 0
ds_read_b64_tr_b16 v[126:127], v57 offset:8960
v_and_b32 v87, 0xffff0000, v67
v_mfma_f32_16x16x32_bf16 v[160:163], a[0:3], v[104:107], 0
s_add_u32 m0, s50, 57344
s_mov_b32 s93, 1835008
v_mfma_f32_16x16x32_bf16 v[164:167], a[4:7], v[104:107], 0
buffer_load_dwordx4 v4, s[20:23], s93 offen lds
v_mfma_f32_16x16x32_bf16 v[168:171], a[8:11], v[104:107], 0
s_add_u32 s94, s83, 0
v_lshlrev_b32 v88, 16, v68
v_mfma_f32_16x16x32_bf16 v[172:175], a[12:15], v[104:107], 0
)ASM"
R"ASM(
buffer_load_dwordx4 v5, s[20:23], s93 offen offset:2048 lds
v_mfma_f32_16x16x32_bf16 v[176:179], a[0:3], v[108:111], 0
buffer_store_dwordx4 v[192:195], v2, s[8:11], s94 offen offset:0 sc1
v_mfma_f32_16x16x32_bf16 v[180:183], a[4:7], v[108:111], 0
v_and_b32 v89, 0xffff0000, v68
v_lshlrev_b32 v90, 16, v69
v_mfma_f32_16x16x32_bf16 v[184:187], a[8:11], v[108:111], 0
v_and_b32 v91, 0xffff0000, v69
v_lshlrev_b32 v92, 16, v70
v_mfma_f32_16x16x32_bf16 v[188:191], a[12:15], v[108:111], 0
v_and_b32 v93, 0xffff0000, v70
v_lshlrev_b32 v94, 16, v71
s_waitcnt lgkmcnt(0)
v_mfma_f32_16x16x32_bf16 v[128:131], a[16:19], v[112:115], v[128:131]
ds_read_b64_tr_b16 v[96:97], v56 offset:16384
v_and_b32 v95, 0xffff0000, v71
v_mfma_f32_16x16x32_bf16 v[132:135], a[20:23], v[112:115], v[132:135]
ds_read_b64_tr_b16 v[98:99], v56 offset:16640
v_add_f32 v72, v80, v81
v_mfma_f32_16x16x32_bf16 v[136:139], a[24:27], v[112:115], v[136:139]
ds_read_b64_tr_b16 v[100:101], v57 offset:16384
v_add_f32 v72, v72, v82
v_mfma_f32_16x16x32_bf16 v[140:143], a[28:31], v[112:115], v[140:143]
ds_read_b64_tr_b16 v[102:103], v57 offset:16640
v_add_f32 v72, v72, v83
v_mfma_f32_16x16x32_bf16 v[144:147], a[16:19], v[116:119], v[144:147]
ds_read_b64_tr_b16 v[104:105], v56 offset:16896
v_add_f32 v72, v72, v84
v_mfma_f32_16x16x32_bf16 v[148:151], a[20:23], v[116:119], v[148:151]
ds_read_b64_tr_b16 v[106:107], v56 offset:17152
v_add_f32 v72, v72, v85
v_mfma_f32_16x16x32_bf16 v[152:155], a[24:27], v[116:119], v[152:155]
ds_read_b64_tr_b16 v[108:109], v57 offset:16896
v_add_f32 v72, v72, v86
v_mfma_f32_16x16x32_bf16 v[156:159], a[28:31], v[116:119], v[156:159]
ds_read_b64_tr_b16 v[110:111], v57 offset:17152
v_add_f32 v72, v72, v87
v_mfma_f32_16x16x32_bf16 v[160:163], a[16:19], v[120:123], v[160:163]
v_add_f32 v72, v72, v88
v_add_f32 v72, v72, v89
v_mfma_f32_16x16x32_bf16 v[164:167], a[20:23], v[120:123], v[164:167]
v_add_f32 v72, v72, v90
v_add_f32 v72, v72, v91
v_mfma_f32_16x16x32_bf16 v[168:171], a[24:27], v[120:123], v[168:171]
s_add_u32 s94, s83, 0
v_add_f32 v72, v72, v92
v_mfma_f32_16x16x32_bf16 v[172:175], a[28:31], v[120:123], v[172:175]
v_add_f32 v72, v72, v93
v_add_f32 v72, v72, v94
v_mfma_f32_16x16x32_bf16 v[176:179], a[16:19], v[124:127], v[176:179]
buffer_store_dwordx4 v[196:199], v2, s[8:11], s94 offen offset:1024 sc1
v_mfma_f32_16x16x32_bf16 v[180:183], a[20:23], v[124:127], v[180:183]
v_add_f32 v72, v72, v95
s_nop 0
v_mfma_f32_16x16x32_bf16 v[184:187], a[24:27], v[124:127], v[184:187]
v_add_f32_dpp v72, v72, v72 quad_perm:[1,0,3,2] row_mask:0xf bank_mask:0xf
s_nop 0
v_mfma_f32_16x16x32_bf16 v[188:191], a[28:31], v[124:127], v[188:191]
v_add_f32_dpp v72, v72, v72 quad_perm:[2,3,0,1] row_mask:0xf bank_mask:0xf
s_nop 0
s_waitcnt vmcnt(18) lgkmcnt(0)
s_barrier
v_mfma_f32_16x16x32_bf16 v[128:131], a[32:35], v[96:99], v[128:131]
ds_read_b64_tr_b16 v[112:113], v56 offset:24576
v_add_f32_dpp v72, v72, v72 row_half_mirror row_mask:0xf bank_mask:0xf
v_mfma_f32_16x16x32_bf16 v[132:135], a[36:39], v[96:99], v[132:135]
ds_read_b64_tr_b16 v[114:115], v56 offset:24832
s_nop 0
v_mfma_f32_16x16x32_bf16 v[136:139], a[40:43], v[96:99], v[136:139]
ds_read_b64_tr_b16 v[116:117], v57 offset:24576
v_add_f32_dpp v72, v72, v72 row_mirror row_mask:0xf bank_mask:0xf
v_mfma_f32_16x16x32_bf16 v[140:143], a[44:47], v[96:99], v[140:143]
ds_read_b64_tr_b16 v[118:119], v57 offset:24832
v_mov_b32 v73, v72
v_mfma_f32_16x16x32_bf16 v[144:147], a[32:35], v[100:103], v[144:147]
ds_read_b64_tr_b16 v[120:121], v56 offset:25088
s_nop 0
v_mfma_f32_16x16x32_bf16 v[148:151], a[36:39], v[100:103], v[148:151]
ds_read_b64_tr_b16 v[122:123], v56 offset:25344
v_permlane16_swap_b32 v72, v73
v_mfma_f32_16x16x32_bf16 v[152:155], a[40:43], v[100:103], v[152:155]
ds_read_b64_tr_b16 v[124:125], v57 offset:25088
v_add_f32 v72, v72, v73
v_mfma_f32_16x16x32_bf16 v[156:159], a[44:47], v[100:103], v[156:159]
ds_read_b64_tr_b16 v[126:127], v57 offset:25344
v_mul_f32 v72, 0x3b000000, v72
v_mfma_f32_16x16x32_bf16 v[160:163], a[32:35], v[104:107], v[160:163]
v_sub_f32 v80, v80, v72
v_sub_f32 v81, v81, v72
v_mfma_f32_16x16x32_bf16 v[164:167], a[36:39], v[104:107], v[164:167]
v_sub_f32 v82, v82, v72
v_sub_f32 v83, v83, v72
v_mfma_f32_16x16x32_bf16 v[168:171], a[40:43], v[104:107], v[168:171]
s_add_u32 s94, s83, 8192
v_sub_f32 v84, v84, v72
v_mfma_f32_16x16x32_bf16 v[172:175], a[44:47], v[104:107], v[172:175]
v_sub_f32 v85, v85, v72
v_sub_f32 v86, v86, v72
v_mfma_f32_16x16x32_bf16 v[176:179], a[32:35], v[108:111], v[176:179]
buffer_store_dwordx4 v[200:203], v2, s[8:11], s94 offen offset:0 sc1
v_mfma_f32_16x16x32_bf16 v[180:183], a[36:39], v[108:111], v[180:183]
v_sub_f32 v87, v87, v72
v_sub_f32 v88, v88, v72
v_mfma_f32_16x16x32_bf16 v[184:187], a[40:43], v[108:111], v[184:187]
v_sub_f32 v89, v89, v72
v_sub_f32 v90, v90, v72
v_mfma_f32_16x16x32_bf16 v[188:191], a[44:47], v[108:111], v[188:191]
v_sub_f32 v91, v91, v72
v_sub_f32 v92, v92, v72
s_waitcnt lgkmcnt(0)
v_mfma_f32_16x16x32_bf16 v[128:131], a[48:51], v[112:115], v[128:131]
ds_read_b64_tr_b16 v[96:97], v56 offset:32768
v_sub_f32 v93, v93, v72
v_mfma_f32_16x16x32_bf16 v[132:135], a[52:55], v[112:115], v[132:135]
ds_read_b64_tr_b16 v[98:99], v56 offset:33024
v_sub_f32 v94, v94, v72
v_mfma_f32_16x16x32_bf16 v[136:139], a[56:59], v[112:115], v[136:139]
ds_read_b64_tr_b16 v[100:101], v57 offset:32768
v_sub_f32 v95, v95, v72
v_mfma_f32_16x16x32_bf16 v[140:143], a[60:63], v[112:115], v[140:143]
ds_read_b64_tr_b16 v[102:103], v57 offset:33024
v_mul_f32 v72, v80, v80
v_mfma_f32_16x16x32_bf16 v[144:147], a[48:51], v[116:119], v[144:147]
ds_read_b64_tr_b16 v[104:105], v56 offset:33280
v_fmac_f32 v72, v81, v81
v_mfma_f32_16x16x32_bf16 v[148:151], a[52:55], v[116:119], v[148:151]
ds_read_b64_tr_b16 v[106:107], v56 offset:33536
v_fmac_f32 v72, v82, v82
v_mfma_f32_16x16x32_bf16 v[152:155], a[56:59], v[116:119], v[152:155]
ds_read_b64_tr_b16 v[108:109], v57 offset:33280
v_fmac_f32 v72, v83, v83
v_mfma_f32_16x16x32_bf16 v[156:159], a[60:63], v[116:119], v[156:159]
ds_read_b64_tr_b16 v[110:111], v57 offset:33536
v_fmac_f32 v72, v84, v84
v_mfma_f32_16x16x32_bf16 v[160:163], a[48:51], v[120:123], v[160:163]
v_fmac_f32 v72, v85, v85
v_fmac_f32 v72, v86, v86
v_mfma_f32_16x16x32_bf16 v[164:167], a[52:55], v[120:123], v[164:167]
v_fmac_f32 v72, v87, v87
v_fmac_f32 v72, v88, v88
v_mfma_f32_16x16x32_bf16 v[168:171], a[56:59], v[120:123], v[168:171]
s_add_u32 s94, s83, 8192
v_fmac_f32 v72, v89, v89
v_mfma_f32_16x16x32_bf16 v[172:175], a[60:63], v[120:123], v[172:175]
v_fmac_f32 v72, v90, v90
v_fmac_f32 v72, v91, v91
v_mfma_f32_16x16x32_bf16 v[176:179], a[48:51], v[124:127], v[176:179]
buffer_store_dwordx4 v[204:207], v2, s[8:11], s94 offen offset:1024 sc1
v_mfma_f32_16x16x32_bf16 v[180:183], a[52:55], v[124:127], v[180:183]
v_fmac_f32 v72, v92, v92
v_fmac_f32 v72, v93, v93
v_mfma_f32_16x16x32_bf16 v[184:187], a[56:59], v[124:127], v[184:187]
v_fmac_f32 v72, v94, v94
v_fmac_f32 v72, v95, v95
v_mfma_f32_16x16x32_bf16 v[188:191], a[60:63], v[124:127], v[188:191]
s_nop 0
v_add_f32_dpp v72, v72, v72 quad_perm:[1,0,3,2] row_mask:0xf bank_mask:0xf
s_waitcnt vmcnt(6) lgkmcnt(0)
s_barrier
v_mfma_f32_16x16x32_bf16 v[128:131], a[64:67], v[96:99], v[128:131]
ds_read_b64_tr_b16 v[112:113], v56 offset:40960
s_nop 0
v_mfma_f32_16x16x32_bf16 v[132:135], a[68:71], v[96:99], v[132:135]
ds_read_b64_tr_b16 v[114:115], v56 offset:41216
v_add_f32_dpp v72, v72, v72 quad_perm:[2,3,0,1] row_mask:0xf bank_mask:0xf
v_mfma_f32_16x16x32_bf16 v[136:139], a[72:75], v[96:99], v[136:139]
ds_read_b64_tr_b16 v[116:117], v57 offset:40960
s_nop 0
v_mfma_f32_16x16x32_bf16 v[140:143], a[76:79], v[96:99], v[140:143]
ds_read_b64_tr_b16 v[118:119], v57 offset:41216
v_add_f32_dpp v72, v72, v72 row_half_mirror row_mask:0xf bank_mask:0xf
v_mfma_f32_16x16x32_bf16 v[144:147], a[64:67], v[100:103], v[144:147]
ds_read_b64_tr_b16 v[120:121], v56 offset:41472
s_nop 0
v_mfma_f32_16x16x32_bf16 v[148:151], a[68:71], v[100:103], v[148:151]
ds_read_b64_tr_b16 v[122:123], v56 offset:41728
v_add_f32_dpp v72, v72, v72 row_mirror row_mask:0xf bank_mask:0xf
v_mfma_f32_16x16x32_bf16 v[152:155], a[72:75], v[100:103], v[152:155]
ds_read_b64_tr_b16 v[124:125], v57 offset:41472
v_mov_b32 v73, v72
v_mfma_f32_16x16x32_bf16 v[156:159], a[76:79], v[100:103], v[156:159]
ds_read_b64_tr_b16 v[126:127], v57 offset:41728
s_nop 0
v_mfma_f32_16x16x32_bf16 v[160:163], a[64:67], v[104:107], v[160:163]
v_permlane16_swap_b32 v72, v73
v_add_f32 v72, v72, v73
v_mfma_f32_16x16x32_bf16 v[164:167], a[68:71], v[104:107], v[164:167]
v_mov_b32 v73, 0x3b000000
v_fmaak_f32 v72, v72, v73, 0x3727c5ac
v_mfma_f32_16x16x32_bf16 v[168:171], a[72:75], v[104:107], v[168:171]
s_add_u32 s94, s83, 0
v_mfma_f32_16x16x32_bf16 v[172:175], a[76:79], v[104:107], v[172:175]
v_rsq_f32 v72, v72
v_mfma_f32_16x16x32_bf16 v[176:179], a[64:67], v[108:111], v[176:179]
buffer_store_dwordx4 v[208:211], v2, s[8:11], s94 offen offset:2048 sc1
v_mfma_f32_16x16x32_bf16 v[180:183], a[68:71], v[108:111], v[180:183]
s_nop 0
v_mul_f32 v80, v80, v72
v_mfma_f32_16x16x32_bf16 v[184:187], a[72:75], v[108:111], v[184:187]
v_mul_f32 v81, v81, v72
)ASM"
R"ASM(
v_mul_f32 v82, v82, v72
v_mfma_f32_16x16x32_bf16 v[188:191], a[76:79], v[108:111], v[188:191]
v_mul_f32 v83, v83, v72
v_mul_f32 v84, v84, v72
s_waitcnt lgkmcnt(0)
v_mfma_f32_16x16x32_bf16 v[128:131], a[80:83], v[112:115], v[128:131]
ds_read_b64_tr_b16 v[96:97], v56 offset:49152
v_mul_f32 v85, v85, v72
v_mfma_f32_16x16x32_bf16 v[132:135], a[84:87], v[112:115], v[132:135]
ds_read_b64_tr_b16 v[98:99], v56 offset:49408
v_mul_f32 v86, v86, v72
v_mfma_f32_16x16x32_bf16 v[136:139], a[88:91], v[112:115], v[136:139]
ds_read_b64_tr_b16 v[100:101], v57 offset:49152
v_mul_f32 v87, v87, v72
v_mfma_f32_16x16x32_bf16 v[140:143], a[92:95], v[112:115], v[140:143]
ds_read_b64_tr_b16 v[102:103], v57 offset:49408
v_mul_f32 v88, v88, v72
v_mfma_f32_16x16x32_bf16 v[144:147], a[80:83], v[116:119], v[144:147]
ds_read_b64_tr_b16 v[104:105], v56 offset:49664
v_mul_f32 v89, v89, v72
v_mfma_f32_16x16x32_bf16 v[148:151], a[84:87], v[116:119], v[148:151]
ds_read_b64_tr_b16 v[106:107], v56 offset:49920
v_mul_f32 v90, v90, v72
v_mfma_f32_16x16x32_bf16 v[152:155], a[88:91], v[116:119], v[152:155]
ds_read_b64_tr_b16 v[108:109], v57 offset:49664
v_mul_f32 v91, v91, v72
v_mfma_f32_16x16x32_bf16 v[156:159], a[92:95], v[116:119], v[156:159]
ds_read_b64_tr_b16 v[110:111], v57 offset:49920
v_mul_f32 v92, v92, v72
v_mfma_f32_16x16x32_bf16 v[160:163], a[80:83], v[120:123], v[160:163]
v_mul_f32 v93, v93, v72
v_mul_f32 v94, v94, v72
v_mfma_f32_16x16x32_bf16 v[164:167], a[84:87], v[120:123], v[164:167]
v_mul_f32 v95, v95, v72
v_add_f32 v72, v80, v81
v_mfma_f32_16x16x32_bf16 v[168:171], a[88:91], v[120:123], v[168:171]
s_add_u32 s94, s83, 0
v_add_f32 v72, v72, v82
v_mfma_f32_16x16x32_bf16 v[172:175], a[92:95], v[120:123], v[172:175]
v_add_f32 v72, v72, v83
v_add_f32 v72, v72, v84
v_mfma_f32_16x16x32_bf16 v[176:179], a[80:83], v[124:127], v[176:179]
buffer_store_dwordx4 v[212:215], v2, s[8:11], s94 offen offset:3072 sc1
v_mfma_f32_16x16x32_bf16 v[180:183], a[84:87], v[124:127], v[180:183]
v_add_f32 v72, v72, v85
v_add_f32 v72, v72, v86
v_mfma_f32_16x16x32_bf16 v[184:187], a[88:91], v[124:127], v[184:187]
v_add_f32 v72, v72, v87
v_add_f32 v72, v72, v88
v_mfma_f32_16x16x32_bf16 v[188:191], a[92:95], v[124:127], v[188:191]
v_add_f32 v72, v72, v89
v_add_f32 v72, v72, v90
s_waitcnt vmcnt(6) lgkmcnt(0)
s_barrier
v_mfma_f32_16x16x32_bf16 v[128:131], a[96:99], v[96:99], v[128:131]
ds_read_b64_tr_b16 v[112:113], v56 offset:57344
v_add_f32 v72, v72, v91
v_mfma_f32_16x16x32_bf16 v[132:135], a[100:103], v[96:99], v[132:135]
ds_read_b64_tr_b16 v[114:115], v56 offset:57600
v_add_f32 v72, v72, v92
v_mfma_f32_16x16x32_bf16 v[136:139], a[104:107], v[96:99], v[136:139]
ds_read_b64_tr_b16 v[116:117], v57 offset:57344
v_add_f32 v72, v72, v93
v_mfma_f32_16x16x32_bf16 v[140:143], a[108:111], v[96:99], v[140:143]
ds_read_b64_tr_b16 v[118:119], v57 offset:57600
v_add_f32 v72, v72, v94
v_mfma_f32_16x16x32_bf16 v[144:147], a[96:99], v[100:103], v[144:147]
ds_read_b64_tr_b16 v[120:121], v56 offset:57856
v_add_f32 v72, v72, v95
v_mfma_f32_16x16x32_bf16 v[148:151], a[100:103], v[100:103], v[148:151]
ds_read_b64_tr_b16 v[122:123], v56 offset:58112
s_nop 0
v_mfma_f32_16x16x32_bf16 v[152:155], a[104:107], v[100:103], v[152:155]
ds_read_b64_tr_b16 v[124:125], v57 offset:57856
v_add_f32_dpp v72, v72, v72 quad_perm:[1,0,3,2] row_mask:0xf bank_mask:0xf
v_mfma_f32_16x16x32_bf16 v[156:159], a[108:111], v[100:103], v[156:159]
ds_read_b64_tr_b16 v[126:127], v57 offset:58112
s_nop 0
v_mfma_f32_16x16x32_bf16 v[160:163], a[96:99], v[104:107], v[160:163]
v_add_f32_dpp v72, v72, v72 quad_perm:[2,3,0,1] row_mask:0xf bank_mask:0xf
s_nop 0
v_mfma_f32_16x16x32_bf16 v[164:167], a[100:103], v[104:107], v[164:167]
v_add_f32_dpp v72, v72, v72 row_half_mirror row_mask:0xf bank_mask:0xf
s_nop 0
v_mfma_f32_16x16x32_bf16 v[168:171], a[104:107], v[104:107], v[168:171]
s_add_u32 s94, s83, 8192
v_add_f32_dpp v72, v72, v72 row_mirror row_mask:0xf bank_mask:0xf
v_mfma_f32_16x16x32_bf16 v[172:175], a[108:111], v[104:107], v[172:175]
v_mov_b32 v73, v72
s_nop 0
v_mfma_f32_16x16x32_bf16 v[176:179], a[96:99], v[108:111], v[176:179]
buffer_store_dwordx4 v[216:219], v2, s[8:11], s94 offen offset:2048 sc1
v_mfma_f32_16x16x32_bf16 v[180:183], a[100:103], v[108:111], v[180:183]
v_permlane16_swap_b32 v72, v73
v_add_f32 v72, v72, v73
v_mfma_f32_16x16x32_bf16 v[184:187], a[104:107], v[108:111], v[184:187]
v_mul_f32 v72, 0x3b000000, v72
v_sub_f32 v80, v80, v72
v_mfma_f32_16x16x32_bf16 v[188:191], a[108:111], v[108:111], v[188:191]
v_sub_f32 v81, v81, v72
v_sub_f32 v82, v82, v72
s_waitcnt lgkmcnt(0)
v_mfma_f32_16x16x32_bf16 v[128:131], a[112:115], v[112:115], v[128:131]
ds_read_b64_tr_b16 v[96:97], v58 offset:0
v_sub_f32 v83, v83, v72
v_mfma_f32_16x16x32_bf16 v[132:135], a[116:119], v[112:115], v[132:135]
ds_read_b64_tr_b16 v[98:99], v58 offset:256
v_sub_f32 v84, v84, v72
v_mfma_f32_16x16x32_bf16 v[136:139], a[120:123], v[112:115], v[136:139]
ds_read_b64_tr_b16 v[100:101], v59 offset:0
v_sub_f32 v85, v85, v72
v_mfma_f32_16x16x32_bf16 v[140:143], a[124:127], v[112:115], v[140:143]
ds_read_b64_tr_b16 v[102:103], v59 offset:256
v_sub_f32 v86, v86, v72
v_mfma_f32_16x16x32_bf16 v[144:147], a[112:115], v[116:119], v[144:147]
ds_read_b64_tr_b16 v[104:105], v58 offset:512
v_sub_f32 v87, v87, v72
v_mfma_f32_16x16x32_bf16 v[148:151], a[116:119], v[116:119], v[148:151]
ds_read_b64_tr_b16 v[106:107], v58 offset:768
v_sub_f32 v88, v88, v72
v_mfma_f32_16x16x32_bf16 v[152:155], a[120:123], v[116:119], v[152:155]
ds_read_b64_tr_b16 v[108:109], v59 offset:512
v_sub_f32 v89, v89, v72
v_mfma_f32_16x16x32_bf16 v[156:159], a[124:127], v[116:119], v[156:159]
ds_read_b64_tr_b16 v[110:111], v59 offset:768
v_sub_f32 v90, v90, v72
v_mfma_f32_16x16x32_bf16 v[160:163], a[112:115], v[120:123], v[160:163]
v_sub_f32 v91, v91, v72
v_sub_f32 v92, v92, v72
v_mfma_f32_16x16x32_bf16 v[164:167], a[116:119], v[120:123], v[164:167]
v_sub_f32 v93, v93, v72
v_sub_f32 v94, v94, v72
v_mfma_f32_16x16x32_bf16 v[168:171], a[120:123], v[120:123], v[168:171]
s_add_u32 s94, s83, 8192
v_sub_f32 v95, v95, v72
v_mfma_f32_16x16x32_bf16 v[172:175], a[124:127], v[120:123], v[172:175]
v_mul_f32 v72, v80, v80
v_fmac_f32 v72, v81, v81
v_mfma_f32_16x16x32_bf16 v[176:179], a[112:115], v[124:127], v[176:179]
buffer_store_dwordx4 v[220:223], v2, s[8:11], s94 offen offset:3072 sc1
v_mfma_f32_16x16x32_bf16 v[180:183], a[116:119], v[124:127], v[180:183]
v_fmac_f32 v72, v82, v82
v_fmac_f32 v72, v83, v83
v_mfma_f32_16x16x32_bf16 v[184:187], a[120:123], v[124:127], v[184:187]
v_fmac_f32 v72, v84, v84
v_fmac_f32 v72, v85, v85
v_mfma_f32_16x16x32_bf16 v[188:191], a[124:127], v[124:127], v[188:191]
v_fmac_f32 v72, v86, v86
v_fmac_f32 v72, v87, v87
s_waitcnt vmcnt(30) lgkmcnt(0)
s_barrier
v_mfma_f32_16x16x32_bf16 v[128:131], a[128:131], v[96:99], v[128:131]
ds_read_b64_tr_b16 v[112:113], v58 offset:8192
v_fmac_f32 v72, v88, v88
v_mfma_f32_16x16x32_bf16 v[132:135], a[132:135], v[96:99], v[132:135]
ds_read_b64_tr_b16 v[114:115], v58 offset:8448
v_fmac_f32 v72, v89, v89
v_mfma_f32_16x16x32_bf16 v[136:139], a[136:139], v[96:99], v[136:139]
ds_read_b64_tr_b16 v[116:117], v59 offset:8192
v_fmac_f32 v72, v90, v90
v_mfma_f32_16x16x32_bf16 v[140:143], a[140:143], v[96:99], v[140:143]
ds_read_b64_tr_b16 v[118:119], v59 offset:8448
v_fmac_f32 v72, v91, v91
v_mfma_f32_16x16x32_bf16 v[144:147], a[128:131], v[100:103], v[144:147]
ds_read_b64_tr_b16 v[120:121], v58 offset:8704
v_fmac_f32 v72, v92, v92
v_mfma_f32_16x16x32_bf16 v[148:151], a[132:135], v[100:103], v[148:151]
ds_read_b64_tr_b16 v[122:123], v58 offset:8960
v_fmac_f32 v72, v93, v93
v_mfma_f32_16x16x32_bf16 v[152:155], a[136:139], v[100:103], v[152:155]
ds_read_b64_tr_b16 v[124:125], v59 offset:8704
v_fmac_f32 v72, v94, v94
v_mfma_f32_16x16x32_bf16 v[156:159], a[140:143], v[100:103], v[156:159]
ds_read_b64_tr_b16 v[126:127], v59 offset:8960
v_fmac_f32 v72, v95, v95
v_mfma_f32_16x16x32_bf16 v[160:163], a[128:131], v[104:107], v[160:163]
s_nop 0
v_add_f32_dpp v72, v72, v72 quad_perm:[1,0,3,2] row_mask:0xf bank_mask:0xf
v_mfma_f32_16x16x32_bf16 v[164:167], a[132:135], v[104:107], v[164:167]
s_nop 0
v_add_f32_dpp v72, v72, v72 quad_perm:[2,3,0,1] row_mask:0xf bank_mask:0xf
v_mfma_f32_16x16x32_bf16 v[168:171], a[136:139], v[104:107], v[168:171]
s_add_u32 s94, s83, 4096
s_nop 0
v_mfma_f32_16x16x32_bf16 v[172:175], a[140:143], v[104:107], v[172:175]
v_add_f32_dpp v72, v72, v72 row_half_mirror row_mask:0xf bank_mask:0xf
s_nop 0
v_mfma_f32_16x16x32_bf16 v[176:179], a[128:131], v[108:111], v[176:179]
buffer_store_dwordx4 v[224:227], v2, s[8:11], s94 offen offset:0 sc1
v_mfma_f32_16x16x32_bf16 v[180:183], a[132:135], v[108:111], v[180:183]
v_add_f32_dpp v72, v72, v72 row_mirror row_mask:0xf bank_mask:0xf
v_mov_b32 v73, v72
v_mfma_f32_16x16x32_bf16 v[184:187], a[136:139], v[108:111], v[184:187]
s_nop 0
v_permlane16_swap_b32 v72, v73
v_mfma_f32_16x16x32_bf16 v[188:191], a[140:143], v[108:111], v[188:191]
v_add_f32 v72, v72, v73
v_mov_b32 v73, 0x3b000000
s_waitcnt lgkmcnt(0)
v_mfma_f32_16x16x32_bf16 v[128:131], a[144:147], v[112:115], v[128:131]
)ASM"
R"ASM(
ds_read_b64_tr_b16 v[96:97], v58 offset:16384
v_fmaak_f32 v72, v72, v73, 0x3727c5ac
v_mfma_f32_16x16x32_bf16 v[132:135], a[148:151], v[112:115], v[132:135]
ds_read_b64_tr_b16 v[98:99], v58 offset:16640
v_mfma_f32_16x16x32_bf16 v[136:139], a[152:155], v[112:115], v[136:139]
ds_read_b64_tr_b16 v[100:101], v59 offset:16384
v_mfma_f32_16x16x32_bf16 v[140:143], a[156:159], v[112:115], v[140:143]
ds_read_b64_tr_b16 v[102:103], v59 offset:16640
v_mfma_f32_16x16x32_bf16 v[144:147], a[144:147], v[116:119], v[144:147]
ds_read_b64_tr_b16 v[104:105], v58 offset:16896
v_mfma_f32_16x16x32_bf16 v[148:151], a[148:151], v[116:119], v[148:151]
ds_read_b64_tr_b16 v[106:107], v58 offset:17152
v_mfma_f32_16x16x32_bf16 v[152:155], a[152:155], v[116:119], v[152:155]
ds_read_b64_tr_b16 v[108:109], v59 offset:16896
v_mfma_f32_16x16x32_bf16 v[156:159], a[156:159], v[116:119], v[156:159]
ds_read_b64_tr_b16 v[110:111], v59 offset:17152
v_mfma_f32_16x16x32_bf16 v[160:163], a[144:147], v[120:123], v[160:163]
v_rsq_f32 v72, v72
v_mfma_f32_16x16x32_bf16 v[164:167], a[148:151], v[120:123], v[164:167]
s_nop 0
v_mul_f32 v72, 0x40faf233, v72
v_mfma_f32_16x16x32_bf16 v[168:171], a[152:155], v[120:123], v[168:171]
s_add_u32 s94, s83, 4096
v_mul_f32 v80, 0x3e0293ee, v80
v_mfma_f32_16x16x32_bf16 v[172:175], a[156:159], v[120:123], v[172:175]
v_mul_f32 v81, 0x3e0293ee, v81
v_mul_f32 v82, 0x3e0293ee, v82
v_mfma_f32_16x16x32_bf16 v[176:179], a[144:147], v[124:127], v[176:179]
buffer_store_dwordx4 v[228:231], v2, s[8:11], s94 offen offset:1024 sc1
v_mfma_f32_16x16x32_bf16 v[180:183], a[148:151], v[124:127], v[180:183]
v_mul_f32 v83, 0x3e0293ee, v83
v_mul_f32 v84, 0x3e0293ee, v84
v_mfma_f32_16x16x32_bf16 v[184:187], a[152:155], v[124:127], v[184:187]
v_mul_f32 v85, 0x3e0293ee, v85
v_mul_f32 v86, 0x3e0293ee, v86
v_mfma_f32_16x16x32_bf16 v[188:191], a[156:159], v[124:127], v[188:191]
v_mul_f32 v87, 0x3e0293ee, v87
v_mul_f32 v88, 0x3e0293ee, v88
s_waitcnt vmcnt(24) lgkmcnt(0)
s_barrier
v_mfma_f32_16x16x32_bf16 v[128:131], a[160:163], v[96:99], v[128:131]
ds_read_b64_tr_b16 v[112:113], v58 offset:24576
v_mul_f32 v89, 0x3e0293ee, v89
v_mfma_f32_16x16x32_bf16 v[132:135], a[164:167], v[96:99], v[132:135]
ds_read_b64_tr_b16 v[114:115], v58 offset:24832
v_mul_f32 v90, 0x3e0293ee, v90
v_mfma_f32_16x16x32_bf16 v[136:139], a[168:171], v[96:99], v[136:139]
ds_read_b64_tr_b16 v[116:117], v59 offset:24576
v_mul_f32 v91, 0x3e0293ee, v91
v_mfma_f32_16x16x32_bf16 v[140:143], a[172:175], v[96:99], v[140:143]
ds_read_b64_tr_b16 v[118:119], v59 offset:24832
v_mul_f32 v92, 0x3e0293ee, v92
v_mfma_f32_16x16x32_bf16 v[144:147], a[160:163], v[100:103], v[144:147]
ds_read_b64_tr_b16 v[120:121], v58 offset:25088
v_mul_f32 v93, 0x3e0293ee, v93
v_mfma_f32_16x16x32_bf16 v[148:151], a[164:167], v[100:103], v[148:151]
ds_read_b64_tr_b16 v[122:123], v58 offset:25344
v_mul_f32 v94, 0x3e0293ee, v94
v_mfma_f32_16x16x32_bf16 v[152:155], a[168:171], v[100:103], v[152:155]
ds_read_b64_tr_b16 v[124:125], v59 offset:25088
v_mul_f32 v95, 0x3e0293ee, v95
v_mfma_f32_16x16x32_bf16 v[156:159], a[172:175], v[100:103], v[156:159]
ds_read_b64_tr_b16 v[126:127], v59 offset:25344
v_cvt_pk_bf16_f32 v64, v80, v81
v_mfma_f32_16x16x32_bf16 v[160:163], a[160:163], v[104:107], v[160:163]
v_cvt_pk_bf16_f32 v65, v82, v83
v_cvt_pk_bf16_f32 v66, v84, v85
v_mfma_f32_16x16x32_bf16 v[164:167], a[164:167], v[104:107], v[164:167]
v_cvt_pk_bf16_f32 v67, v86, v87
v_cvt_pk_bf16_f32 v68, v88, v89
v_mfma_f32_16x16x32_bf16 v[168:171], a[168:171], v[104:107], v[168:171]
s_add_u32 s94, s83, 12288
v_cvt_pk_bf16_f32 v69, v90, v91
v_mfma_f32_16x16x32_bf16 v[172:175], a[172:175], v[104:107], v[172:175]
v_cvt_pk_bf16_f32 v70, v92, v93
v_cvt_pk_bf16_f32 v71, v94, v95
v_mfma_f32_16x16x32_bf16 v[176:179], a[160:163], v[108:111], v[176:179]
buffer_store_dwordx4 v[232:235], v2, s[8:11], s94 offen offset:0 sc1
v_mfma_f32_16x16x32_bf16 v[180:183], a[164:167], v[108:111], v[180:183]
s_mov_b32 s98, 8192
v_mfma_f32_16x16x32_bf16 v[184:187], a[168:171], v[108:111], v[184:187]
buffer_store_dwordx4 v[64:67], v51, s[8:11], 0 offen sc1
v_mfma_f32_16x16x32_bf16 v[188:191], a[172:175], v[108:111], v[188:191]
buffer_store_dwordx4 v[68:71], v51, s[8:11], s98 offen sc1
s_waitcnt lgkmcnt(0)
v_mfma_f32_16x16x32_bf16 v[128:131], a[176:179], v[112:115], v[128:131]
ds_read_b64_tr_b16 v[96:97], v58 offset:32768
v_mfma_f32_16x16x32_bf16 v[132:135], a[180:183], v[112:115], v[132:135]
ds_read_b64_tr_b16 v[98:99], v58 offset:33024
v_mfma_f32_16x16x32_bf16 v[136:139], a[184:187], v[112:115], v[136:139]
ds_read_b64_tr_b16 v[100:101], v59 offset:32768
v_mfma_f32_16x16x32_bf16 v[140:143], a[188:191], v[112:115], v[140:143]
ds_read_b64_tr_b16 v[102:103], v59 offset:33024
v_mfma_f32_16x16x32_bf16 v[144:147], a[176:179], v[116:119], v[144:147]
ds_read_b64_tr_b16 v[104:105], v58 offset:33280
v_mfma_f32_16x16x32_bf16 v[148:151], a[180:183], v[116:119], v[148:151]
ds_read_b64_tr_b16 v[106:107], v58 offset:33536
v_mfma_f32_16x16x32_bf16 v[152:155], a[184:187], v[116:119], v[152:155]
ds_read_b64_tr_b16 v[108:109], v59 offset:33280
v_mfma_f32_16x16x32_bf16 v[156:159], a[188:191], v[116:119], v[156:159]
ds_read_b64_tr_b16 v[110:111], v59 offset:33536
v_mfma_f32_16x16x32_bf16 v[160:163], a[176:179], v[120:123], v[160:163]
buffer_store_dword v72, v52, s[8:11], 0 offen sc1
v_mfma_f32_16x16x32_bf16 v[164:167], a[180:183], v[120:123], v[164:167]
v_mfma_f32_16x16x32_bf16 v[168:171], a[184:187], v[120:123], v[168:171]
s_add_u32 s94, s83, 12288
v_mfma_f32_16x16x32_bf16 v[172:175], a[188:191], v[120:123], v[172:175]
v_mfma_f32_16x16x32_bf16 v[176:179], a[176:179], v[124:127], v[176:179]
buffer_store_dwordx4 v[236:239], v2, s[8:11], s94 offen offset:1024 sc1
v_mfma_f32_16x16x32_bf16 v[180:183], a[180:183], v[124:127], v[180:183]
v_mfma_f32_16x16x32_bf16 v[184:187], a[184:187], v[124:127], v[184:187]
v_mfma_f32_16x16x32_bf16 v[188:191], a[188:191], v[124:127], v[188:191]
s_waitcnt vmcnt(25) lgkmcnt(0)
s_barrier
v_mfma_f32_16x16x32_bf16 v[128:131], a[192:195], v[96:99], v[128:131]
ds_read_b64_tr_b16 v[112:113], v58 offset:40960
v_mfma_f32_16x16x32_bf16 v[132:135], a[196:199], v[96:99], v[132:135]
ds_read_b64_tr_b16 v[114:115], v58 offset:41216
v_mfma_f32_16x16x32_bf16 v[136:139], a[200:203], v[96:99], v[136:139]
ds_read_b64_tr_b16 v[116:117], v59 offset:40960
v_mfma_f32_16x16x32_bf16 v[140:143], a[204:207], v[96:99], v[140:143]
ds_read_b64_tr_b16 v[118:119], v59 offset:41216
v_mfma_f32_16x16x32_bf16 v[144:147], a[192:195], v[100:103], v[144:147]
ds_read_b64_tr_b16 v[120:121], v58 offset:41472
v_mfma_f32_16x16x32_bf16 v[148:151], a[196:199], v[100:103], v[148:151]
ds_read_b64_tr_b16 v[122:123], v58 offset:41728
v_mfma_f32_16x16x32_bf16 v[152:155], a[200:203], v[100:103], v[152:155]
ds_read_b64_tr_b16 v[124:125], v59 offset:41472
v_mfma_f32_16x16x32_bf16 v[156:159], a[204:207], v[100:103], v[156:159]
ds_read_b64_tr_b16 v[126:127], v59 offset:41728
v_mfma_f32_16x16x32_bf16 v[160:163], a[192:195], v[104:107], v[160:163]
v_mfma_f32_16x16x32_bf16 v[164:167], a[196:199], v[104:107], v[164:167]
v_mfma_f32_16x16x32_bf16 v[168:171], a[200:203], v[104:107], v[168:171]
s_add_u32 s94, s83, 4096
v_mfma_f32_16x16x32_bf16 v[172:175], a[204:207], v[104:107], v[172:175]
v_mfma_f32_16x16x32_bf16 v[176:179], a[192:195], v[108:111], v[176:179]
buffer_store_dwordx4 v[240:243], v2, s[8:11], s94 offen offset:2048 sc1
v_mfma_f32_16x16x32_bf16 v[180:183], a[196:199], v[108:111], v[180:183]
v_mfma_f32_16x16x32_bf16 v[184:187], a[200:203], v[108:111], v[184:187]
v_mfma_f32_16x16x32_bf16 v[188:191], a[204:207], v[108:111], v[188:191]
s_waitcnt lgkmcnt(0)
v_mfma_f32_16x16x32_bf16 v[128:131], a[208:211], v[112:115], v[128:131]
ds_read_b64_tr_b16 v[96:97], v58 offset:49152
v_mfma_f32_16x16x32_bf16 v[132:135], a[212:215], v[112:115], v[132:135]
ds_read_b64_tr_b16 v[98:99], v58 offset:49408
v_mfma_f32_16x16x32_bf16 v[136:139], a[216:219], v[112:115], v[136:139]
ds_read_b64_tr_b16 v[100:101], v59 offset:49152
v_mfma_f32_16x16x32_bf16 v[140:143], a[220:223], v[112:115], v[140:143]
ds_read_b64_tr_b16 v[102:103], v59 offset:49408
v_mfma_f32_16x16x32_bf16 v[144:147], a[208:211], v[116:119], v[144:147]
ds_read_b64_tr_b16 v[104:105], v58 offset:49664
v_mfma_f32_16x16x32_bf16 v[148:151], a[212:215], v[116:119], v[148:151]
ds_read_b64_tr_b16 v[106:107], v58 offset:49920
v_mfma_f32_16x16x32_bf16 v[152:155], a[216:219], v[116:119], v[152:155]
ds_read_b64_tr_b16 v[108:109], v59 offset:49664
v_mfma_f32_16x16x32_bf16 v[156:159], a[220:223], v[116:119], v[156:159]
ds_read_b64_tr_b16 v[110:111], v59 offset:49920
v_mfma_f32_16x16x32_bf16 v[160:163], a[208:211], v[120:123], v[160:163]
v_mfma_f32_16x16x32_bf16 v[164:167], a[212:215], v[120:123], v[164:167]
v_mfma_f32_16x16x32_bf16 v[168:171], a[216:219], v[120:123], v[168:171]
s_add_u32 s94, s83, 4096
v_mfma_f32_16x16x32_bf16 v[172:175], a[220:223], v[120:123], v[172:175]
v_mfma_f32_16x16x32_bf16 v[176:179], a[208:211], v[124:127], v[176:179]
buffer_store_dwordx4 v[244:247], v2, s[8:11], s94 offen offset:3072 sc1
v_mfma_f32_16x16x32_bf16 v[180:183], a[212:215], v[124:127], v[180:183]
v_mfma_f32_16x16x32_bf16 v[184:187], a[216:219], v[124:127], v[184:187]
v_mfma_f32_16x16x32_bf16 v[188:191], a[220:223], v[124:127], v[188:191]
s_waitcnt vmcnt(3) lgkmcnt(0)
s_barrier
v_mfma_f32_16x16x32_bf16 v[128:131], a[224:227], v[96:99], v[128:131]
ds_read_b64_tr_b16 v[112:113], v58 offset:57344
v_mfma_f32_16x16x32_bf16 v[132:135], a[228:231], v[96:99], v[132:135]
ds_read_b64_tr_b16 v[114:115], v58 offset:57600
v_mfma_f32_16x16x32_bf16 v[136:139], a[232:235], v[96:99], v[136:139]
ds_read_b64_tr_b16 v[116:117], v59 offset:57344
v_mfma_f32_16x16x32_bf16 v[140:143], a[236:239], v[96:99], v[140:143]
ds_read_b64_tr_b16 v[118:119], v59 offset:57600
v_mfma_f32_16x16x32_bf16 v[144:147], a[224:227], v[100:103], v[144:147]
ds_read_b64_tr_b16 v[120:121], v58 offset:57856
v_mfma_f32_16x16x32_bf16 v[148:151], a[228:231], v[100:103], v[148:151]
ds_read_b64_tr_b16 v[122:123], v58 offset:58112
v_mfma_f32_16x16x32_bf16 v[152:155], a[232:235], v[100:103], v[152:155]
ds_read_b64_tr_b16 v[124:125], v59 offset:57856
v_mfma_f32_16x16x32_bf16 v[156:159], a[236:239], v[100:103], v[156:159]
ds_read_b64_tr_b16 v[126:127], v59 offset:58112
v_mfma_f32_16x16x32_bf16 v[160:163], a[224:227], v[104:107], v[160:163]
s_add_u32 s98, s100, 1
v_mov_b32 v72, s98
v_mov_b32 v73, 0
buffer_store_dword v72, v73, s[8:11], s84 offen sc1
v_mfma_f32_16x16x32_bf16 v[164:167], a[228:231], v[104:107], v[164:167]
v_mfma_f32_16x16x32_bf16 v[168:171], a[232:235], v[104:107], v[168:171]
s_add_u32 s94, s83, 12288
v_mfma_f32_16x16x32_bf16 v[172:175], a[236:239], v[104:107], v[172:175]
v_mfma_f32_16x16x32_bf16 v[176:179], a[224:227], v[108:111], v[176:179]
buffer_store_dwordx4 v[248:251], v2, s[8:11], s94 offen offset:2048 sc1
v_mfma_f32_16x16x32_bf16 v[180:183], a[228:231], v[108:111], v[180:183]
v_mfma_f32_16x16x32_bf16 v[184:187], a[232:235], v[108:111], v[184:187]
v_mfma_f32_16x16x32_bf16 v[188:191], a[236:239], v[108:111], v[188:191]
s_waitcnt lgkmcnt(0)
)ASM"
R"ASM(
v_mfma_f32_16x16x32_bf16 v[128:131], a[240:243], v[112:115], v[128:131]
ds_read_b64_tr_b16 v[96:97], v58 offset:1024
v_mfma_f32_16x16x32_bf16 v[132:135], a[244:247], v[112:115], v[132:135]
ds_read_b64_tr_b16 v[98:99], v58 offset:1280
v_mfma_f32_16x16x32_bf16 v[136:139], a[248:251], v[112:115], v[136:139]
ds_read_b64_tr_b16 v[100:101], v59 offset:1024
v_mfma_f32_16x16x32_bf16 v[140:143], a[252:255], v[112:115], v[140:143]
ds_read_b64_tr_b16 v[102:103], v59 offset:1280
v_mfma_f32_16x16x32_bf16 v[144:147], a[240:243], v[116:119], v[144:147]
ds_read_b64_tr_b16 v[104:105], v58 offset:1536
v_mfma_f32_16x16x32_bf16 v[148:151], a[244:247], v[116:119], v[148:151]
ds_read_b64_tr_b16 v[106:107], v58 offset:1792
v_mfma_f32_16x16x32_bf16 v[152:155], a[248:251], v[116:119], v[152:155]
ds_read_b64_tr_b16 v[108:109], v59 offset:1536
v_mfma_f32_16x16x32_bf16 v[156:159], a[252:255], v[116:119], v[156:159]
ds_read_b64_tr_b16 v[110:111], v59 offset:1792
v_mfma_f32_16x16x32_bf16 v[160:163], a[240:243], v[120:123], v[160:163]
v_mfma_f32_16x16x32_bf16 v[164:167], a[244:247], v[120:123], v[164:167]
v_mfma_f32_16x16x32_bf16 v[168:171], a[248:251], v[120:123], v[168:171]
s_add_u32 s94, s83, 12288
v_mfma_f32_16x16x32_bf16 v[172:175], a[252:255], v[120:123], v[172:175]
v_mfma_f32_16x16x32_bf16 v[176:179], a[240:243], v[124:127], v[176:179]
buffer_store_dwordx4 v[252:255], v2, s[8:11], s94 offen offset:3072 sc1
v_mfma_f32_16x16x32_bf16 v[180:183], a[244:247], v[124:127], v[180:183]
v_mfma_f32_16x16x32_bf16 v[184:187], a[248:251], v[124:127], v[184:187]
v_mfma_f32_16x16x32_bf16 v[188:191], a[252:255], v[124:127], v[188:191]
s_waitcnt lgkmcnt(0)
v_mfma_f32_16x16x32_bf16 v[192:195], a[128:131], v[96:99], 0
ds_read_b64_tr_b16 v[112:113], v58 offset:9216
v_mfma_f32_16x16x32_bf16 v[196:199], a[132:135], v[96:99], 0
ds_read_b64_tr_b16 v[114:115], v58 offset:9472
v_mfma_f32_16x16x32_bf16 v[200:203], a[136:139], v[96:99], 0
ds_read_b64_tr_b16 v[116:117], v59 offset:9216
v_mfma_f32_16x16x32_bf16 v[204:207], a[140:143], v[96:99], 0
ds_read_b64_tr_b16 v[118:119], v59 offset:9472
v_mfma_f32_16x16x32_bf16 v[208:211], a[128:131], v[100:103], 0
ds_read_b64_tr_b16 v[120:121], v58 offset:9728
v_mfma_f32_16x16x32_bf16 v[212:215], a[132:135], v[100:103], 0
ds_read_b64_tr_b16 v[122:123], v58 offset:9984
v_mfma_f32_16x16x32_bf16 v[216:219], a[136:139], v[100:103], 0
ds_read_b64_tr_b16 v[124:125], v59 offset:9728
v_mfma_f32_16x16x32_bf16 v[220:223], a[140:143], v[100:103], 0
ds_read_b64_tr_b16 v[126:127], v59 offset:9984
v_mfma_f32_16x16x32_bf16 v[224:227], a[128:131], v[104:107], 0
v_mfma_f32_16x16x32_bf16 v[228:231], a[132:135], v[104:107], 0
v_mfma_f32_16x16x32_bf16 v[232:235], a[136:139], v[104:107], 0
v_mfma_f32_16x16x32_bf16 v[236:239], a[140:143], v[104:107], 0
v_mfma_f32_16x16x32_bf16 v[240:243], a[128:131], v[108:111], 0
v_mfma_f32_16x16x32_bf16 v[244:247], a[132:135], v[108:111], 0
v_mfma_f32_16x16x32_bf16 v[248:251], a[136:139], v[108:111], 0
v_mfma_f32_16x16x32_bf16 v[252:255], a[140:143], v[108:111], 0
s_waitcnt lgkmcnt(0)
v_mfma_f32_16x16x32_bf16 v[192:195], a[144:147], v[112:115], v[192:195]
ds_read_b64_tr_b16 v[96:97], v58 offset:17408
v_mfma_f32_16x16x32_bf16 v[196:199], a[148:151], v[112:115], v[196:199]
ds_read_b64_tr_b16 v[98:99], v58 offset:17664
v_mfma_f32_16x16x32_bf16 v[200:203], a[152:155], v[112:115], v[200:203]
ds_read_b64_tr_b16 v[100:101], v59 offset:17408
v_mfma_f32_16x16x32_bf16 v[204:207], a[156:159], v[112:115], v[204:207]
ds_read_b64_tr_b16 v[102:103], v59 offset:17664
v_mfma_f32_16x16x32_bf16 v[208:211], a[144:147], v[116:119], v[208:211]
ds_read_b64_tr_b16 v[104:105], v58 offset:17920
v_mfma_f32_16x16x32_bf16 v[212:215], a[148:151], v[116:119], v[212:215]
ds_read_b64_tr_b16 v[106:107], v58 offset:18176
v_mfma_f32_16x16x32_bf16 v[216:219], a[152:155], v[116:119], v[216:219]
ds_read_b64_tr_b16 v[108:109], v59 offset:17920
v_mfma_f32_16x16x32_bf16 v[220:223], a[156:159], v[116:119], v[220:223]
ds_read_b64_tr_b16 v[110:111], v59 offset:18176
v_mfma_f32_16x16x32_bf16 v[224:227], a[144:147], v[120:123], v[224:227]
v_cvt_pk_bf16_f32 v128, v128, v129
v_cvt_pk_bf16_f32 v129, v130, v131
v_mfma_f32_16x16x32_bf16 v[228:231], a[148:151], v[120:123], v[228:231]
v_cvt_pk_bf16_f32 v130, v132, v133
v_cvt_pk_bf16_f32 v131, v134, v135
v_mfma_f32_16x16x32_bf16 v[232:235], a[152:155], v[120:123], v[232:235]
s_add_u32 s94, s83, 16777216
v_mfma_f32_16x16x32_bf16 v[236:239], a[156:159], v[120:123], v[236:239]
v_mfma_f32_16x16x32_bf16 v[240:243], a[144:147], v[124:127], v[240:243]
buffer_store_dwordx4 v[128:131], v2, s[8:11], s94 offen offset:0 sc1
v_mfma_f32_16x16x32_bf16 v[244:247], a[148:151], v[124:127], v[244:247]
v_mfma_f32_16x16x32_bf16 v[248:251], a[152:155], v[124:127], v[248:251]
v_mfma_f32_16x16x32_bf16 v[252:255], a[156:159], v[124:127], v[252:255]
s_waitcnt lgkmcnt(0)
v_mfma_f32_16x16x32_bf16 v[192:195], a[160:163], v[96:99], v[192:195]
ds_read_b64_tr_b16 v[112:113], v58 offset:25600
v_mfma_f32_16x16x32_bf16 v[196:199], a[164:167], v[96:99], v[196:199]
ds_read_b64_tr_b16 v[114:115], v58 offset:25856
v_mfma_f32_16x16x32_bf16 v[200:203], a[168:171], v[96:99], v[200:203]
ds_read_b64_tr_b16 v[116:117], v59 offset:25600
v_mfma_f32_16x16x32_bf16 v[204:207], a[172:175], v[96:99], v[204:207]
ds_read_b64_tr_b16 v[118:119], v59 offset:25856
v_mfma_f32_16x16x32_bf16 v[208:211], a[160:163], v[100:103], v[208:211]
ds_read_b64_tr_b16 v[120:121], v58 offset:26112
v_mfma_f32_16x16x32_bf16 v[212:215], a[164:167], v[100:103], v[212:215]
ds_read_b64_tr_b16 v[122:123], v58 offset:26368
v_mfma_f32_16x16x32_bf16 v[216:219], a[168:171], v[100:103], v[216:219]
ds_read_b64_tr_b16 v[124:125], v59 offset:26112
v_mfma_f32_16x16x32_bf16 v[220:223], a[172:175], v[100:103], v[220:223]
ds_read_b64_tr_b16 v[126:127], v59 offset:26368
v_mfma_f32_16x16x32_bf16 v[224:227], a[160:163], v[104:107], v[224:227]
v_cvt_pk_bf16_f32 v136, v136, v137
v_cvt_pk_bf16_f32 v137, v138, v139
v_mfma_f32_16x16x32_bf16 v[228:231], a[164:167], v[104:107], v[228:231]
v_cvt_pk_bf16_f32 v138, v140, v141
v_cvt_pk_bf16_f32 v139, v142, v143
v_mfma_f32_16x16x32_bf16 v[232:235], a[168:171], v[104:107], v[232:235]
s_add_u32 s94, s83, 16785408
v_mfma_f32_16x16x32_bf16 v[236:239], a[172:175], v[104:107], v[236:239]
v_mfma_f32_16x16x32_bf16 v[240:243], a[160:163], v[108:111], v[240:243]
buffer_store_dwordx4 v[136:139], v2, s[8:11], s94 offen offset:0 sc1
v_mfma_f32_16x16x32_bf16 v[244:247], a[164:167], v[108:111], v[244:247]
v_mfma_f32_16x16x32_bf16 v[248:251], a[168:171], v[108:111], v[248:251]
v_mfma_f32_16x16x32_bf16 v[252:255], a[172:175], v[108:111], v[252:255]
s_waitcnt lgkmcnt(0)
v_mfma_f32_16x16x32_bf16 v[192:195], a[176:179], v[112:115], v[192:195]
ds_read_b64_tr_b16 v[96:97], v58 offset:33792
v_mfma_f32_16x16x32_bf16 v[196:199], a[180:183], v[112:115], v[196:199]
ds_read_b64_tr_b16 v[98:99], v58 offset:34048
v_mfma_f32_16x16x32_bf16 v[200:203], a[184:187], v[112:115], v[200:203]
ds_read_b64_tr_b16 v[100:101], v59 offset:33792
v_mfma_f32_16x16x32_bf16 v[204:207], a[188:191], v[112:115], v[204:207]
ds_read_b64_tr_b16 v[102:103], v59 offset:34048
v_mfma_f32_16x16x32_bf16 v[208:211], a[176:179], v[116:119], v[208:211]
ds_read_b64_tr_b16 v[104:105], v58 offset:34304
v_mfma_f32_16x16x32_bf16 v[212:215], a[180:183], v[116:119], v[212:215]
ds_read_b64_tr_b16 v[106:107], v58 offset:34560
v_mfma_f32_16x16x32_bf16 v[216:219], a[184:187], v[116:119], v[216:219]
ds_read_b64_tr_b16 v[108:109], v59 offset:34304
v_mfma_f32_16x16x32_bf16 v[220:223], a[188:191], v[116:119], v[220:223]
ds_read_b64_tr_b16 v[110:111], v59 offset:34560
v_mfma_f32_16x16x32_bf16 v[224:227], a[176:179], v[120:123], v[224:227]
v_cvt_pk_bf16_f32 v144, v144, v145
v_cvt_pk_bf16_f32 v145, v146, v147
v_mfma_f32_16x16x32_bf16 v[228:231], a[180:183], v[120:123], v[228:231]
v_cvt_pk_bf16_f32 v146, v148, v149
v_cvt_pk_bf16_f32 v147, v150, v151
v_mfma_f32_16x16x32_bf16 v[232:235], a[184:187], v[120:123], v[232:235]
s_add_u32 s94, s83, 16777216
v_mfma_f32_16x16x32_bf16 v[236:239], a[188:191], v[120:123], v[236:239]
v_mfma_f32_16x16x32_bf16 v[240:243], a[176:179], v[124:127], v[240:243]
buffer_store_dwordx4 v[144:147], v2, s[8:11], s94 offen offset:1024 sc1
v_mfma_f32_16x16x32_bf16 v[244:247], a[180:183], v[124:127], v[244:247]
v_mfma_f32_16x16x32_bf16 v[248:251], a[184:187], v[124:127], v[248:251]
v_mfma_f32_16x16x32_bf16 v[252:255], a[188:191], v[124:127], v[252:255]
s_waitcnt lgkmcnt(0)
v_mfma_f32_16x16x32_bf16 v[192:195], a[192:195], v[96:99], v[192:195]
ds_read_b64_tr_b16 v[112:113], v58 offset:41984
v_mfma_f32_16x16x32_bf16 v[196:199], a[196:199], v[96:99], v[196:199]
ds_read_b64_tr_b16 v[114:115], v58 offset:42240
v_mfma_f32_16x16x32_bf16 v[200:203], a[200:203], v[96:99], v[200:203]
ds_read_b64_tr_b16 v[116:117], v59 offset:41984
v_mfma_f32_16x16x32_bf16 v[204:207], a[204:207], v[96:99], v[204:207]
ds_read_b64_tr_b16 v[118:119], v59 offset:42240
v_mfma_f32_16x16x32_bf16 v[208:211], a[192:195], v[100:103], v[208:211]
ds_read_b64_tr_b16 v[120:121], v58 offset:42496
v_mfma_f32_16x16x32_bf16 v[212:215], a[196:199], v[100:103], v[212:215]
ds_read_b64_tr_b16 v[122:123], v58 offset:42752
v_mfma_f32_16x16x32_bf16 v[216:219], a[200:203], v[100:103], v[216:219]
ds_read_b64_tr_b16 v[124:125], v59 offset:42496
v_mfma_f32_16x16x32_bf16 v[220:223], a[204:207], v[100:103], v[220:223]
ds_read_b64_tr_b16 v[126:127], v59 offset:42752
v_mfma_f32_16x16x32_bf16 v[224:227], a[192:195], v[104:107], v[224:227]
v_cvt_pk_bf16_f32 v152, v152, v153
v_cvt_pk_bf16_f32 v153, v154, v155
v_mfma_f32_16x16x32_bf16 v[228:231], a[196:199], v[104:107], v[228:231]
v_cvt_pk_bf16_f32 v154, v156, v157
v_cvt_pk_bf16_f32 v155, v158, v159
v_mfma_f32_16x16x32_bf16 v[232:235], a[200:203], v[104:107], v[232:235]
s_add_u32 s94, s83, 16785408
v_mfma_f32_16x16x32_bf16 v[236:239], a[204:207], v[104:107], v[236:239]
v_mfma_f32_16x16x32_bf16 v[240:243], a[192:195], v[108:111], v[240:243]
buffer_store_dwordx4 v[152:155], v2, s[8:11], s94 offen offset:1024 sc1
v_mfma_f32_16x16x32_bf16 v[244:247], a[196:199], v[108:111], v[244:247]
v_mfma_f32_16x16x32_bf16 v[248:251], a[200:203], v[108:111], v[248:251]
v_mfma_f32_16x16x32_bf16 v[252:255], a[204:207], v[108:111], v[252:255]
s_waitcnt lgkmcnt(0)
v_mfma_f32_16x16x32_bf16 v[192:195], a[208:211], v[112:115], v[192:195]
ds_read_b64_tr_b16 v[96:97], v58 offset:50176
v_mfma_f32_16x16x32_bf16 v[196:199], a[212:215], v[112:115], v[196:199]
ds_read_b64_tr_b16 v[98:99], v58 offset:50432
v_mfma_f32_16x16x32_bf16 v[200:203], a[216:219], v[112:115], v[200:203]
ds_read_b64_tr_b16 v[100:101], v59 offset:50176
v_mfma_f32_16x16x32_bf16 v[204:207], a[220:223], v[112:115], v[204:207]
ds_read_b64_tr_b16 v[102:103], v59 offset:50432
v_mfma_f32_16x16x32_bf16 v[208:211], a[208:211], v[116:119], v[208:211]
ds_read_b64_tr_b16 v[104:105], v58 offset:50688
v_mfma_f32_16x16x32_bf16 v[212:215], a[212:215], v[116:119], v[212:215]
ds_read_b64_tr_b16 v[106:107], v58 offset:50944
v_mfma_f32_16x16x32_bf16 v[216:219], a[216:219], v[116:119], v[216:219]
ds_read_b64_tr_b16 v[108:109], v59 offset:50688
v_mfma_f32_16x16x32_bf16 v[220:223], a[220:223], v[116:119], v[220:223]
ds_read_b64_tr_b16 v[110:111], v59 offset:50944
v_mfma_f32_16x16x32_bf16 v[224:227], a[208:211], v[120:123], v[224:227]
v_cvt_pk_bf16_f32 v160, v160, v161
v_cvt_pk_bf16_f32 v161, v162, v163
v_mfma_f32_16x16x32_bf16 v[228:231], a[212:215], v[120:123], v[228:231]
v_cvt_pk_bf16_f32 v162, v164, v165
v_cvt_pk_bf16_f32 v163, v166, v167
v_mfma_f32_16x16x32_bf16 v[232:235], a[216:219], v[120:123], v[232:235]
s_add_u32 s94, s83, 16777216
)ASM"
R"ASM(
v_mfma_f32_16x16x32_bf16 v[236:239], a[220:223], v[120:123], v[236:239]
v_mfma_f32_16x16x32_bf16 v[240:243], a[208:211], v[124:127], v[240:243]
buffer_store_dwordx4 v[160:163], v2, s[8:11], s94 offen offset:2048 sc1
v_mfma_f32_16x16x32_bf16 v[244:247], a[212:215], v[124:127], v[244:247]
v_mfma_f32_16x16x32_bf16 v[248:251], a[216:219], v[124:127], v[248:251]
v_mfma_f32_16x16x32_bf16 v[252:255], a[220:223], v[124:127], v[252:255]
s_waitcnt lgkmcnt(0)
v_mfma_f32_16x16x32_bf16 v[192:195], a[224:227], v[96:99], v[192:195]
ds_read_b64_tr_b16 v[112:113], v58 offset:58368
v_mfma_f32_16x16x32_bf16 v[196:199], a[228:231], v[96:99], v[196:199]
ds_read_b64_tr_b16 v[114:115], v58 offset:58624
v_mfma_f32_16x16x32_bf16 v[200:203], a[232:235], v[96:99], v[200:203]
ds_read_b64_tr_b16 v[116:117], v59 offset:58368
v_mfma_f32_16x16x32_bf16 v[204:207], a[236:239], v[96:99], v[204:207]
ds_read_b64_tr_b16 v[118:119], v59 offset:58624
v_mfma_f32_16x16x32_bf16 v[208:211], a[224:227], v[100:103], v[208:211]
ds_read_b64_tr_b16 v[120:121], v58 offset:58880
v_mfma_f32_16x16x32_bf16 v[212:215], a[228:231], v[100:103], v[212:215]
ds_read_b64_tr_b16 v[122:123], v58 offset:59136
v_mfma_f32_16x16x32_bf16 v[216:219], a[232:235], v[100:103], v[216:219]
ds_read_b64_tr_b16 v[124:125], v59 offset:58880
v_mfma_f32_16x16x32_bf16 v[220:223], a[236:239], v[100:103], v[220:223]
ds_read_b64_tr_b16 v[126:127], v59 offset:59136
v_mfma_f32_16x16x32_bf16 v[224:227], a[224:227], v[104:107], v[224:227]
v_cvt_pk_bf16_f32 v168, v168, v169
v_cvt_pk_bf16_f32 v169, v170, v171
v_mfma_f32_16x16x32_bf16 v[228:231], a[228:231], v[104:107], v[228:231]
v_cvt_pk_bf16_f32 v170, v172, v173
v_cvt_pk_bf16_f32 v171, v174, v175
v_mfma_f32_16x16x32_bf16 v[232:235], a[232:235], v[104:107], v[232:235]
s_add_u32 s94, s83, 16785408
v_mfma_f32_16x16x32_bf16 v[236:239], a[236:239], v[104:107], v[236:239]
v_mfma_f32_16x16x32_bf16 v[240:243], a[224:227], v[108:111], v[240:243]
buffer_store_dwordx4 v[168:171], v2, s[8:11], s94 offen offset:2048 sc1
v_mfma_f32_16x16x32_bf16 v[244:247], a[228:231], v[108:111], v[244:247]
v_mfma_f32_16x16x32_bf16 v[248:251], a[232:235], v[108:111], v[248:251]
v_and_b32 v72, 496, v2
v_lshlrev_b32 v72, 4, v72
buffer_load_dword v73, v72, s[8:11], s88 offen sc1
v_mfma_f32_16x16x32_bf16 v[252:255], a[236:239], v[108:111], v[252:255]
s_waitcnt lgkmcnt(0)
v_mfma_f32_16x16x32_bf16 v[192:195], a[240:243], v[112:115], v[192:195]
ds_read_b64_tr_b16 v[96:97], v56 offset:1024
v_mfma_f32_16x16x32_bf16 v[196:199], a[244:247], v[112:115], v[196:199]
ds_read_b64_tr_b16 v[98:99], v56 offset:1280
v_mfma_f32_16x16x32_bf16 v[200:203], a[248:251], v[112:115], v[200:203]
ds_read_b64_tr_b16 v[100:101], v57 offset:1024
v_mfma_f32_16x16x32_bf16 v[204:207], a[252:255], v[112:115], v[204:207]
ds_read_b64_tr_b16 v[102:103], v57 offset:1280
v_mfma_f32_16x16x32_bf16 v[208:211], a[240:243], v[116:119], v[208:211]
ds_read_b64_tr_b16 v[104:105], v56 offset:1536
v_mfma_f32_16x16x32_bf16 v[212:215], a[244:247], v[116:119], v[212:215]
ds_read_b64_tr_b16 v[106:107], v56 offset:1792
v_mfma_f32_16x16x32_bf16 v[216:219], a[248:251], v[116:119], v[216:219]
ds_read_b64_tr_b16 v[108:109], v57 offset:1536
v_mfma_f32_16x16x32_bf16 v[220:223], a[252:255], v[116:119], v[220:223]
ds_read_b64_tr_b16 v[110:111], v57 offset:1792
v_mfma_f32_16x16x32_bf16 v[224:227], a[240:243], v[120:123], v[224:227]
v_cvt_pk_bf16_f32 v176, v176, v177
v_cvt_pk_bf16_f32 v177, v178, v179
v_mfma_f32_16x16x32_bf16 v[228:231], a[244:247], v[120:123], v[228:231]
v_cvt_pk_bf16_f32 v178, v180, v181
v_cvt_pk_bf16_f32 v179, v182, v183
v_mfma_f32_16x16x32_bf16 v[232:235], a[248:251], v[120:123], v[232:235]
s_add_u32 s94, s83, 16777216
v_mfma_f32_16x16x32_bf16 v[236:239], a[252:255], v[120:123], v[236:239]
v_mfma_f32_16x16x32_bf16 v[240:243], a[240:243], v[124:127], v[240:243]
buffer_store_dwordx4 v[176:179], v2, s[8:11], s94 offen offset:3072 sc1
v_mfma_f32_16x16x32_bf16 v[244:247], a[244:247], v[124:127], v[244:247]
v_mfma_f32_16x16x32_bf16 v[248:251], a[248:251], v[124:127], v[248:251]
v_mfma_f32_16x16x32_bf16 v[252:255], a[252:255], v[124:127], v[252:255]
s_waitcnt vmcnt(1) lgkmcnt(0)
s_add_u32 s98, s100, 1
v_subrev_u32 v73, s98, v73
v_cmp_lt_u32 vcc, 3, v73
s_cmp_lg_u64 vcc, 0
s_cbranch_scc1 .Lsp1_%=
.Lsr1_%=:
v_mfma_f32_16x16x32_bf16 v[192:195], a[0:3], v[96:99], v[192:195]
ds_read_b64_tr_b16 v[112:113], v56 offset:9216
v_mfma_f32_16x16x32_bf16 v[196:199], a[4:7], v[96:99], v[196:199]
ds_read_b64_tr_b16 v[114:115], v56 offset:9472
v_mfma_f32_16x16x32_bf16 v[200:203], a[8:11], v[96:99], v[200:203]
ds_read_b64_tr_b16 v[116:117], v57 offset:9216
v_mfma_f32_16x16x32_bf16 v[204:207], a[12:15], v[96:99], v[204:207]
ds_read_b64_tr_b16 v[118:119], v57 offset:9472
v_mfma_f32_16x16x32_bf16 v[208:211], a[0:3], v[100:103], v[208:211]
ds_read_b64_tr_b16 v[120:121], v56 offset:9728
v_mfma_f32_16x16x32_bf16 v[212:215], a[4:7], v[100:103], v[212:215]
ds_read_b64_tr_b16 v[122:123], v56 offset:9984
v_mfma_f32_16x16x32_bf16 v[216:219], a[8:11], v[100:103], v[216:219]
ds_read_b64_tr_b16 v[124:125], v57 offset:9728
v_mfma_f32_16x16x32_bf16 v[220:223], a[12:15], v[100:103], v[220:223]
ds_read_b64_tr_b16 v[126:127], v57 offset:9984
v_mfma_f32_16x16x32_bf16 v[224:227], a[0:3], v[104:107], v[224:227]
v_cvt_pk_bf16_f32 v184, v184, v185
v_cvt_pk_bf16_f32 v185, v186, v187
v_mfma_f32_16x16x32_bf16 v[228:231], a[4:7], v[104:107], v[228:231]
v_cvt_pk_bf16_f32 v186, v188, v189
v_cvt_pk_bf16_f32 v187, v190, v191
v_mfma_f32_16x16x32_bf16 v[232:235], a[8:11], v[104:107], v[232:235]
s_add_u32 s94, s83, 16785408
v_mfma_f32_16x16x32_bf16 v[236:239], a[12:15], v[104:107], v[236:239]
v_mfma_f32_16x16x32_bf16 v[240:243], a[0:3], v[108:111], v[240:243]
buffer_store_dwordx4 v[184:187], v2, s[8:11], s94 offen offset:3072 sc1
v_mfma_f32_16x16x32_bf16 v[244:247], a[4:7], v[108:111], v[244:247]
v_mfma_f32_16x16x32_bf16 v[248:251], a[8:11], v[108:111], v[248:251]
v_mov_b32 v72, s100
v_mov_b32 v73, 0
buffer_store_dword v72, v73, s[8:11], s86 offen sc1
v_mfma_f32_16x16x32_bf16 v[252:255], a[12:15], v[108:111], v[252:255]
s_waitcnt lgkmcnt(0)
s_barrier
v_mfma_f32_16x16x32_bf16 v[192:195], a[16:19], v[112:115], v[192:195]
ds_read_b64_tr_b16 v[96:97], v56 offset:17408
v_mfma_f32_16x16x32_bf16 v[196:199], a[20:23], v[112:115], v[196:199]
ds_read_b64_tr_b16 v[98:99], v56 offset:17664
v_mfma_f32_16x16x32_bf16 v[200:203], a[24:27], v[112:115], v[200:203]
ds_read_b64_tr_b16 v[100:101], v57 offset:17408
v_mfma_f32_16x16x32_bf16 v[204:207], a[28:31], v[112:115], v[204:207]
ds_read_b64_tr_b16 v[102:103], v57 offset:17664
v_mfma_f32_16x16x32_bf16 v[208:211], a[16:19], v[116:119], v[208:211]
ds_read_b64_tr_b16 v[104:105], v56 offset:17920
v_mfma_f32_16x16x32_bf16 v[212:215], a[20:23], v[116:119], v[212:215]
ds_read_b64_tr_b16 v[106:107], v56 offset:18176
v_mfma_f32_16x16x32_bf16 v[216:219], a[24:27], v[116:119], v[216:219]
ds_read_b64_tr_b16 v[108:109], v57 offset:17920
v_mfma_f32_16x16x32_bf16 v[220:223], a[28:31], v[116:119], v[220:223]
ds_read_b64_tr_b16 v[110:111], v57 offset:18176
v_mfma_f32_16x16x32_bf16 v[224:227], a[16:19], v[120:123], v[224:227]
s_add_u32 m0, s50, 65536
s_mov_b32 s12, 0
v_mfma_f32_16x16x32_bf16 v[228:231], a[20:23], v[120:123], v[228:231]
buffer_load_dwordx4 v4, s[28:31], s12 offen lds
v_mfma_f32_16x16x32_bf16 v[232:235], a[24:27], v[120:123], v[232:235]
buffer_load_dwordx4 v5, s[28:31], s12 offen offset:2048 lds
v_mfma_f32_16x16x32_bf16 v[236:239], a[28:31], v[120:123], v[236:239]
s_add_u32 m0, s51, 73728
s_mov_b32 s13, 0
v_mfma_f32_16x16x32_bf16 v[240:243], a[16:19], v[124:127], v[240:243]
buffer_load_dwordx4 v6, s[32:35], s13 offen lds
v_mfma_f32_16x16x32_bf16 v[244:247], a[20:23], v[124:127], v[244:247]
buffer_load_dwordx4 a[128:131], v2, s[8:11], s48 offen offset:0 sc1
v_mfma_f32_16x16x32_bf16 v[248:251], a[24:27], v[124:127], v[248:251]
buffer_load_dwordx4 a[132:135], v18, s[8:11], s48 offen offset:0 sc1
v_mfma_f32_16x16x32_bf16 v[252:255], a[28:31], v[124:127], v[252:255]
buffer_load_dwordx4 a[136:139], v2, s[8:11], s49 offen offset:0 sc1
s_waitcnt lgkmcnt(0)
v_mfma_f32_16x16x32_bf16 v[192:195], a[32:35], v[96:99], v[192:195]
ds_read_b64_tr_b16 v[112:113], v56 offset:25600
v_mfma_f32_16x16x32_bf16 v[196:199], a[36:39], v[96:99], v[196:199]
ds_read_b64_tr_b16 v[114:115], v56 offset:25856
v_mfma_f32_16x16x32_bf16 v[200:203], a[40:43], v[96:99], v[200:203]
ds_read_b64_tr_b16 v[116:117], v57 offset:25600
v_mfma_f32_16x16x32_bf16 v[204:207], a[44:47], v[96:99], v[204:207]
ds_read_b64_tr_b16 v[118:119], v57 offset:25856
v_mfma_f32_16x16x32_bf16 v[208:211], a[32:35], v[100:103], v[208:211]
ds_read_b64_tr_b16 v[120:121], v56 offset:26112
v_mfma_f32_16x16x32_bf16 v[212:215], a[36:39], v[100:103], v[212:215]
ds_read_b64_tr_b16 v[122:123], v56 offset:26368
v_mfma_f32_16x16x32_bf16 v[216:219], a[40:43], v[100:103], v[216:219]
ds_read_b64_tr_b16 v[124:125], v57 offset:26112
v_mfma_f32_16x16x32_bf16 v[220:223], a[44:47], v[100:103], v[220:223]
ds_read_b64_tr_b16 v[126:127], v57 offset:26368
v_mfma_f32_16x16x32_bf16 v[224:227], a[32:35], v[104:107], v[224:227]
v_mfma_f32_16x16x32_bf16 v[228:231], a[36:39], v[104:107], v[228:231]
buffer_load_dwordx4 a[140:143], v18, s[8:11], s49 offen offset:0 sc1
s_add_u32 m0, s50, 77824
s_mov_b32 s12, 262144
v_mfma_f32_16x16x32_bf16 v[232:235], a[40:43], v[104:107], v[232:235]
buffer_load_dwordx4 v4, s[28:31], s12 offen lds
v_mfma_f32_16x16x32_bf16 v[236:239], a[44:47], v[104:107], v[236:239]
v_mfma_f32_16x16x32_bf16 v[240:243], a[32:35], v[108:111], v[240:243]
buffer_load_dwordx4 v5, s[28:31], s12 offen offset:2048 lds
s_add_u32 m0, s51, 86016
s_mov_b32 s13, 88064
v_mfma_f32_16x16x32_bf16 v[244:247], a[36:39], v[108:111], v[244:247]
buffer_load_dwordx4 v6, s[32:35], s13 offen lds
v_mfma_f32_16x16x32_bf16 v[248:251], a[40:43], v[108:111], v[248:251]
buffer_load_dwordx4 a[144:147], v2, s[8:11], s48 offen offset:1024 sc1
v_mfma_f32_16x16x32_bf16 v[252:255], a[44:47], v[108:111], v[252:255]
buffer_load_dwordx4 a[148:151], v18, s[8:11], s48 offen offset:1024 sc1
s_waitcnt lgkmcnt(0)
v_mfma_f32_16x16x32_bf16 v[192:195], a[48:51], v[112:115], v[192:195]
ds_read_b64_tr_b16 v[96:97], v56 offset:33792
v_mfma_f32_16x16x32_bf16 v[196:199], a[52:55], v[112:115], v[196:199]
ds_read_b64_tr_b16 v[98:99], v56 offset:34048
v_mfma_f32_16x16x32_bf16 v[200:203], a[56:59], v[112:115], v[200:203]
ds_read_b64_tr_b16 v[100:101], v57 offset:33792
v_mfma_f32_16x16x32_bf16 v[204:207], a[60:63], v[112:115], v[204:207]
ds_read_b64_tr_b16 v[102:103], v57 offset:34048
v_mfma_f32_16x16x32_bf16 v[208:211], a[48:51], v[116:119], v[208:211]
ds_read_b64_tr_b16 v[104:105], v56 offset:34304
v_mfma_f32_16x16x32_bf16 v[212:215], a[52:55], v[116:119], v[212:215]
ds_read_b64_tr_b16 v[106:107], v56 offset:34560
v_mfma_f32_16x16x32_bf16 v[216:219], a[56:59], v[116:119], v[216:219]
ds_read_b64_tr_b16 v[108:109], v57 offset:34304
v_mfma_f32_16x16x32_bf16 v[220:223], a[60:63], v[116:119], v[220:223]
ds_read_b64_tr_b16 v[110:111], v57 offset:34560
v_mfma_f32_16x16x32_bf16 v[224:227], a[48:51], v[120:123], v[224:227]
)ASM"
R"ASM(
v_mfma_f32_16x16x32_bf16 v[228:231], a[52:55], v[120:123], v[228:231]
buffer_load_dwordx4 a[152:155], v2, s[8:11], s49 offen offset:1024 sc1
v_mfma_f32_16x16x32_bf16 v[232:235], a[56:59], v[120:123], v[232:235]
buffer_load_dwordx4 a[156:159], v18, s[8:11], s49 offen offset:1024 sc1
v_mfma_f32_16x16x32_bf16 v[236:239], a[60:63], v[120:123], v[236:239]
s_add_u32 m0, s50, 90112
s_mov_b32 s12, 524288
v_mfma_f32_16x16x32_bf16 v[240:243], a[48:51], v[124:127], v[240:243]
buffer_load_dwordx4 v4, s[28:31], s12 offen lds
v_mfma_f32_16x16x32_bf16 v[244:247], a[52:55], v[124:127], v[244:247]
buffer_load_dwordx4 v5, s[28:31], s12 offen offset:2048 lds
s_add_u32 m0, s51, 98304
s_mov_b32 s13, 176128
v_mfma_f32_16x16x32_bf16 v[248:251], a[56:59], v[124:127], v[248:251]
buffer_load_dwordx4 v6, s[32:35], s13 offen lds
v_mfma_f32_16x16x32_bf16 v[252:255], a[60:63], v[124:127], v[252:255]
buffer_load_dwordx4 a[160:163], v2, s[8:11], s48 offen offset:2048 sc1
s_waitcnt lgkmcnt(0)
v_mfma_f32_16x16x32_bf16 v[192:195], a[64:67], v[96:99], v[192:195]
ds_read_b64_tr_b16 v[112:113], v56 offset:41984
v_mfma_f32_16x16x32_bf16 v[196:199], a[68:71], v[96:99], v[196:199]
ds_read_b64_tr_b16 v[114:115], v56 offset:42240
v_mfma_f32_16x16x32_bf16 v[200:203], a[72:75], v[96:99], v[200:203]
ds_read_b64_tr_b16 v[116:117], v57 offset:41984
v_mfma_f32_16x16x32_bf16 v[204:207], a[76:79], v[96:99], v[204:207]
ds_read_b64_tr_b16 v[118:119], v57 offset:42240
v_mfma_f32_16x16x32_bf16 v[208:211], a[64:67], v[100:103], v[208:211]
ds_read_b64_tr_b16 v[120:121], v56 offset:42496
v_mfma_f32_16x16x32_bf16 v[212:215], a[68:71], v[100:103], v[212:215]
ds_read_b64_tr_b16 v[122:123], v56 offset:42752
v_mfma_f32_16x16x32_bf16 v[216:219], a[72:75], v[100:103], v[216:219]
ds_read_b64_tr_b16 v[124:125], v57 offset:42496
v_mfma_f32_16x16x32_bf16 v[220:223], a[76:79], v[100:103], v[220:223]
ds_read_b64_tr_b16 v[126:127], v57 offset:42752
v_mfma_f32_16x16x32_bf16 v[224:227], a[64:67], v[104:107], v[224:227]
v_mfma_f32_16x16x32_bf16 v[228:231], a[68:71], v[104:107], v[228:231]
buffer_load_dwordx4 a[164:167], v18, s[8:11], s48 offen offset:2048 sc1
v_mfma_f32_16x16x32_bf16 v[232:235], a[72:75], v[104:107], v[232:235]
buffer_load_dwordx4 a[168:171], v2, s[8:11], s49 offen offset:2048 sc1
v_mfma_f32_16x16x32_bf16 v[236:239], a[76:79], v[104:107], v[236:239]
v_mfma_f32_16x16x32_bf16 v[240:243], a[64:67], v[108:111], v[240:243]
buffer_load_dwordx4 a[172:175], v18, s[8:11], s49 offen offset:2048 sc1
s_add_u32 m0, s50, 102400
s_mov_b32 s12, 786432
v_mfma_f32_16x16x32_bf16 v[244:247], a[68:71], v[108:111], v[244:247]
buffer_load_dwordx4 v4, s[28:31], s12 offen lds
v_mfma_f32_16x16x32_bf16 v[248:251], a[72:75], v[108:111], v[248:251]
buffer_load_dwordx4 v5, s[28:31], s12 offen offset:2048 lds
s_add_u32 m0, s51, 110592
s_mov_b32 s13, 264192
v_mfma_f32_16x16x32_bf16 v[252:255], a[76:79], v[108:111], v[252:255]
buffer_load_dwordx4 v6, s[32:35], s13 offen lds
s_waitcnt lgkmcnt(0)
v_mfma_f32_16x16x32_bf16 v[192:195], a[80:83], v[112:115], v[192:195]
ds_read_b64_tr_b16 v[96:97], v56 offset:50176
v_mfma_f32_16x16x32_bf16 v[196:199], a[84:87], v[112:115], v[196:199]
ds_read_b64_tr_b16 v[98:99], v56 offset:50432
v_mfma_f32_16x16x32_bf16 v[200:203], a[88:91], v[112:115], v[200:203]
ds_read_b64_tr_b16 v[100:101], v57 offset:50176
v_mfma_f32_16x16x32_bf16 v[204:207], a[92:95], v[112:115], v[204:207]
ds_read_b64_tr_b16 v[102:103], v57 offset:50432
v_mfma_f32_16x16x32_bf16 v[208:211], a[80:83], v[116:119], v[208:211]
ds_read_b64_tr_b16 v[104:105], v56 offset:50688
v_mfma_f32_16x16x32_bf16 v[212:215], a[84:87], v[116:119], v[212:215]
ds_read_b64_tr_b16 v[106:107], v56 offset:50944
v_mfma_f32_16x16x32_bf16 v[216:219], a[88:91], v[116:119], v[216:219]
ds_read_b64_tr_b16 v[108:109], v57 offset:50688
v_mfma_f32_16x16x32_bf16 v[220:223], a[92:95], v[116:119], v[220:223]
ds_read_b64_tr_b16 v[110:111], v57 offset:50944
v_mfma_f32_16x16x32_bf16 v[224:227], a[80:83], v[120:123], v[224:227]
v_mfma_f32_16x16x32_bf16 v[228:231], a[84:87], v[120:123], v[228:231]
buffer_load_dwordx4 a[176:179], v2, s[8:11], s48 offen offset:3072 sc1
v_mfma_f32_16x16x32_bf16 v[232:235], a[88:91], v[120:123], v[232:235]
buffer_load_dwordx4 a[180:183], v18, s[8:11], s48 offen offset:3072 sc1
v_mfma_f32_16x16x32_bf16 v[236:239], a[92:95], v[120:123], v[236:239]
v_mfma_f32_16x16x32_bf16 v[240:243], a[80:83], v[124:127], v[240:243]
buffer_load_dwordx4 a[184:187], v2, s[8:11], s49 offen offset:3072 sc1
v_mfma_f32_16x16x32_bf16 v[244:247], a[84:87], v[124:127], v[244:247]
buffer_load_dwordx4 a[188:191], v18, s[8:11], s49 offen offset:3072 sc1
s_add_u32 s48, s48, 4096
s_add_u32 s49, s49, 4096
s_add_u32 m0, s50, 114688
s_mov_b32 s12, 1048576
v_mfma_f32_16x16x32_bf16 v[248:251], a[88:91], v[124:127], v[248:251]
buffer_load_dwordx4 v4, s[28:31], s12 offen lds
v_mfma_f32_16x16x32_bf16 v[252:255], a[92:95], v[124:127], v[252:255]
buffer_load_dwordx4 v5, s[28:31], s12 offen offset:2048 lds
s_waitcnt lgkmcnt(0)
v_mfma_f32_16x16x32_bf16 v[192:195], a[96:99], v[96:99], v[192:195]
ds_read_b64_tr_b16 v[112:113], v56 offset:58368
v_mfma_f32_16x16x32_bf16 v[196:199], a[100:103], v[96:99], v[196:199]
ds_read_b64_tr_b16 v[114:115], v56 offset:58624
v_mfma_f32_16x16x32_bf16 v[200:203], a[104:107], v[96:99], v[200:203]
ds_read_b64_tr_b16 v[116:117], v57 offset:58368
v_mfma_f32_16x16x32_bf16 v[204:207], a[108:111], v[96:99], v[204:207]
ds_read_b64_tr_b16 v[118:119], v57 offset:58624
v_mfma_f32_16x16x32_bf16 v[208:211], a[96:99], v[100:103], v[208:211]
ds_read_b64_tr_b16 v[120:121], v56 offset:58880
v_mfma_f32_16x16x32_bf16 v[212:215], a[100:103], v[100:103], v[212:215]
ds_read_b64_tr_b16 v[122:123], v56 offset:59136
v_mfma_f32_16x16x32_bf16 v[216:219], a[104:107], v[100:103], v[216:219]
ds_read_b64_tr_b16 v[124:125], v57 offset:58880
v_mfma_f32_16x16x32_bf16 v[220:223], a[108:111], v[100:103], v[220:223]
ds_read_b64_tr_b16 v[126:127], v57 offset:59136
v_mfma_f32_16x16x32_bf16 v[224:227], a[96:99], v[104:107], v[224:227]
s_add_u32 m0, s51, 122880
s_mov_b32 s13, 352256
v_mfma_f32_16x16x32_bf16 v[228:231], a[100:103], v[104:107], v[228:231]
buffer_load_dwordx4 v6, s[32:35], s13 offen lds
v_mfma_f32_16x16x32_bf16 v[232:235], a[104:107], v[104:107], v[232:235]
buffer_load_dwordx4 a[192:195], v2, s[8:11], s48 offen offset:0 sc1
v_mfma_f32_16x16x32_bf16 v[236:239], a[108:111], v[104:107], v[236:239]
v_mfma_f32_16x16x32_bf16 v[240:243], a[96:99], v[108:111], v[240:243]
buffer_load_dwordx4 a[196:199], v18, s[8:11], s48 offen offset:0 sc1
v_mfma_f32_16x16x32_bf16 v[244:247], a[100:103], v[108:111], v[244:247]
buffer_load_dwordx4 a[200:203], v2, s[8:11], s49 offen offset:0 sc1
v_mfma_f32_16x16x32_bf16 v[248:251], a[104:107], v[108:111], v[248:251]
buffer_load_dwordx4 a[204:207], v18, s[8:11], s49 offen offset:0 sc1
s_add_u32 m0, s50, 126976
s_mov_b32 s12, 1310720
v_mfma_f32_16x16x32_bf16 v[252:255], a[108:111], v[108:111], v[252:255]
buffer_load_dwordx4 v4, s[28:31], s12 offen lds
s_waitcnt vmcnt(29) lgkmcnt(0)
s_barrier
v_mfma_f32_16x16x32_bf16 v[192:195], a[112:115], v[112:115], v[192:195]
ds_read_b64_tr_b16 v[40:41], v7 offset:0
v_mfma_f32_16x16x32_bf16 v[196:199], a[116:119], v[112:115], v[196:199]
ds_read_b64_tr_b16 v[42:43], v7 offset:256
v_mfma_f32_16x16x32_bf16 v[200:203], a[120:123], v[112:115], v[200:203]
ds_read_b64_tr_b16 v[44:45], v8 offset:0
v_mfma_f32_16x16x32_bf16 v[204:207], a[124:127], v[112:115], v[204:207]
ds_read_b64_tr_b16 v[46:47], v8 offset:256
v_mfma_f32_16x16x32_bf16 v[208:211], a[112:115], v[116:119], v[208:211]
ds_read_b64_tr_b16 v[48:49], v7 offset:512
v_mfma_f32_16x16x32_bf16 v[212:215], a[116:119], v[116:119], v[212:215]
ds_read_b64_tr_b16 v[50:51], v7 offset:768
v_mfma_f32_16x16x32_bf16 v[216:219], a[120:123], v[116:119], v[216:219]
ds_read_b64_tr_b16 v[52:53], v8 offset:512
v_mfma_f32_16x16x32_bf16 v[220:223], a[124:127], v[116:119], v[220:223]
ds_read_b64_tr_b16 v[54:55], v8 offset:768
v_mfma_f32_16x16x32_bf16 v[224:227], a[112:115], v[120:123], v[224:227]
v_mfma_f32_16x16x32_bf16 v[228:231], a[116:119], v[120:123], v[228:231]
buffer_load_dwordx4 v5, s[28:31], s12 offen offset:2048 lds
s_add_u32 m0, s51, 135168
s_mov_b32 s13, 440320
v_mfma_f32_16x16x32_bf16 v[232:235], a[120:123], v[120:123], v[232:235]
buffer_load_dwordx4 v6, s[32:35], s13 offen lds
v_mfma_f32_16x16x32_bf16 v[236:239], a[124:127], v[120:123], v[236:239]
v_mfma_f32_16x16x32_bf16 v[240:243], a[112:115], v[124:127], v[240:243]
buffer_load_dwordx4 a[208:211], v2, s[8:11], s48 offen offset:1024 sc1
v_mfma_f32_16x16x32_bf16 v[244:247], a[116:119], v[124:127], v[244:247]
buffer_load_dwordx4 a[212:215], v18, s[8:11], s48 offen offset:1024 sc1
v_mfma_f32_16x16x32_bf16 v[248:251], a[120:123], v[124:127], v[248:251]
buffer_load_dwordx4 a[216:219], v2, s[8:11], s49 offen offset:1024 sc1
v_mfma_f32_16x16x32_bf16 v[252:255], a[124:127], v[124:127], v[252:255]
buffer_load_dwordx4 a[220:223], v18, s[8:11], s49 offen offset:1024 sc1
s_waitcnt vmcnt(28) lgkmcnt(0)
s_barrier
v_mfma_f32_16x16x32_bf16 v[80:83], v[40:43], a[128:131], 0
ds_read_b64_tr_b16 v[56:57], v7 offset:1024
v_mfma_f32_16x16x32_bf16 v[84:87], v[40:43], a[132:135], 0
ds_read_b64_tr_b16 v[58:59], v7 offset:1280
v_mfma_f32_16x16x32_bf16 v[88:91], v[40:43], a[136:139], 0
ds_read_b64_tr_b16 v[60:61], v8 offset:1024
v_cvt_pk_bf16_f32 v192, v192, v193
v_cvt_pk_bf16_f32 v193, v194, v195
v_cvt_pk_bf16_f32 v194, v196, v197
v_mfma_f32_16x16x32_bf16 v[92:95], v[40:43], a[140:143], 0
ds_read_b64_tr_b16 v[62:63], v8 offset:1280
v_cvt_pk_bf16_f32 v195, v198, v199
s_add_u32 s94, s83, 16781312
v_mfma_f32_16x16x32_bf16 v[96:99], v[44:47], a[128:131], 0
ds_read_b64_tr_b16 v[64:65], v7 offset:1536
buffer_store_dwordx4 v[192:195], v2, s[8:11], s94 offen offset:0 sc1
v_mfma_f32_16x16x32_bf16 v[100:103], v[44:47], a[132:135], 0
ds_read_b64_tr_b16 v[66:67], v7 offset:1792
v_cvt_pk_bf16_f32 v200, v200, v201
v_cvt_pk_bf16_f32 v201, v202, v203
v_cvt_pk_bf16_f32 v202, v204, v205
v_mfma_f32_16x16x32_bf16 v[104:107], v[44:47], a[136:139], 0
ds_read_b64_tr_b16 v[68:69], v8 offset:1536
v_cvt_pk_bf16_f32 v203, v206, v207
s_add_u32 s94, s83, 16789504
v_mfma_f32_16x16x32_bf16 v[108:111], v[44:47], a[140:143], 0
ds_read_b64_tr_b16 v[70:71], v8 offset:1792
buffer_store_dwordx4 v[200:203], v2, s[8:11], s94 offen offset:0 sc1
v_mfma_f32_16x16x32_bf16 v[112:115], v[48:51], a[128:131], 0
v_cvt_pk_bf16_f32 v208, v208, v209
v_cvt_pk_bf16_f32 v209, v210, v211
v_cvt_pk_bf16_f32 v210, v212, v213
v_cvt_pk_bf16_f32 v211, v214, v215
v_mfma_f32_16x16x32_bf16 v[116:119], v[48:51], a[132:135], 0
s_add_u32 s94, s83, 16781312
buffer_store_dwordx4 v[208:211], v2, s[8:11], s94 offen offset:1024 sc1
v_mfma_f32_16x16x32_bf16 v[120:123], v[48:51], a[136:139], 0
v_cvt_pk_bf16_f32 v216, v216, v217
v_cvt_pk_bf16_f32 v217, v218, v219
v_cvt_pk_bf16_f32 v218, v220, v221
v_cvt_pk_bf16_f32 v219, v222, v223
v_mfma_f32_16x16x32_bf16 v[124:127], v[48:51], a[140:143], 0
)ASM"
R"ASM(
s_add_u32 s94, s83, 16789504
buffer_store_dwordx4 v[216:219], v2, s[8:11], s94 offen offset:1024 sc1
v_mfma_f32_16x16x32_bf16 v[128:131], v[52:55], a[128:131], 0
v_cvt_pk_bf16_f32 v224, v224, v225
v_cvt_pk_bf16_f32 v225, v226, v227
v_cvt_pk_bf16_f32 v226, v228, v229
v_cvt_pk_bf16_f32 v227, v230, v231
v_mfma_f32_16x16x32_bf16 v[132:135], v[52:55], a[132:135], 0
s_add_u32 s94, s83, 16781312
buffer_store_dwordx4 v[224:227], v2, s[8:11], s94 offen offset:2048 sc1
v_mfma_f32_16x16x32_bf16 v[136:139], v[52:55], a[136:139], 0
v_cvt_pk_bf16_f32 v232, v232, v233
v_cvt_pk_bf16_f32 v233, v234, v235
v_cvt_pk_bf16_f32 v234, v236, v237
v_cvt_pk_bf16_f32 v235, v238, v239
v_mfma_f32_16x16x32_bf16 v[140:143], v[52:55], a[140:143], 0
s_add_u32 s94, s83, 16789504
buffer_store_dwordx4 v[232:235], v2, s[8:11], s94 offen offset:2048 sc1
s_waitcnt lgkmcnt(0)
v_mfma_f32_16x16x32_bf16 v[144:147], v[56:59], a[128:131], 0
ds_read_b64_tr_b16 v[40:41], v9 offset:0
v_cvt_pk_bf16_f32 v240, v240, v241
v_cvt_pk_bf16_f32 v241, v242, v243
v_cvt_pk_bf16_f32 v242, v244, v245
v_mfma_f32_16x16x32_bf16 v[148:151], v[56:59], a[132:135], 0
ds_read_b64_tr_b16 v[42:43], v9 offset:256
v_cvt_pk_bf16_f32 v243, v246, v247
s_add_u32 s94, s83, 16781312
v_mfma_f32_16x16x32_bf16 v[152:155], v[56:59], a[136:139], 0
ds_read_b64_tr_b16 v[44:45], v10 offset:0
buffer_store_dwordx4 v[240:243], v2, s[8:11], s94 offen offset:3072 sc1
v_mfma_f32_16x16x32_bf16 v[156:159], v[56:59], a[140:143], 0
ds_read_b64_tr_b16 v[46:47], v10 offset:256
v_cvt_pk_bf16_f32 v248, v248, v249
v_cvt_pk_bf16_f32 v249, v250, v251
v_cvt_pk_bf16_f32 v250, v252, v253
v_mfma_f32_16x16x32_bf16 v[160:163], v[60:63], a[128:131], 0
ds_read_b64_tr_b16 v[48:49], v9 offset:512
v_cvt_pk_bf16_f32 v251, v254, v255
s_add_u32 s94, s83, 16789504
v_mfma_f32_16x16x32_bf16 v[164:167], v[60:63], a[132:135], 0
ds_read_b64_tr_b16 v[50:51], v9 offset:768
buffer_store_dwordx4 v[248:251], v2, s[8:11], s94 offen offset:3072 sc1
v_mfma_f32_16x16x32_bf16 v[168:171], v[60:63], a[136:139], 0
ds_read_b64_tr_b16 v[52:53], v10 offset:512
v_mfma_f32_16x16x32_bf16 v[172:175], v[60:63], a[140:143], 0
ds_read_b64_tr_b16 v[54:55], v10 offset:768
v_mfma_f32_16x16x32_bf16 v[176:179], v[64:67], a[128:131], 0
v_mfma_f32_16x16x32_bf16 v[180:183], v[64:67], a[132:135], 0
v_mfma_f32_16x16x32_bf16 v[184:187], v[64:67], a[136:139], 0
v_mfma_f32_16x16x32_bf16 v[188:191], v[64:67], a[140:143], 0
v_mfma_f32_16x16x32_bf16 v[192:195], v[68:71], a[128:131], 0
v_mfma_f32_16x16x32_bf16 v[196:199], v[68:71], a[132:135], 0
v_mfma_f32_16x16x32_bf16 v[200:203], v[68:71], a[136:139], 0
v_mfma_f32_16x16x32_bf16 v[204:207], v[68:71], a[140:143], 0
s_waitcnt lgkmcnt(0)
v_mfma_f32_16x16x32_bf16 v[208:211], v[40:43], a[128:131], 0
ds_read_b64_tr_b16 v[56:57], v7 offset:12288
v_mfma_f32_16x16x32_bf16 v[216:219], v[40:43], a[132:135], 0
ds_read_b64_tr_b16 v[58:59], v7 offset:12544
v_mfma_f32_16x16x32_bf16 v[224:227], v[40:43], a[136:139], 0
ds_read_b64_tr_b16 v[60:61], v8 offset:12288
v_mfma_f32_16x16x32_bf16 v[232:235], v[40:43], a[140:143], 0
ds_read_b64_tr_b16 v[62:63], v8 offset:12544
v_mfma_f32_16x16x32_bf16 v[212:215], v[44:47], a[128:131], 0
ds_read_b64_tr_b16 v[64:65], v7 offset:12800
v_mfma_f32_16x16x32_bf16 v[220:223], v[44:47], a[132:135], 0
ds_read_b64_tr_b16 v[66:67], v7 offset:13056
v_mfma_f32_16x16x32_bf16 v[228:231], v[44:47], a[136:139], 0
ds_read_b64_tr_b16 v[68:69], v8 offset:12800
v_mfma_f32_16x16x32_bf16 v[236:239], v[44:47], a[140:143], 0
ds_read_b64_tr_b16 v[70:71], v8 offset:13056
v_mfma_f32_16x16x32_bf16 v[240:243], v[48:51], a[128:131], 0
s_add_u32 m0, s50, 139264
s_mov_b32 s12, 1572864
v_mfma_f32_16x16x32_bf16 v[248:251], v[48:51], a[132:135], 0
buffer_load_dwordx4 v4, s[28:31], s12 offen lds
v_mfma_f32_16x16x32_bf16 v[244:247], v[52:55], a[128:131], 0
v_mfma_f32_16x16x32_bf16 v[252:255], v[52:55], a[132:135], 0
buffer_load_dwordx4 v5, s[28:31], s12 offen offset:2048 lds
s_waitcnt vmcnt(31) lgkmcnt(0)
s_barrier
v_mfma_f32_16x16x32_bf16 v[80:83], v[56:59], a[144:147], v[80:83]
ds_read_b64_tr_b16 v[40:41], v7 offset:13312
v_mfma_f32_16x16x32_bf16 v[84:87], v[56:59], a[148:151], v[84:87]
ds_read_b64_tr_b16 v[42:43], v7 offset:13568
v_mfma_f32_16x16x32_bf16 v[88:91], v[56:59], a[152:155], v[88:91]
ds_read_b64_tr_b16 v[44:45], v8 offset:13312
v_mfma_f32_16x16x32_bf16 v[92:95], v[56:59], a[156:159], v[92:95]
ds_read_b64_tr_b16 v[46:47], v8 offset:13568
v_mfma_f32_16x16x32_bf16 v[96:99], v[60:63], a[144:147], v[96:99]
ds_read_b64_tr_b16 v[48:49], v7 offset:13824
v_mfma_f32_16x16x32_bf16 v[100:103], v[60:63], a[148:151], v[100:103]
ds_read_b64_tr_b16 v[50:51], v7 offset:14080
v_mfma_f32_16x16x32_bf16 v[104:107], v[60:63], a[152:155], v[104:107]
ds_read_b64_tr_b16 v[52:53], v8 offset:13824
v_mfma_f32_16x16x32_bf16 v[108:111], v[60:63], a[156:159], v[108:111]
ds_read_b64_tr_b16 v[54:55], v8 offset:14080
v_mfma_f32_16x16x32_bf16 v[112:115], v[64:67], a[144:147], v[112:115]
s_add_u32 m0, s51, 147456
s_mov_b32 s13, 528384
v_mfma_f32_16x16x32_bf16 v[116:119], v[64:67], a[148:151], v[116:119]
buffer_load_dwordx4 v6, s[32:35], s13 offen lds
v_mfma_f32_16x16x32_bf16 v[120:123], v[64:67], a[152:155], v[120:123]
v_mfma_f32_16x16x32_bf16 v[124:127], v[64:67], a[156:159], v[124:127]
buffer_load_dwordx4 a[224:227], v2, s[8:11], s48 offen offset:2048 sc1
v_mfma_f32_16x16x32_bf16 v[128:131], v[68:71], a[144:147], v[128:131]
v_mfma_f32_16x16x32_bf16 v[132:135], v[68:71], a[148:151], v[132:135]
buffer_load_dwordx4 a[228:231], v18, s[8:11], s48 offen offset:2048 sc1
v_mfma_f32_16x16x32_bf16 v[136:139], v[68:71], a[152:155], v[136:139]
v_mfma_f32_16x16x32_bf16 v[140:143], v[68:71], a[156:159], v[140:143]
buffer_load_dwordx4 a[232:235], v2, s[8:11], s49 offen offset:2048 sc1
s_waitcnt lgkmcnt(0)
v_mfma_f32_16x16x32_bf16 v[144:147], v[40:43], a[144:147], v[144:147]
ds_read_b64_tr_b16 v[56:57], v9 offset:12288
v_mfma_f32_16x16x32_bf16 v[148:151], v[40:43], a[148:151], v[148:151]
ds_read_b64_tr_b16 v[58:59], v9 offset:12544
v_mfma_f32_16x16x32_bf16 v[152:155], v[40:43], a[152:155], v[152:155]
ds_read_b64_tr_b16 v[60:61], v10 offset:12288
v_mfma_f32_16x16x32_bf16 v[156:159], v[40:43], a[156:159], v[156:159]
ds_read_b64_tr_b16 v[62:63], v10 offset:12544
v_mfma_f32_16x16x32_bf16 v[160:163], v[44:47], a[144:147], v[160:163]
ds_read_b64_tr_b16 v[64:65], v9 offset:12800
v_mfma_f32_16x16x32_bf16 v[164:167], v[44:47], a[148:151], v[164:167]
ds_read_b64_tr_b16 v[66:67], v9 offset:13056
v_mfma_f32_16x16x32_bf16 v[168:171], v[44:47], a[152:155], v[168:171]
ds_read_b64_tr_b16 v[68:69], v10 offset:12800
v_mfma_f32_16x16x32_bf16 v[172:175], v[44:47], a[156:159], v[172:175]
ds_read_b64_tr_b16 v[70:71], v10 offset:13056
v_mfma_f32_16x16x32_bf16 v[176:179], v[48:51], a[144:147], v[176:179]
v_mfma_f32_16x16x32_bf16 v[180:183], v[48:51], a[148:151], v[180:183]
buffer_load_dwordx4 a[236:239], v18, s[8:11], s49 offen offset:2048 sc1
v_mfma_f32_16x16x32_bf16 v[184:187], v[48:51], a[152:155], v[184:187]
s_add_u32 m0, s50, 151552
s_mov_b32 s12, 1835008
v_mfma_f32_16x16x32_bf16 v[188:191], v[48:51], a[156:159], v[188:191]
buffer_load_dwordx4 v4, s[28:31], s12 offen lds
v_mfma_f32_16x16x32_bf16 v[192:195], v[52:55], a[144:147], v[192:195]
v_mfma_f32_16x16x32_bf16 v[196:199], v[52:55], a[148:151], v[196:199]
buffer_load_dwordx4 v5, s[28:31], s12 offen offset:2048 lds
v_mfma_f32_16x16x32_bf16 v[200:203], v[52:55], a[152:155], v[200:203]
s_add_u32 m0, s51, 159744
s_mov_b32 s13, 616448
v_mfma_f32_16x16x32_bf16 v[204:207], v[52:55], a[156:159], v[204:207]
buffer_load_dwordx4 v6, s[32:35], s13 offen lds
s_waitcnt lgkmcnt(0)
v_mfma_f32_16x16x32_bf16 v[208:211], v[56:59], a[144:147], v[208:211]
ds_read_b64_tr_b16 v[40:41], v7 offset:24576
v_mfma_f32_16x16x32_bf16 v[216:219], v[56:59], a[148:151], v[216:219]
ds_read_b64_tr_b16 v[42:43], v7 offset:24832
v_mfma_f32_16x16x32_bf16 v[224:227], v[56:59], a[152:155], v[224:227]
ds_read_b64_tr_b16 v[44:45], v8 offset:24576
v_mfma_f32_16x16x32_bf16 v[232:235], v[56:59], a[156:159], v[232:235]
ds_read_b64_tr_b16 v[46:47], v8 offset:24832
v_mfma_f32_16x16x32_bf16 v[212:215], v[60:63], a[144:147], v[212:215]
ds_read_b64_tr_b16 v[48:49], v7 offset:25088
v_mfma_f32_16x16x32_bf16 v[220:223], v[60:63], a[148:151], v[220:223]
ds_read_b64_tr_b16 v[50:51], v7 offset:25344
v_mfma_f32_16x16x32_bf16 v[228:231], v[60:63], a[152:155], v[228:231]
ds_read_b64_tr_b16 v[52:53], v8 offset:25088
v_mfma_f32_16x16x32_bf16 v[236:239], v[60:63], a[156:159], v[236:239]
ds_read_b64_tr_b16 v[54:55], v8 offset:25344
v_mfma_f32_16x16x32_bf16 v[240:243], v[64:67], a[144:147], v[240:243]
v_mfma_f32_16x16x32_bf16 v[248:251], v[64:67], a[148:151], v[248:251]
buffer_load_dwordx4 a[240:243], v2, s[8:11], s48 offen offset:3072 sc1
v_mfma_f32_16x16x32_bf16 v[244:247], v[68:71], a[144:147], v[244:247]
v_mfma_f32_16x16x32_bf16 v[252:255], v[68:71], a[148:151], v[252:255]
buffer_load_dwordx4 a[244:247], v18, s[8:11], s48 offen offset:3072 sc1
s_waitcnt vmcnt(34) lgkmcnt(0)
s_barrier
v_mfma_f32_16x16x32_bf16 v[80:83], v[40:43], a[160:163], v[80:83]
ds_read_b64_tr_b16 v[56:57], v7 offset:25600
v_mfma_f32_16x16x32_bf16 v[84:87], v[40:43], a[164:167], v[84:87]
ds_read_b64_tr_b16 v[58:59], v7 offset:25856
v_mfma_f32_16x16x32_bf16 v[88:91], v[40:43], a[168:171], v[88:91]
ds_read_b64_tr_b16 v[60:61], v8 offset:25600
v_mfma_f32_16x16x32_bf16 v[92:95], v[40:43], a[172:175], v[92:95]
ds_read_b64_tr_b16 v[62:63], v8 offset:25856
v_mfma_f32_16x16x32_bf16 v[96:99], v[44:47], a[160:163], v[96:99]
ds_read_b64_tr_b16 v[64:65], v7 offset:26112
v_mfma_f32_16x16x32_bf16 v[100:103], v[44:47], a[164:167], v[100:103]
ds_read_b64_tr_b16 v[66:67], v7 offset:26368
v_mfma_f32_16x16x32_bf16 v[104:107], v[44:47], a[168:171], v[104:107]
ds_read_b64_tr_b16 v[68:69], v8 offset:26112
v_mfma_f32_16x16x32_bf16 v[108:111], v[44:47], a[172:175], v[108:111]
ds_read_b64_tr_b16 v[70:71], v8 offset:26368
v_mfma_f32_16x16x32_bf16 v[112:115], v[48:51], a[160:163], v[112:115]
v_mfma_f32_16x16x32_bf16 v[116:119], v[48:51], a[164:167], v[116:119]
buffer_load_dwordx4 a[248:251], v2, s[8:11], s49 offen offset:3072 sc1
v_mfma_f32_16x16x32_bf16 v[120:123], v[48:51], a[168:171], v[120:123]
v_mfma_f32_16x16x32_bf16 v[124:127], v[48:51], a[172:175], v[124:127]
buffer_load_dwordx4 a[252:255], v18, s[8:11], s49 offen offset:3072 sc1
s_add_u32 s48, s48, 4096
s_add_u32 s49, s49, 4096
v_mfma_f32_16x16x32_bf16 v[128:131], v[52:55], a[160:163], v[128:131]
s_add_u32 m0, s50, 65536
s_mov_b32 s12, 2097152
v_mfma_f32_16x16x32_bf16 v[132:135], v[52:55], a[164:167], v[132:135]
buffer_load_dwordx4 v4, s[28:31], s12 offen lds
v_mfma_f32_16x16x32_bf16 v[136:139], v[52:55], a[168:171], v[136:139]
)ASM"
R"ASM(
v_mfma_f32_16x16x32_bf16 v[140:143], v[52:55], a[172:175], v[140:143]
buffer_load_dwordx4 v5, s[28:31], s12 offen offset:2048 lds
s_waitcnt lgkmcnt(0)
v_mfma_f32_16x16x32_bf16 v[144:147], v[56:59], a[160:163], v[144:147]
ds_read_b64_tr_b16 v[40:41], v9 offset:24576
v_mfma_f32_16x16x32_bf16 v[148:151], v[56:59], a[164:167], v[148:151]
ds_read_b64_tr_b16 v[42:43], v9 offset:24832
v_mfma_f32_16x16x32_bf16 v[152:155], v[56:59], a[168:171], v[152:155]
ds_read_b64_tr_b16 v[44:45], v10 offset:24576
v_mfma_f32_16x16x32_bf16 v[156:159], v[56:59], a[172:175], v[156:159]
ds_read_b64_tr_b16 v[46:47], v10 offset:24832
v_mfma_f32_16x16x32_bf16 v[160:163], v[60:63], a[160:163], v[160:163]
ds_read_b64_tr_b16 v[48:49], v9 offset:25088
v_mfma_f32_16x16x32_bf16 v[164:167], v[60:63], a[164:167], v[164:167]
ds_read_b64_tr_b16 v[50:51], v9 offset:25344
v_mfma_f32_16x16x32_bf16 v[168:171], v[60:63], a[168:171], v[168:171]
ds_read_b64_tr_b16 v[52:53], v10 offset:25088
v_mfma_f32_16x16x32_bf16 v[172:175], v[60:63], a[172:175], v[172:175]
ds_read_b64_tr_b16 v[54:55], v10 offset:25344
v_mfma_f32_16x16x32_bf16 v[176:179], v[64:67], a[160:163], v[176:179]
s_add_u32 m0, s51, 73728
s_mov_b32 s13, 704512
v_mfma_f32_16x16x32_bf16 v[180:183], v[64:67], a[164:167], v[180:183]
buffer_load_dwordx4 v6, s[32:35], s13 offen lds
v_mfma_f32_16x16x32_bf16 v[184:187], v[64:67], a[168:171], v[184:187]
v_mfma_f32_16x16x32_bf16 v[188:191], v[64:67], a[172:175], v[188:191]
buffer_load_dwordx4 a[128:131], v2, s[8:11], s48 offen offset:0 sc1
v_mfma_f32_16x16x32_bf16 v[192:195], v[68:71], a[160:163], v[192:195]
v_mfma_f32_16x16x32_bf16 v[196:199], v[68:71], a[164:167], v[196:199]
buffer_load_dwordx4 a[132:135], v18, s[8:11], s48 offen offset:0 sc1
v_mfma_f32_16x16x32_bf16 v[200:203], v[68:71], a[168:171], v[200:203]
v_mfma_f32_16x16x32_bf16 v[204:207], v[68:71], a[172:175], v[204:207]
buffer_load_dwordx4 a[136:139], v2, s[8:11], s49 offen offset:0 sc1
s_waitcnt lgkmcnt(0)
v_mfma_f32_16x16x32_bf16 v[208:211], v[40:43], a[160:163], v[208:211]
ds_read_b64_tr_b16 v[56:57], v7 offset:36864
v_mfma_f32_16x16x32_bf16 v[216:219], v[40:43], a[164:167], v[216:219]
ds_read_b64_tr_b16 v[58:59], v7 offset:37120
v_mfma_f32_16x16x32_bf16 v[224:227], v[40:43], a[168:171], v[224:227]
ds_read_b64_tr_b16 v[60:61], v8 offset:36864
v_mfma_f32_16x16x32_bf16 v[232:235], v[40:43], a[172:175], v[232:235]
ds_read_b64_tr_b16 v[62:63], v8 offset:37120
v_mfma_f32_16x16x32_bf16 v[212:215], v[44:47], a[160:163], v[212:215]
ds_read_b64_tr_b16 v[64:65], v7 offset:37376
v_mfma_f32_16x16x32_bf16 v[220:223], v[44:47], a[164:167], v[220:223]
ds_read_b64_tr_b16 v[66:67], v7 offset:37632
v_mfma_f32_16x16x32_bf16 v[228:231], v[44:47], a[168:171], v[228:231]
ds_read_b64_tr_b16 v[68:69], v8 offset:37376
v_mfma_f32_16x16x32_bf16 v[236:239], v[44:47], a[172:175], v[236:239]
ds_read_b64_tr_b16 v[70:71], v8 offset:37632
v_mfma_f32_16x16x32_bf16 v[240:243], v[48:51], a[160:163], v[240:243]
v_mfma_f32_16x16x32_bf16 v[248:251], v[48:51], a[164:167], v[248:251]
buffer_load_dwordx4 a[140:143], v18, s[8:11], s49 offen offset:0 sc1
v_mfma_f32_16x16x32_bf16 v[244:247], v[52:55], a[160:163], v[244:247]
s_add_u32 m0, s50, 77824
s_mov_b32 s12, 2359296
v_mfma_f32_16x16x32_bf16 v[252:255], v[52:55], a[164:167], v[252:255]
buffer_load_dwordx4 v4, s[28:31], s12 offen lds
s_waitcnt vmcnt(37) lgkmcnt(0)
s_barrier
v_mfma_f32_16x16x32_bf16 v[80:83], v[56:59], a[176:179], v[80:83]
ds_read_b64_tr_b16 v[40:41], v7 offset:37888
v_mfma_f32_16x16x32_bf16 v[84:87], v[56:59], a[180:183], v[84:87]
ds_read_b64_tr_b16 v[42:43], v7 offset:38144
v_mfma_f32_16x16x32_bf16 v[88:91], v[56:59], a[184:187], v[88:91]
ds_read_b64_tr_b16 v[44:45], v8 offset:37888
v_mfma_f32_16x16x32_bf16 v[92:95], v[56:59], a[188:191], v[92:95]
ds_read_b64_tr_b16 v[46:47], v8 offset:38144
v_mfma_f32_16x16x32_bf16 v[96:99], v[60:63], a[176:179], v[96:99]
ds_read_b64_tr_b16 v[48:49], v7 offset:38400
v_mfma_f32_16x16x32_bf16 v[100:103], v[60:63], a[180:183], v[100:103]
ds_read_b64_tr_b16 v[50:51], v7 offset:38656
v_mfma_f32_16x16x32_bf16 v[104:107], v[60:63], a[184:187], v[104:107]
ds_read_b64_tr_b16 v[52:53], v8 offset:38400
v_mfma_f32_16x16x32_bf16 v[108:111], v[60:63], a[188:191], v[108:111]
ds_read_b64_tr_b16 v[54:55], v8 offset:38656
v_mfma_f32_16x16x32_bf16 v[112:115], v[64:67], a[176:179], v[112:115]
v_mfma_f32_16x16x32_bf16 v[116:119], v[64:67], a[180:183], v[116:119]
buffer_load_dwordx4 v5, s[28:31], s12 offen offset:2048 lds
v_mfma_f32_16x16x32_bf16 v[120:123], v[64:67], a[184:187], v[120:123]
s_add_u32 m0, s51, 86016
s_mov_b32 s13, 792576
v_mfma_f32_16x16x32_bf16 v[124:127], v[64:67], a[188:191], v[124:127]
buffer_load_dwordx4 v6, s[32:35], s13 offen lds
v_mfma_f32_16x16x32_bf16 v[128:131], v[68:71], a[176:179], v[128:131]
v_mfma_f32_16x16x32_bf16 v[132:135], v[68:71], a[180:183], v[132:135]
buffer_load_dwordx4 a[144:147], v2, s[8:11], s48 offen offset:1024 sc1
v_mfma_f32_16x16x32_bf16 v[136:139], v[68:71], a[184:187], v[136:139]
v_mfma_f32_16x16x32_bf16 v[140:143], v[68:71], a[188:191], v[140:143]
buffer_load_dwordx4 a[148:151], v18, s[8:11], s48 offen offset:1024 sc1
s_waitcnt lgkmcnt(0)
v_mfma_f32_16x16x32_bf16 v[144:147], v[40:43], a[176:179], v[144:147]
ds_read_b64_tr_b16 v[56:57], v9 offset:36864
v_mfma_f32_16x16x32_bf16 v[148:151], v[40:43], a[180:183], v[148:151]
ds_read_b64_tr_b16 v[58:59], v9 offset:37120
v_mfma_f32_16x16x32_bf16 v[152:155], v[40:43], a[184:187], v[152:155]
ds_read_b64_tr_b16 v[60:61], v10 offset:36864
v_mfma_f32_16x16x32_bf16 v[156:159], v[40:43], a[188:191], v[156:159]
ds_read_b64_tr_b16 v[62:63], v10 offset:37120
v_mfma_f32_16x16x32_bf16 v[160:163], v[44:47], a[176:179], v[160:163]
ds_read_b64_tr_b16 v[64:65], v9 offset:37376
v_mfma_f32_16x16x32_bf16 v[164:167], v[44:47], a[180:183], v[164:167]
ds_read_b64_tr_b16 v[66:67], v9 offset:37632
v_mfma_f32_16x16x32_bf16 v[168:171], v[44:47], a[184:187], v[168:171]
ds_read_b64_tr_b16 v[68:69], v10 offset:37376
v_mfma_f32_16x16x32_bf16 v[172:175], v[44:47], a[188:191], v[172:175]
ds_read_b64_tr_b16 v[70:71], v10 offset:37632
v_mfma_f32_16x16x32_bf16 v[176:179], v[48:51], a[176:179], v[176:179]
v_mfma_f32_16x16x32_bf16 v[180:183], v[48:51], a[180:183], v[180:183]
buffer_load_dwordx4 a[152:155], v2, s[8:11], s49 offen offset:1024 sc1
v_mfma_f32_16x16x32_bf16 v[184:187], v[48:51], a[184:187], v[184:187]
v_mfma_f32_16x16x32_bf16 v[188:191], v[48:51], a[188:191], v[188:191]
buffer_load_dwordx4 a[156:159], v18, s[8:11], s49 offen offset:1024 sc1
v_mfma_f32_16x16x32_bf16 v[192:195], v[52:55], a[176:179], v[192:195]
s_add_u32 m0, s50, 90112
s_mov_b32 s12, 2621440
v_mfma_f32_16x16x32_bf16 v[196:199], v[52:55], a[180:183], v[196:199]
buffer_load_dwordx4 v4, s[28:31], s12 offen lds
v_mfma_f32_16x16x32_bf16 v[200:203], v[52:55], a[184:187], v[200:203]
v_mfma_f32_16x16x32_bf16 v[204:207], v[52:55], a[188:191], v[204:207]
buffer_load_dwordx4 v5, s[28:31], s12 offen offset:2048 lds
s_waitcnt lgkmcnt(0)
v_mfma_f32_16x16x32_bf16 v[208:211], v[56:59], a[176:179], v[208:211]
ds_read_b64_tr_b16 v[40:41], v20 offset:0
v_mfma_f32_16x16x32_bf16 v[216:219], v[56:59], a[180:183], v[216:219]
ds_read_b64_tr_b16 v[42:43], v20 offset:256
v_mfma_f32_16x16x32_bf16 v[224:227], v[56:59], a[184:187], v[224:227]
ds_read_b64_tr_b16 v[44:45], v21 offset:0
v_mfma_f32_16x16x32_bf16 v[232:235], v[56:59], a[188:191], v[232:235]
ds_read_b64_tr_b16 v[46:47], v21 offset:256
v_mfma_f32_16x16x32_bf16 v[212:215], v[60:63], a[176:179], v[212:215]
ds_read_b64_tr_b16 v[48:49], v20 offset:512
v_mfma_f32_16x16x32_bf16 v[220:223], v[60:63], a[180:183], v[220:223]
ds_read_b64_tr_b16 v[50:51], v20 offset:768
v_mfma_f32_16x16x32_bf16 v[228:231], v[60:63], a[184:187], v[228:231]
ds_read_b64_tr_b16 v[52:53], v21 offset:512
v_mfma_f32_16x16x32_bf16 v[236:239], v[60:63], a[188:191], v[236:239]
ds_read_b64_tr_b16 v[54:55], v21 offset:768
v_mfma_f32_16x16x32_bf16 v[240:243], v[64:67], a[176:179], v[240:243]
s_add_u32 m0, s51, 98304
s_mov_b32 s13, 880640
v_mfma_f32_16x16x32_bf16 v[248:251], v[64:67], a[180:183], v[248:251]
buffer_load_dwordx4 v6, s[32:35], s13 offen lds
v_mfma_f32_16x16x32_bf16 v[244:247], v[68:71], a[176:179], v[244:247]
v_mfma_f32_16x16x32_bf16 v[252:255], v[68:71], a[180:183], v[252:255]
buffer_load_dwordx4 a[160:163], v2, s[8:11], s48 offen offset:2048 sc1
s_waitcnt vmcnt(40) lgkmcnt(0)
s_barrier
v_mfma_f32_16x16x32_bf16 v[80:83], v[40:43], a[192:195], v[80:83]
ds_read_b64_tr_b16 v[56:57], v20 offset:1024
v_mfma_f32_16x16x32_bf16 v[84:87], v[40:43], a[196:199], v[84:87]
ds_read_b64_tr_b16 v[58:59], v20 offset:1280
v_mfma_f32_16x16x32_bf16 v[88:91], v[40:43], a[200:203], v[88:91]
ds_read_b64_tr_b16 v[60:61], v21 offset:1024
v_mfma_f32_16x16x32_bf16 v[92:95], v[40:43], a[204:207], v[92:95]
ds_read_b64_tr_b16 v[62:63], v21 offset:1280
v_mfma_f32_16x16x32_bf16 v[96:99], v[44:47], a[192:195], v[96:99]
ds_read_b64_tr_b16 v[64:65], v20 offset:1536
v_mfma_f32_16x16x32_bf16 v[100:103], v[44:47], a[196:199], v[100:103]
ds_read_b64_tr_b16 v[66:67], v20 offset:1792
v_mfma_f32_16x16x32_bf16 v[104:107], v[44:47], a[200:203], v[104:107]
ds_read_b64_tr_b16 v[68:69], v21 offset:1536
v_mfma_f32_16x16x32_bf16 v[108:111], v[44:47], a[204:207], v[108:111]
ds_read_b64_tr_b16 v[70:71], v21 offset:1792
v_mfma_f32_16x16x32_bf16 v[112:115], v[48:51], a[192:195], v[112:115]
v_mfma_f32_16x16x32_bf16 v[116:119], v[48:51], a[196:199], v[116:119]
buffer_load_dwordx4 a[164:167], v18, s[8:11], s48 offen offset:2048 sc1
v_mfma_f32_16x16x32_bf16 v[120:123], v[48:51], a[200:203], v[120:123]
v_mfma_f32_16x16x32_bf16 v[124:127], v[48:51], a[204:207], v[124:127]
buffer_load_dwordx4 a[168:171], v2, s[8:11], s49 offen offset:2048 sc1
v_mfma_f32_16x16x32_bf16 v[128:131], v[52:55], a[192:195], v[128:131]
v_mfma_f32_16x16x32_bf16 v[132:135], v[52:55], a[196:199], v[132:135]
buffer_load_dwordx4 a[172:175], v18, s[8:11], s49 offen offset:2048 sc1
v_mfma_f32_16x16x32_bf16 v[136:139], v[52:55], a[200:203], v[136:139]
s_add_u32 m0, s50, 102400
s_mov_b32 s12, 2883584
v_mfma_f32_16x16x32_bf16 v[140:143], v[52:55], a[204:207], v[140:143]
buffer_load_dwordx4 v4, s[28:31], s12 offen lds
s_waitcnt lgkmcnt(0)
v_mfma_f32_16x16x32_bf16 v[144:147], v[56:59], a[192:195], v[144:147]
ds_read_b64_tr_b16 v[40:41], v22 offset:0
v_mfma_f32_16x16x32_bf16 v[148:151], v[56:59], a[196:199], v[148:151]
ds_read_b64_tr_b16 v[42:43], v22 offset:256
v_mfma_f32_16x16x32_bf16 v[152:155], v[56:59], a[200:203], v[152:155]
ds_read_b64_tr_b16 v[44:45], v23 offset:0
v_mfma_f32_16x16x32_bf16 v[156:159], v[56:59], a[204:207], v[156:159]
ds_read_b64_tr_b16 v[46:47], v23 offset:256
v_mfma_f32_16x16x32_bf16 v[160:163], v[60:63], a[192:195], v[160:163]
ds_read_b64_tr_b16 v[48:49], v22 offset:512
v_mfma_f32_16x16x32_bf16 v[164:167], v[60:63], a[196:199], v[164:167]
ds_read_b64_tr_b16 v[50:51], v22 offset:768
v_mfma_f32_16x16x32_bf16 v[168:171], v[60:63], a[200:203], v[168:171]
ds_read_b64_tr_b16 v[52:53], v23 offset:512
v_mfma_f32_16x16x32_bf16 v[172:175], v[60:63], a[204:207], v[172:175]
ds_read_b64_tr_b16 v[54:55], v23 offset:768
v_mfma_f32_16x16x32_bf16 v[176:179], v[64:67], a[192:195], v[176:179]
v_mfma_f32_16x16x32_bf16 v[180:183], v[64:67], a[196:199], v[180:183]
buffer_load_dwordx4 v5, s[28:31], s12 offen offset:2048 lds
v_mfma_f32_16x16x32_bf16 v[184:187], v[64:67], a[200:203], v[184:187]
s_add_u32 m0, s51, 110592
)ASM"
R"ASM(
s_mov_b32 s13, 968704
v_mfma_f32_16x16x32_bf16 v[188:191], v[64:67], a[204:207], v[188:191]
buffer_load_dwordx4 v6, s[32:35], s13 offen lds
v_mfma_f32_16x16x32_bf16 v[192:195], v[68:71], a[192:195], v[192:195]
v_mfma_f32_16x16x32_bf16 v[196:199], v[68:71], a[196:199], v[196:199]
buffer_load_dwordx4 a[176:179], v2, s[8:11], s48 offen offset:3072 sc1
v_mfma_f32_16x16x32_bf16 v[200:203], v[68:71], a[200:203], v[200:203]
v_mfma_f32_16x16x32_bf16 v[204:207], v[68:71], a[204:207], v[204:207]
buffer_load_dwordx4 a[180:183], v18, s[8:11], s48 offen offset:3072 sc1
s_waitcnt lgkmcnt(0)
v_mfma_f32_16x16x32_bf16 v[208:211], v[40:43], a[192:195], v[208:211]
ds_read_b64_tr_b16 v[56:57], v20 offset:12288
v_mfma_f32_16x16x32_bf16 v[216:219], v[40:43], a[196:199], v[216:219]
ds_read_b64_tr_b16 v[58:59], v20 offset:12544
v_mfma_f32_16x16x32_bf16 v[224:227], v[40:43], a[200:203], v[224:227]
ds_read_b64_tr_b16 v[60:61], v21 offset:12288
v_mfma_f32_16x16x32_bf16 v[232:235], v[40:43], a[204:207], v[232:235]
ds_read_b64_tr_b16 v[62:63], v21 offset:12544
v_mfma_f32_16x16x32_bf16 v[212:215], v[44:47], a[192:195], v[212:215]
ds_read_b64_tr_b16 v[64:65], v20 offset:12800
v_mfma_f32_16x16x32_bf16 v[220:223], v[44:47], a[196:199], v[220:223]
ds_read_b64_tr_b16 v[66:67], v20 offset:13056
v_mfma_f32_16x16x32_bf16 v[228:231], v[44:47], a[200:203], v[228:231]
ds_read_b64_tr_b16 v[68:69], v21 offset:12800
v_mfma_f32_16x16x32_bf16 v[236:239], v[44:47], a[204:207], v[236:239]
ds_read_b64_tr_b16 v[70:71], v21 offset:13056
v_mfma_f32_16x16x32_bf16 v[240:243], v[48:51], a[192:195], v[240:243]
v_mfma_f32_16x16x32_bf16 v[248:251], v[48:51], a[196:199], v[248:251]
buffer_load_dwordx4 a[184:187], v2, s[8:11], s49 offen offset:3072 sc1
v_mfma_f32_16x16x32_bf16 v[244:247], v[52:55], a[192:195], v[244:247]
v_mfma_f32_16x16x32_bf16 v[252:255], v[52:55], a[196:199], v[252:255]
buffer_load_dwordx4 a[188:191], v18, s[8:11], s49 offen offset:3072 sc1
s_add_u32 s48, s48, 4096
s_add_u32 s49, s49, 4096
s_waitcnt vmcnt(35) lgkmcnt(0)
s_barrier
v_mfma_f32_16x16x32_bf16 v[80:83], v[56:59], a[208:211], v[80:83]
ds_read_b64_tr_b16 v[40:41], v20 offset:13312
v_mfma_f32_16x16x32_bf16 v[84:87], v[56:59], a[212:215], v[84:87]
ds_read_b64_tr_b16 v[42:43], v20 offset:13568
v_mfma_f32_16x16x32_bf16 v[88:91], v[56:59], a[216:219], v[88:91]
ds_read_b64_tr_b16 v[44:45], v21 offset:13312
v_mfma_f32_16x16x32_bf16 v[92:95], v[56:59], a[220:223], v[92:95]
ds_read_b64_tr_b16 v[46:47], v21 offset:13568
v_mfma_f32_16x16x32_bf16 v[96:99], v[60:63], a[208:211], v[96:99]
ds_read_b64_tr_b16 v[48:49], v20 offset:13824
v_mfma_f32_16x16x32_bf16 v[100:103], v[60:63], a[212:215], v[100:103]
ds_read_b64_tr_b16 v[50:51], v20 offset:14080
v_mfma_f32_16x16x32_bf16 v[104:107], v[60:63], a[216:219], v[104:107]
ds_read_b64_tr_b16 v[52:53], v21 offset:13824
v_mfma_f32_16x16x32_bf16 v[108:111], v[60:63], a[220:223], v[108:111]
ds_read_b64_tr_b16 v[54:55], v21 offset:14080
v_mfma_f32_16x16x32_bf16 v[112:115], v[64:67], a[208:211], v[112:115]
s_add_u32 m0, s50, 114688
s_mov_b32 s12, 3145728
s_add_u32 s98, s101, 1
v_mov_b32 v38, s98
v_mov_b32 v39, 0
buffer_store_dword v38, v39, s[8:11], s85 offen sc1
v_mfma_f32_16x16x32_bf16 v[116:119], v[64:67], a[212:215], v[116:119]
buffer_load_dwordx4 v4, s[28:31], s12 offen lds
v_mfma_f32_16x16x32_bf16 v[120:123], v[64:67], a[216:219], v[120:123]
v_mfma_f32_16x16x32_bf16 v[124:127], v[64:67], a[220:223], v[124:127]
buffer_load_dwordx4 v5, s[28:31], s12 offen offset:2048 lds
v_mfma_f32_16x16x32_bf16 v[128:131], v[68:71], a[208:211], v[128:131]
s_add_u32 m0, s51, 122880
s_mov_b32 s13, 1056768
v_mfma_f32_16x16x32_bf16 v[132:135], v[68:71], a[212:215], v[132:135]
buffer_load_dwordx4 v6, s[32:35], s13 offen lds
v_mfma_f32_16x16x32_bf16 v[136:139], v[68:71], a[216:219], v[136:139]
v_mfma_f32_16x16x32_bf16 v[140:143], v[68:71], a[220:223], v[140:143]
buffer_load_dwordx4 a[192:195], v2, s[8:11], s48 offen offset:0 sc1
s_waitcnt lgkmcnt(0)
v_mfma_f32_16x16x32_bf16 v[144:147], v[40:43], a[208:211], v[144:147]
ds_read_b64_tr_b16 v[56:57], v22 offset:12288
v_mfma_f32_16x16x32_bf16 v[148:151], v[40:43], a[212:215], v[148:151]
ds_read_b64_tr_b16 v[58:59], v22 offset:12544
v_mfma_f32_16x16x32_bf16 v[152:155], v[40:43], a[216:219], v[152:155]
ds_read_b64_tr_b16 v[60:61], v23 offset:12288
v_mfma_f32_16x16x32_bf16 v[156:159], v[40:43], a[220:223], v[156:159]
ds_read_b64_tr_b16 v[62:63], v23 offset:12544
v_mfma_f32_16x16x32_bf16 v[160:163], v[44:47], a[208:211], v[160:163]
ds_read_b64_tr_b16 v[64:65], v22 offset:12800
v_mfma_f32_16x16x32_bf16 v[164:167], v[44:47], a[212:215], v[164:167]
ds_read_b64_tr_b16 v[66:67], v22 offset:13056
v_mfma_f32_16x16x32_bf16 v[168:171], v[44:47], a[216:219], v[168:171]
ds_read_b64_tr_b16 v[68:69], v23 offset:12800
v_mfma_f32_16x16x32_bf16 v[172:175], v[44:47], a[220:223], v[172:175]
ds_read_b64_tr_b16 v[70:71], v23 offset:13056
v_mfma_f32_16x16x32_bf16 v[176:179], v[48:51], a[208:211], v[176:179]
v_mfma_f32_16x16x32_bf16 v[180:183], v[48:51], a[212:215], v[180:183]
buffer_load_dwordx4 a[196:199], v18, s[8:11], s48 offen offset:0 sc1
v_mfma_f32_16x16x32_bf16 v[184:187], v[48:51], a[216:219], v[184:187]
v_mfma_f32_16x16x32_bf16 v[188:191], v[48:51], a[220:223], v[188:191]
buffer_load_dwordx4 a[200:203], v2, s[8:11], s49 offen offset:0 sc1
v_mfma_f32_16x16x32_bf16 v[192:195], v[52:55], a[208:211], v[192:195]
v_mfma_f32_16x16x32_bf16 v[196:199], v[52:55], a[212:215], v[196:199]
buffer_load_dwordx4 a[204:207], v18, s[8:11], s49 offen offset:0 sc1
v_mfma_f32_16x16x32_bf16 v[200:203], v[52:55], a[216:219], v[200:203]
v_mfma_f32_16x16x32_bf16 v[204:207], v[52:55], a[220:223], v[204:207]
s_waitcnt lgkmcnt(0)
v_mfma_f32_16x16x32_bf16 v[208:211], v[56:59], a[208:211], v[208:211]
ds_read_b64_tr_b16 v[40:41], v20 offset:24576
v_mfma_f32_16x16x32_bf16 v[216:219], v[56:59], a[212:215], v[216:219]
ds_read_b64_tr_b16 v[42:43], v20 offset:24832
v_mfma_f32_16x16x32_bf16 v[224:227], v[56:59], a[216:219], v[224:227]
ds_read_b64_tr_b16 v[44:45], v21 offset:24576
v_mfma_f32_16x16x32_bf16 v[232:235], v[56:59], a[220:223], v[232:235]
ds_read_b64_tr_b16 v[46:47], v21 offset:24832
v_mfma_f32_16x16x32_bf16 v[212:215], v[60:63], a[208:211], v[212:215]
ds_read_b64_tr_b16 v[48:49], v20 offset:25088
v_mfma_f32_16x16x32_bf16 v[220:223], v[60:63], a[212:215], v[220:223]
ds_read_b64_tr_b16 v[50:51], v20 offset:25344
v_mfma_f32_16x16x32_bf16 v[228:231], v[60:63], a[216:219], v[228:231]
ds_read_b64_tr_b16 v[52:53], v21 offset:25088
v_mfma_f32_16x16x32_bf16 v[236:239], v[60:63], a[220:223], v[236:239]
ds_read_b64_tr_b16 v[54:55], v21 offset:25344
v_mfma_f32_16x16x32_bf16 v[240:243], v[64:67], a[208:211], v[240:243]
v_mfma_f32_16x16x32_bf16 v[248:251], v[64:67], a[212:215], v[248:251]
v_mfma_f32_16x16x32_bf16 v[244:247], v[68:71], a[208:211], v[244:247]
v_mfma_f32_16x16x32_bf16 v[252:255], v[68:71], a[212:215], v[252:255]
s_waitcnt vmcnt(36) lgkmcnt(0)
s_barrier
v_mfma_f32_16x16x32_bf16 v[80:83], v[40:43], a[224:227], v[80:83]
ds_read_b64_tr_b16 v[56:57], v20 offset:25600
v_mfma_f32_16x16x32_bf16 v[84:87], v[40:43], a[228:231], v[84:87]
ds_read_b64_tr_b16 v[58:59], v20 offset:25856
v_mfma_f32_16x16x32_bf16 v[88:91], v[40:43], a[232:235], v[88:91]
ds_read_b64_tr_b16 v[60:61], v21 offset:25600
v_mfma_f32_16x16x32_bf16 v[92:95], v[40:43], a[236:239], v[92:95]
ds_read_b64_tr_b16 v[62:63], v21 offset:25856
v_mfma_f32_16x16x32_bf16 v[96:99], v[44:47], a[224:227], v[96:99]
ds_read_b64_tr_b16 v[64:65], v20 offset:26112
v_mfma_f32_16x16x32_bf16 v[100:103], v[44:47], a[228:231], v[100:103]
ds_read_b64_tr_b16 v[66:67], v20 offset:26368
v_mfma_f32_16x16x32_bf16 v[104:107], v[44:47], a[232:235], v[104:107]
ds_read_b64_tr_b16 v[68:69], v21 offset:26112
v_mfma_f32_16x16x32_bf16 v[108:111], v[44:47], a[236:239], v[108:111]
ds_read_b64_tr_b16 v[70:71], v21 offset:26368
v_mfma_f32_16x16x32_bf16 v[112:115], v[48:51], a[224:227], v[112:115]
s_add_u32 m0, s50, 126976
s_mov_b32 s12, 3407872
v_mfma_f32_16x16x32_bf16 v[116:119], v[48:51], a[228:231], v[116:119]
buffer_load_dwordx4 v4, s[28:31], s12 offen lds
v_mfma_f32_16x16x32_bf16 v[120:123], v[48:51], a[232:235], v[120:123]
v_mfma_f32_16x16x32_bf16 v[124:127], v[48:51], a[236:239], v[124:127]
buffer_load_dwordx4 v5, s[28:31], s12 offen offset:2048 lds
v_mfma_f32_16x16x32_bf16 v[128:131], v[52:55], a[224:227], v[128:131]
s_add_u32 m0, s51, 135168
s_mov_b32 s13, 1144832
v_mfma_f32_16x16x32_bf16 v[132:135], v[52:55], a[228:231], v[132:135]
buffer_load_dwordx4 v6, s[32:35], s13 offen lds
v_mfma_f32_16x16x32_bf16 v[136:139], v[52:55], a[232:235], v[136:139]
v_mfma_f32_16x16x32_bf16 v[140:143], v[52:55], a[236:239], v[140:143]
buffer_load_dwordx4 a[208:211], v2, s[8:11], s48 offen offset:1024 sc1
s_waitcnt lgkmcnt(0)
v_mfma_f32_16x16x32_bf16 v[144:147], v[56:59], a[224:227], v[144:147]
ds_read_b64_tr_b16 v[40:41], v22 offset:24576
v_mfma_f32_16x16x32_bf16 v[148:151], v[56:59], a[228:231], v[148:151]
ds_read_b64_tr_b16 v[42:43], v22 offset:24832
v_mfma_f32_16x16x32_bf16 v[152:155], v[56:59], a[232:235], v[152:155]
ds_read_b64_tr_b16 v[44:45], v23 offset:24576
v_mfma_f32_16x16x32_bf16 v[156:159], v[56:59], a[236:239], v[156:159]
ds_read_b64_tr_b16 v[46:47], v23 offset:24832
v_mfma_f32_16x16x32_bf16 v[160:163], v[60:63], a[224:227], v[160:163]
ds_read_b64_tr_b16 v[48:49], v22 offset:25088
v_mfma_f32_16x16x32_bf16 v[164:167], v[60:63], a[228:231], v[164:167]
ds_read_b64_tr_b16 v[50:51], v22 offset:25344
v_mfma_f32_16x16x32_bf16 v[168:171], v[60:63], a[232:235], v[168:171]
ds_read_b64_tr_b16 v[52:53], v23 offset:25088
v_mfma_f32_16x16x32_bf16 v[172:175], v[60:63], a[236:239], v[172:175]
ds_read_b64_tr_b16 v[54:55], v23 offset:25344
v_mfma_f32_16x16x32_bf16 v[176:179], v[64:67], a[224:227], v[176:179]
v_mfma_f32_16x16x32_bf16 v[180:183], v[64:67], a[228:231], v[180:183]
buffer_load_dwordx4 a[212:215], v18, s[8:11], s48 offen offset:1024 sc1
v_mfma_f32_16x16x32_bf16 v[184:187], v[64:67], a[232:235], v[184:187]
v_mfma_f32_16x16x32_bf16 v[188:191], v[64:67], a[236:239], v[188:191]
buffer_load_dwordx4 a[216:219], v2, s[8:11], s49 offen offset:1024 sc1
v_mfma_f32_16x16x32_bf16 v[192:195], v[68:71], a[224:227], v[192:195]
v_mfma_f32_16x16x32_bf16 v[196:199], v[68:71], a[228:231], v[196:199]
buffer_load_dwordx4 a[220:223], v18, s[8:11], s49 offen offset:1024 sc1
v_mfma_f32_16x16x32_bf16 v[200:203], v[68:71], a[232:235], v[200:203]
v_mfma_f32_16x16x32_bf16 v[204:207], v[68:71], a[236:239], v[204:207]
s_waitcnt lgkmcnt(0)
v_mfma_f32_16x16x32_bf16 v[208:211], v[40:43], a[224:227], v[208:211]
ds_read_b64_tr_b16 v[56:57], v20 offset:36864
v_mfma_f32_16x16x32_bf16 v[216:219], v[40:43], a[228:231], v[216:219]
ds_read_b64_tr_b16 v[58:59], v20 offset:37120
v_mfma_f32_16x16x32_bf16 v[224:227], v[40:43], a[232:235], v[224:227]
ds_read_b64_tr_b16 v[60:61], v21 offset:36864
v_mfma_f32_16x16x32_bf16 v[232:235], v[40:43], a[236:239], v[232:235]
ds_read_b64_tr_b16 v[62:63], v21 offset:37120
v_mfma_f32_16x16x32_bf16 v[212:215], v[44:47], a[224:227], v[212:215]
ds_read_b64_tr_b16 v[64:65], v20 offset:37376
v_mfma_f32_16x16x32_bf16 v[220:223], v[44:47], a[228:231], v[220:223]
ds_read_b64_tr_b16 v[66:67], v20 offset:37632
v_mfma_f32_16x16x32_bf16 v[228:231], v[44:47], a[232:235], v[228:231]
ds_read_b64_tr_b16 v[68:69], v21 offset:37376
v_mfma_f32_16x16x32_bf16 v[236:239], v[44:47], a[236:239], v[236:239]
ds_read_b64_tr_b16 v[70:71], v21 offset:37632
)ASM"
R"ASM(
v_mfma_f32_16x16x32_bf16 v[240:243], v[48:51], a[224:227], v[240:243]
v_mfma_f32_16x16x32_bf16 v[248:251], v[48:51], a[228:231], v[248:251]
v_mfma_f32_16x16x32_bf16 v[244:247], v[52:55], a[224:227], v[244:247]
v_mfma_f32_16x16x32_bf16 v[252:255], v[52:55], a[228:231], v[252:255]
s_waitcnt vmcnt(36) lgkmcnt(0)
s_barrier
v_mfma_f32_16x16x32_bf16 v[80:83], v[56:59], a[240:243], v[80:83]
ds_read_b64_tr_b16 v[40:41], v20 offset:37888
v_mfma_f32_16x16x32_bf16 v[84:87], v[56:59], a[244:247], v[84:87]
ds_read_b64_tr_b16 v[42:43], v20 offset:38144
v_mfma_f32_16x16x32_bf16 v[88:91], v[56:59], a[248:251], v[88:91]
ds_read_b64_tr_b16 v[44:45], v21 offset:37888
v_mfma_f32_16x16x32_bf16 v[92:95], v[56:59], a[252:255], v[92:95]
ds_read_b64_tr_b16 v[46:47], v21 offset:38144
v_mfma_f32_16x16x32_bf16 v[96:99], v[60:63], a[240:243], v[96:99]
ds_read_b64_tr_b16 v[48:49], v20 offset:38400
v_mfma_f32_16x16x32_bf16 v[100:103], v[60:63], a[244:247], v[100:103]
ds_read_b64_tr_b16 v[50:51], v20 offset:38656
v_mfma_f32_16x16x32_bf16 v[104:107], v[60:63], a[248:251], v[104:107]
ds_read_b64_tr_b16 v[52:53], v21 offset:38400
v_mfma_f32_16x16x32_bf16 v[108:111], v[60:63], a[252:255], v[108:111]
ds_read_b64_tr_b16 v[54:55], v21 offset:38656
v_mfma_f32_16x16x32_bf16 v[112:115], v[64:67], a[240:243], v[112:115]
s_add_u32 m0, s50, 139264
s_mov_b32 s12, 3670016
buffer_load_dword v72, v11, s[8:11], s62 offen sc1
v_mfma_f32_16x16x32_bf16 v[116:119], v[64:67], a[244:247], v[116:119]
buffer_load_dwordx4 v4, s[28:31], s12 offen lds
v_mfma_f32_16x16x32_bf16 v[120:123], v[64:67], a[248:251], v[120:123]
buffer_load_dword v73, v11, s[8:11], s62 offen offset:64 sc1
v_mfma_f32_16x16x32_bf16 v[124:127], v[64:67], a[252:255], v[124:127]
buffer_load_dwordx4 v5, s[28:31], s12 offen offset:2048 lds
v_mfma_f32_16x16x32_bf16 v[128:131], v[68:71], a[240:243], v[128:131]
s_add_u32 m0, s51, 147456
s_mov_b32 s13, 1232896
buffer_load_dword v74, v11, s[8:11], s63 offen sc1
v_mfma_f32_16x16x32_bf16 v[132:135], v[68:71], a[244:247], v[132:135]
buffer_load_dwordx4 v6, s[32:35], s13 offen lds
v_mfma_f32_16x16x32_bf16 v[136:139], v[68:71], a[248:251], v[136:139]
buffer_load_dword v75, v11, s[8:11], s63 offen offset:64 sc1
v_mfma_f32_16x16x32_bf16 v[140:143], v[68:71], a[252:255], v[140:143]
buffer_load_dwordx4 a[224:227], v2, s[8:11], s48 offen offset:2048 sc1
s_waitcnt lgkmcnt(0)
v_mfma_f32_16x16x32_bf16 v[144:147], v[40:43], a[240:243], v[144:147]
ds_read_b64_tr_b16 v[56:57], v22 offset:36864
v_mfma_f32_16x16x32_bf16 v[148:151], v[40:43], a[244:247], v[148:151]
ds_read_b64_tr_b16 v[58:59], v22 offset:37120
v_mfma_f32_16x16x32_bf16 v[152:155], v[40:43], a[248:251], v[152:155]
ds_read_b64_tr_b16 v[60:61], v23 offset:36864
v_mfma_f32_16x16x32_bf16 v[156:159], v[40:43], a[252:255], v[156:159]
ds_read_b64_tr_b16 v[62:63], v23 offset:37120
v_mfma_f32_16x16x32_bf16 v[160:163], v[44:47], a[240:243], v[160:163]
ds_read_b64_tr_b16 v[64:65], v22 offset:37376
v_mfma_f32_16x16x32_bf16 v[164:167], v[44:47], a[244:247], v[164:167]
ds_read_b64_tr_b16 v[66:67], v22 offset:37632
v_mfma_f32_16x16x32_bf16 v[168:171], v[44:47], a[248:251], v[168:171]
ds_read_b64_tr_b16 v[68:69], v23 offset:37376
v_mfma_f32_16x16x32_bf16 v[172:175], v[44:47], a[252:255], v[172:175]
ds_read_b64_tr_b16 v[70:71], v23 offset:37632
v_mfma_f32_16x16x32_bf16 v[176:179], v[48:51], a[240:243], v[176:179]
buffer_load_dwordx2 v[28:29], v12, s[36:39], s80 offen
v_mfma_f32_16x16x32_bf16 v[180:183], v[48:51], a[244:247], v[180:183]
buffer_load_dwordx4 a[228:231], v18, s[8:11], s48 offen offset:2048 sc1
v_mfma_f32_16x16x32_bf16 v[184:187], v[48:51], a[248:251], v[184:187]
buffer_load_dwordx2 v[30:31], v12, s[36:39], s80 offen offset:32
v_mfma_f32_16x16x32_bf16 v[188:191], v[48:51], a[252:255], v[188:191]
buffer_load_dwordx4 a[232:235], v2, s[8:11], s49 offen offset:2048 sc1
v_mfma_f32_16x16x32_bf16 v[192:195], v[52:55], a[240:243], v[192:195]
buffer_load_dwordx2 v[32:33], v12, s[36:39], s81 offen
v_mfma_f32_16x16x32_bf16 v[196:199], v[52:55], a[244:247], v[196:199]
buffer_load_dwordx4 a[236:239], v18, s[8:11], s49 offen offset:2048 sc1
v_mfma_f32_16x16x32_bf16 v[200:203], v[52:55], a[248:251], v[200:203]
buffer_load_dwordx2 v[34:35], v12, s[36:39], s81 offen offset:32
v_mfma_f32_16x16x32_bf16 v[204:207], v[52:55], a[252:255], v[204:207]
s_waitcnt lgkmcnt(0)
v_mfma_f32_16x16x32_bf16 v[208:211], v[56:59], a[240:243], v[208:211]
ds_read_b64_tr_b16 v[40:41], v7 offset:0
v_mfma_f32_16x16x32_bf16 v[216:219], v[56:59], a[244:247], v[216:219]
ds_read_b64_tr_b16 v[42:43], v7 offset:256
v_mfma_f32_16x16x32_bf16 v[224:227], v[56:59], a[248:251], v[224:227]
ds_read_b64_tr_b16 v[44:45], v8 offset:0
v_mfma_f32_16x16x32_bf16 v[232:235], v[56:59], a[252:255], v[232:235]
ds_read_b64_tr_b16 v[46:47], v8 offset:256
v_mfma_f32_16x16x32_bf16 v[212:215], v[60:63], a[240:243], v[212:215]
ds_read_b64_tr_b16 v[48:49], v7 offset:512
v_mfma_f32_16x16x32_bf16 v[220:223], v[60:63], a[244:247], v[220:223]
ds_read_b64_tr_b16 v[50:51], v7 offset:768
v_mfma_f32_16x16x32_bf16 v[228:231], v[60:63], a[248:251], v[228:231]
ds_read_b64_tr_b16 v[52:53], v8 offset:512
v_mfma_f32_16x16x32_bf16 v[236:239], v[60:63], a[252:255], v[236:239]
ds_read_b64_tr_b16 v[54:55], v8 offset:768
v_mfma_f32_16x16x32_bf16 v[240:243], v[64:67], a[240:243], v[240:243]
v_mfma_f32_16x16x32_bf16 v[248:251], v[64:67], a[244:247], v[248:251]
v_mfma_f32_16x16x32_bf16 v[244:247], v[68:71], a[240:243], v[244:247]
v_mfma_f32_16x16x32_bf16 v[252:255], v[68:71], a[244:247], v[252:255]
s_waitcnt vmcnt(44) lgkmcnt(0)
s_barrier
v_mfma_f32_16x16x32_bf16 v[80:83], v[40:43], a[128:131], v[80:83]
ds_read_b64_tr_b16 v[56:57], v7 offset:1024
v_mfma_f32_16x16x32_bf16 v[84:87], v[40:43], a[132:135], v[84:87]
ds_read_b64_tr_b16 v[58:59], v7 offset:1280
v_mfma_f32_16x16x32_bf16 v[88:91], v[40:43], a[136:139], v[88:91]
ds_read_b64_tr_b16 v[60:61], v8 offset:1024
v_mfma_f32_16x16x32_bf16 v[92:95], v[40:43], a[140:143], v[92:95]
ds_read_b64_tr_b16 v[62:63], v8 offset:1280
v_mfma_f32_16x16x32_bf16 v[96:99], v[44:47], a[128:131], v[96:99]
ds_read_b64_tr_b16 v[64:65], v7 offset:1536
v_mfma_f32_16x16x32_bf16 v[100:103], v[44:47], a[132:135], v[100:103]
ds_read_b64_tr_b16 v[66:67], v7 offset:1792
v_mfma_f32_16x16x32_bf16 v[104:107], v[44:47], a[136:139], v[104:107]
ds_read_b64_tr_b16 v[68:69], v8 offset:1536
v_mfma_f32_16x16x32_bf16 v[108:111], v[44:47], a[140:143], v[108:111]
ds_read_b64_tr_b16 v[70:71], v8 offset:1792
v_mfma_f32_16x16x32_bf16 v[112:115], v[48:51], a[128:131], v[112:115]
s_add_u32 m0, s50, 151552
s_mov_b32 s12, 3932160
v_mfma_f32_16x16x32_bf16 v[116:119], v[48:51], a[132:135], v[116:119]
buffer_load_dwordx4 v4, s[28:31], s12 offen lds
v_mfma_f32_16x16x32_bf16 v[120:123], v[48:51], a[136:139], v[120:123]
v_mfma_f32_16x16x32_bf16 v[124:127], v[48:51], a[140:143], v[124:127]
buffer_load_dwordx4 v5, s[28:31], s12 offen offset:2048 lds
v_mfma_f32_16x16x32_bf16 v[128:131], v[52:55], a[128:131], v[128:131]
s_add_u32 m0, s51, 159744
s_mov_b32 s13, 1320960
v_mfma_f32_16x16x32_bf16 v[132:135], v[52:55], a[132:135], v[132:135]
buffer_load_dwordx4 v6, s[32:35], s13 offen lds
v_mfma_f32_16x16x32_bf16 v[136:139], v[52:55], a[136:139], v[136:139]
v_mfma_f32_16x16x32_bf16 v[140:143], v[52:55], a[140:143], v[140:143]
buffer_load_dwordx4 a[240:243], v2, s[8:11], s48 offen offset:3072 sc1
s_waitcnt lgkmcnt(0)
v_mfma_f32_16x16x32_bf16 v[144:147], v[56:59], a[128:131], v[144:147]
ds_read_b64_tr_b16 v[40:41], v9 offset:0
v_mfma_f32_16x16x32_bf16 v[148:151], v[56:59], a[132:135], v[148:151]
ds_read_b64_tr_b16 v[42:43], v9 offset:256
v_mfma_f32_16x16x32_bf16 v[152:155], v[56:59], a[136:139], v[152:155]
ds_read_b64_tr_b16 v[44:45], v10 offset:0
v_mfma_f32_16x16x32_bf16 v[156:159], v[56:59], a[140:143], v[156:159]
ds_read_b64_tr_b16 v[46:47], v10 offset:256
v_mfma_f32_16x16x32_bf16 v[160:163], v[60:63], a[128:131], v[160:163]
ds_read_b64_tr_b16 v[48:49], v9 offset:512
v_mfma_f32_16x16x32_bf16 v[164:167], v[60:63], a[132:135], v[164:167]
ds_read_b64_tr_b16 v[50:51], v9 offset:768
v_mfma_f32_16x16x32_bf16 v[168:171], v[60:63], a[136:139], v[168:171]
ds_read_b64_tr_b16 v[52:53], v10 offset:512
v_mfma_f32_16x16x32_bf16 v[172:175], v[60:63], a[140:143], v[172:175]
ds_read_b64_tr_b16 v[54:55], v10 offset:768
v_mfma_f32_16x16x32_bf16 v[176:179], v[64:67], a[128:131], v[176:179]
v_mfma_f32_16x16x32_bf16 v[180:183], v[64:67], a[132:135], v[180:183]
buffer_load_dwordx4 a[244:247], v18, s[8:11], s48 offen offset:3072 sc1
v_mfma_f32_16x16x32_bf16 v[184:187], v[64:67], a[136:139], v[184:187]
v_mfma_f32_16x16x32_bf16 v[188:191], v[64:67], a[140:143], v[188:191]
buffer_load_dwordx4 a[248:251], v2, s[8:11], s49 offen offset:3072 sc1
v_mfma_f32_16x16x32_bf16 v[192:195], v[68:71], a[128:131], v[192:195]
v_mfma_f32_16x16x32_bf16 v[196:199], v[68:71], a[132:135], v[196:199]
buffer_load_dwordx4 a[252:255], v18, s[8:11], s49 offen offset:3072 sc1
s_add_u32 s48, s48, 4096
s_add_u32 s49, s49, 4096
v_mfma_f32_16x16x32_bf16 v[200:203], v[68:71], a[136:139], v[200:203]
v_mfma_f32_16x16x32_bf16 v[204:207], v[68:71], a[140:143], v[204:207]
s_waitcnt lgkmcnt(0)
v_mfma_f32_16x16x32_bf16 v[208:211], v[40:43], a[128:131], v[208:211]
ds_read_b64_tr_b16 v[56:57], v7 offset:12288
v_mfma_f32_16x16x32_bf16 v[216:219], v[40:43], a[132:135], v[216:219]
ds_read_b64_tr_b16 v[58:59], v7 offset:12544
v_mfma_f32_16x16x32_bf16 v[224:227], v[40:43], a[136:139], v[224:227]
ds_read_b64_tr_b16 v[60:61], v8 offset:12288
v_mfma_f32_16x16x32_bf16 v[232:235], v[40:43], a[140:143], v[232:235]
ds_read_b64_tr_b16 v[62:63], v8 offset:12544
v_mfma_f32_16x16x32_bf16 v[212:215], v[44:47], a[128:131], v[212:215]
ds_read_b64_tr_b16 v[64:65], v7 offset:12800
v_mfma_f32_16x16x32_bf16 v[220:223], v[44:47], a[132:135], v[220:223]
ds_read_b64_tr_b16 v[66:67], v7 offset:13056
v_mfma_f32_16x16x32_bf16 v[228:231], v[44:47], a[136:139], v[228:231]
ds_read_b64_tr_b16 v[68:69], v8 offset:12800
v_mfma_f32_16x16x32_bf16 v[236:239], v[44:47], a[140:143], v[236:239]
ds_read_b64_tr_b16 v[70:71], v8 offset:13056
v_mfma_f32_16x16x32_bf16 v[240:243], v[48:51], a[128:131], v[240:243]
v_and_b32 v38, 112, v2
v_lshlrev_b32 v38, 4, v38
buffer_load_dword v39, v38, s[8:11], s89 offen sc1
v_mfma_f32_16x16x32_bf16 v[248:251], v[48:51], a[132:135], v[248:251]
v_mfma_f32_16x16x32_bf16 v[244:247], v[52:55], a[128:131], v[244:247]
v_mfma_f32_16x16x32_bf16 v[252:255], v[52:55], a[132:135], v[252:255]
s_waitcnt vmcnt(0) lgkmcnt(0)
s_barrier
s_add_u32 s98, s101, 1
v_subrev_u32 v39, s98, v39
v_cmp_lt_u32 vcc, 0, v39
s_cmp_lg_u64 vcc, 0
s_cbranch_scc1 .Lsp2_%=
.Lsr2_%=:
v_mfma_f32_16x16x32_bf16 v[80:83], v[56:59], a[144:147], v[80:83]
ds_read_b64_tr_b16 v[40:41], v7 offset:13312
v_mfma_f32_16x16x32_bf16 v[84:87], v[56:59], a[148:151], v[84:87]
ds_read_b64_tr_b16 v[42:43], v7 offset:13568
v_mfma_f32_16x16x32_bf16 v[88:91], v[56:59], a[152:155], v[88:91]
ds_read_b64_tr_b16 v[44:45], v8 offset:13312
v_mfma_f32_16x16x32_bf16 v[92:95], v[56:59], a[156:159], v[92:95]
ds_read_b64_tr_b16 v[46:47], v8 offset:13568
v_mfma_f32_16x16x32_bf16 v[96:99], v[60:63], a[144:147], v[96:99]
)ASM"
R"ASM(
ds_read_b64_tr_b16 v[48:49], v7 offset:13824
v_mfma_f32_16x16x32_bf16 v[100:103], v[60:63], a[148:151], v[100:103]
ds_read_b64_tr_b16 v[50:51], v7 offset:14080
v_mfma_f32_16x16x32_bf16 v[104:107], v[60:63], a[152:155], v[104:107]
ds_read_b64_tr_b16 v[52:53], v8 offset:13824
v_mfma_f32_16x16x32_bf16 v[108:111], v[60:63], a[156:159], v[108:111]
ds_read_b64_tr_b16 v[54:55], v8 offset:14080
v_mfma_f32_16x16x32_bf16 v[112:115], v[64:67], a[144:147], v[112:115]
v_mov_b32 v38, s101
v_mov_b32 v39, 0
buffer_store_dword v38, v39, s[8:11], s87 offen sc1
v_mfma_f32_16x16x32_bf16 v[116:119], v[64:67], a[148:151], v[116:119]
v_accvgpr_write_b32 a0, 0
v_accvgpr_write_b32 a1, 0
v_mfma_f32_16x16x32_bf16 v[120:123], v[64:67], a[152:155], v[120:123]
s_mov_b32 s92, s82
s_mov_b32 s93, s14
v_mfma_f32_16x16x32_bf16 v[124:127], v[64:67], a[156:159], v[124:127]
v_accvgpr_write_b32 a2, 0
v_accvgpr_write_b32 a3, 0
v_mfma_f32_16x16x32_bf16 v[128:131], v[68:71], a[144:147], v[128:131]
s_mov_b32 s94, s61
s_add_u32 s95, s61, 65536
v_mfma_f32_16x16x32_bf16 v[132:135], v[68:71], a[148:151], v[132:135]
v_accvgpr_write_b32 a4, 0
v_accvgpr_write_b32 a5, 0
v_mfma_f32_16x16x32_bf16 v[136:139], v[68:71], a[152:155], v[136:139]
v_accvgpr_write_b32 a6, 0
v_accvgpr_write_b32 a7, 0
v_mfma_f32_16x16x32_bf16 v[140:143], v[68:71], a[156:159], v[140:143]
v_accvgpr_write_b32 a8, 0
v_accvgpr_write_b32 a9, 0
s_waitcnt lgkmcnt(0)
v_mfma_f32_16x16x32_bf16 v[144:147], v[40:43], a[144:147], v[144:147]
ds_read_b64_tr_b16 v[56:57], v9 offset:12288
v_mfma_f32_16x16x32_bf16 v[148:151], v[40:43], a[148:151], v[148:151]
ds_read_b64_tr_b16 v[58:59], v9 offset:12544
v_mfma_f32_16x16x32_bf16 v[152:155], v[40:43], a[152:155], v[152:155]
ds_read_b64_tr_b16 v[60:61], v10 offset:12288
v_mfma_f32_16x16x32_bf16 v[156:159], v[40:43], a[156:159], v[156:159]
ds_read_b64_tr_b16 v[62:63], v10 offset:12544
v_mfma_f32_16x16x32_bf16 v[160:163], v[44:47], a[144:147], v[160:163]
ds_read_b64_tr_b16 v[64:65], v9 offset:12800
v_mfma_f32_16x16x32_bf16 v[164:167], v[44:47], a[148:151], v[164:167]
ds_read_b64_tr_b16 v[66:67], v9 offset:13056
v_mfma_f32_16x16x32_bf16 v[168:171], v[44:47], a[152:155], v[168:171]
ds_read_b64_tr_b16 v[68:69], v10 offset:12800
v_mfma_f32_16x16x32_bf16 v[172:175], v[44:47], a[156:159], v[172:175]
ds_read_b64_tr_b16 v[70:71], v10 offset:13056
v_mfma_f32_16x16x32_bf16 v[176:179], v[48:51], a[144:147], v[176:179]
v_accvgpr_write_b32 a10, 0
v_accvgpr_write_b32 a11, 0
v_mfma_f32_16x16x32_bf16 v[180:183], v[48:51], a[148:151], v[180:183]
v_accvgpr_write_b32 a12, 0
v_accvgpr_write_b32 a13, 0
v_mfma_f32_16x16x32_bf16 v[184:187], v[48:51], a[152:155], v[184:187]
v_accvgpr_write_b32 a14, 0
v_accvgpr_write_b32 a15, 0
v_mfma_f32_16x16x32_bf16 v[188:191], v[48:51], a[156:159], v[188:191]
v_accvgpr_write_b32 a16, 0
v_accvgpr_write_b32 a17, 0
v_mfma_f32_16x16x32_bf16 v[192:195], v[52:55], a[144:147], v[192:195]
v_accvgpr_write_b32 a18, 0
v_accvgpr_write_b32 a19, 0
v_mfma_f32_16x16x32_bf16 v[196:199], v[52:55], a[148:151], v[196:199]
v_accvgpr_write_b32 a20, 0
v_accvgpr_write_b32 a21, 0
v_mfma_f32_16x16x32_bf16 v[200:203], v[52:55], a[152:155], v[200:203]
v_accvgpr_write_b32 a22, 0
v_accvgpr_write_b32 a23, 0
v_mfma_f32_16x16x32_bf16 v[204:207], v[52:55], a[156:159], v[204:207]
v_accvgpr_write_b32 a24, 0
v_accvgpr_write_b32 a25, 0
s_waitcnt lgkmcnt(0)
v_mfma_f32_16x16x32_bf16 v[208:211], v[56:59], a[144:147], v[208:211]
ds_read_b64_tr_b16 v[40:41], v7 offset:24576
v_mfma_f32_16x16x32_bf16 v[216:219], v[56:59], a[148:151], v[216:219]
ds_read_b64_tr_b16 v[42:43], v7 offset:24832
v_mfma_f32_16x16x32_bf16 v[224:227], v[56:59], a[152:155], v[224:227]
ds_read_b64_tr_b16 v[44:45], v8 offset:24576
v_mfma_f32_16x16x32_bf16 v[232:235], v[56:59], a[156:159], v[232:235]
ds_read_b64_tr_b16 v[46:47], v8 offset:24832
v_mfma_f32_16x16x32_bf16 v[212:215], v[60:63], a[144:147], v[212:215]
ds_read_b64_tr_b16 v[48:49], v7 offset:25088
v_mfma_f32_16x16x32_bf16 v[220:223], v[60:63], a[148:151], v[220:223]
ds_read_b64_tr_b16 v[50:51], v7 offset:25344
v_mfma_f32_16x16x32_bf16 v[228:231], v[60:63], a[152:155], v[228:231]
ds_read_b64_tr_b16 v[52:53], v8 offset:25088
v_mfma_f32_16x16x32_bf16 v[236:239], v[60:63], a[156:159], v[236:239]
ds_read_b64_tr_b16 v[54:55], v8 offset:25344
v_mfma_f32_16x16x32_bf16 v[240:243], v[64:67], a[144:147], v[240:243]
v_accvgpr_write_b32 a26, 0
v_accvgpr_write_b32 a27, 0
v_mfma_f32_16x16x32_bf16 v[248:251], v[64:67], a[148:151], v[248:251]
v_accvgpr_write_b32 a28, 0
v_accvgpr_write_b32 a29, 0
v_mfma_f32_16x16x32_bf16 v[244:247], v[68:71], a[144:147], v[244:247]
v_accvgpr_write_b32 a30, 0
v_accvgpr_write_b32 a31, 0
v_mfma_f32_16x16x32_bf16 v[252:255], v[68:71], a[148:151], v[252:255]
v_accvgpr_write_b32 a32, 0
v_accvgpr_write_b32 a33, 0
s_waitcnt vmcnt(39) lgkmcnt(0)
s_barrier
v_mfma_f32_16x16x32_bf16 v[80:83], v[40:43], a[160:163], v[80:83]
ds_read_b64_tr_b16 v[56:57], v7 offset:25600
v_mfma_f32_16x16x32_bf16 v[84:87], v[40:43], a[164:167], v[84:87]
ds_read_b64_tr_b16 v[58:59], v7 offset:25856
v_mfma_f32_16x16x32_bf16 v[88:91], v[40:43], a[168:171], v[88:91]
ds_read_b64_tr_b16 v[60:61], v8 offset:25600
v_mfma_f32_16x16x32_bf16 v[92:95], v[40:43], a[172:175], v[92:95]
ds_read_b64_tr_b16 v[62:63], v8 offset:25856
v_mfma_f32_16x16x32_bf16 v[96:99], v[44:47], a[160:163], v[96:99]
ds_read_b64_tr_b16 v[64:65], v7 offset:26112
v_mfma_f32_16x16x32_bf16 v[100:103], v[44:47], a[164:167], v[100:103]
ds_read_b64_tr_b16 v[66:67], v7 offset:26368
v_mfma_f32_16x16x32_bf16 v[104:107], v[44:47], a[168:171], v[104:107]
ds_read_b64_tr_b16 v[68:69], v8 offset:26112
v_mfma_f32_16x16x32_bf16 v[108:111], v[44:47], a[172:175], v[108:111]
ds_read_b64_tr_b16 v[70:71], v8 offset:26368
v_mfma_f32_16x16x32_bf16 v[112:115], v[48:51], a[160:163], v[112:115]
s_mov_b32 m0, s94
v_accvgpr_write_b32 a34, 0
v_mfma_f32_16x16x32_bf16 v[116:119], v[48:51], a[164:167], v[116:119]
buffer_load_dwordx4 v3, s[8:11], s92 offen offset:0 sc1 lds
v_mfma_f32_16x16x32_bf16 v[120:123], v[48:51], a[168:171], v[120:123]
v_accvgpr_write_b32 a35, 0
v_accvgpr_write_b32 a36, 0
v_mfma_f32_16x16x32_bf16 v[124:127], v[48:51], a[172:175], v[124:127]
buffer_load_dwordx4 v3, s[8:11], s92 offen offset:1024 sc1 lds
v_mfma_f32_16x16x32_bf16 v[128:131], v[52:55], a[160:163], v[128:131]
v_accvgpr_write_b32 a37, 0
v_accvgpr_write_b32 a38, 0
v_mfma_f32_16x16x32_bf16 v[132:135], v[52:55], a[164:167], v[132:135]
v_accvgpr_write_b32 a39, 0
v_accvgpr_write_b32 a40, 0
v_mfma_f32_16x16x32_bf16 v[136:139], v[52:55], a[168:171], v[136:139]
v_accvgpr_write_b32 a41, 0
v_accvgpr_write_b32 a42, 0
v_mfma_f32_16x16x32_bf16 v[140:143], v[52:55], a[172:175], v[140:143]
v_accvgpr_write_b32 a43, 0
v_accvgpr_write_b32 a44, 0
s_waitcnt lgkmcnt(0)
v_mfma_f32_16x16x32_bf16 v[144:147], v[56:59], a[160:163], v[144:147]
ds_read_b64_tr_b16 v[40:41], v9 offset:24576
v_mfma_f32_16x16x32_bf16 v[148:151], v[56:59], a[164:167], v[148:151]
ds_read_b64_tr_b16 v[42:43], v9 offset:24832
v_mfma_f32_16x16x32_bf16 v[152:155], v[56:59], a[168:171], v[152:155]
ds_read_b64_tr_b16 v[44:45], v10 offset:24576
v_mfma_f32_16x16x32_bf16 v[156:159], v[56:59], a[172:175], v[156:159]
ds_read_b64_tr_b16 v[46:47], v10 offset:24832
v_mfma_f32_16x16x32_bf16 v[160:163], v[60:63], a[160:163], v[160:163]
ds_read_b64_tr_b16 v[48:49], v9 offset:25088
v_mfma_f32_16x16x32_bf16 v[164:167], v[60:63], a[164:167], v[164:167]
ds_read_b64_tr_b16 v[50:51], v9 offset:25344
v_mfma_f32_16x16x32_bf16 v[168:171], v[60:63], a[168:171], v[168:171]
ds_read_b64_tr_b16 v[52:53], v10 offset:25088
v_mfma_f32_16x16x32_bf16 v[172:175], v[60:63], a[172:175], v[172:175]
ds_read_b64_tr_b16 v[54:55], v10 offset:25344
v_mfma_f32_16x16x32_bf16 v[176:179], v[64:67], a[160:163], v[176:179]
v_accvgpr_write_b32 a45, 0
v_accvgpr_write_b32 a46, 0
v_mfma_f32_16x16x32_bf16 v[180:183], v[64:67], a[164:167], v[180:183]
buffer_load_dwordx4 v3, s[8:11], s92 offen offset:2048 sc1 lds
v_mfma_f32_16x16x32_bf16 v[184:187], v[64:67], a[168:171], v[184:187]
v_accvgpr_write_b32 a47, 0
v_accvgpr_write_b32 a48, 0
v_mfma_f32_16x16x32_bf16 v[188:191], v[64:67], a[172:175], v[188:191]
buffer_load_dwordx4 v3, s[8:11], s92 offen offset:3072 sc1 lds
s_add_u32 s92, s92, 8192
v_mfma_f32_16x16x32_bf16 v[192:195], v[68:71], a[160:163], v[192:195]
v_accvgpr_write_b32 a49, 0
v_accvgpr_write_b32 a50, 0
v_mfma_f32_16x16x32_bf16 v[196:199], v[68:71], a[164:167], v[196:199]
v_accvgpr_write_b32 a51, 0
v_accvgpr_write_b32 a52, 0
v_mfma_f32_16x16x32_bf16 v[200:203], v[68:71], a[168:171], v[200:203]
v_accvgpr_write_b32 a53, 0
v_accvgpr_write_b32 a54, 0
v_mfma_f32_16x16x32_bf16 v[204:207], v[68:71], a[172:175], v[204:207]
v_accvgpr_write_b32 a55, 0
v_accvgpr_write_b32 a56, 0
s_waitcnt lgkmcnt(0)
v_mfma_f32_16x16x32_bf16 v[208:211], v[40:43], a[160:163], v[208:211]
ds_read_b64_tr_b16 v[56:57], v7 offset:36864
v_mfma_f32_16x16x32_bf16 v[216:219], v[40:43], a[164:167], v[216:219]
ds_read_b64_tr_b16 v[58:59], v7 offset:37120
v_mfma_f32_16x16x32_bf16 v[224:227], v[40:43], a[168:171], v[224:227]
ds_read_b64_tr_b16 v[60:61], v8 offset:36864
v_mfma_f32_16x16x32_bf16 v[232:235], v[40:43], a[172:175], v[232:235]
ds_read_b64_tr_b16 v[62:63], v8 offset:37120
v_mfma_f32_16x16x32_bf16 v[212:215], v[44:47], a[160:163], v[212:215]
ds_read_b64_tr_b16 v[64:65], v7 offset:37376
v_mfma_f32_16x16x32_bf16 v[220:223], v[44:47], a[164:167], v[220:223]
ds_read_b64_tr_b16 v[66:67], v7 offset:37632
v_mfma_f32_16x16x32_bf16 v[228:231], v[44:47], a[168:171], v[228:231]
ds_read_b64_tr_b16 v[68:69], v8 offset:37376
v_mfma_f32_16x16x32_bf16 v[236:239], v[44:47], a[172:175], v[236:239]
ds_read_b64_tr_b16 v[70:71], v8 offset:37632
v_mfma_f32_16x16x32_bf16 v[240:243], v[48:51], a[160:163], v[240:243]
)ASM"
R"ASM(
s_add_u32 m0, s94, 16384
v_accvgpr_write_b32 a57, 0
v_mfma_f32_16x16x32_bf16 v[248:251], v[48:51], a[164:167], v[248:251]
buffer_load_dwordx4 v3, s[8:11], s92 offen offset:0 sc1 lds
v_mfma_f32_16x16x32_bf16 v[244:247], v[52:55], a[160:163], v[244:247]
v_accvgpr_write_b32 a58, 0
v_accvgpr_write_b32 a59, 0
v_mfma_f32_16x16x32_bf16 v[252:255], v[52:55], a[164:167], v[252:255]
buffer_load_dwordx4 v3, s[8:11], s92 offen offset:1024 sc1 lds
s_waitcnt vmcnt(37) lgkmcnt(0)
s_barrier
v_mfma_f32_16x16x32_bf16 v[80:83], v[56:59], a[176:179], v[80:83]
ds_read_b64_tr_b16 v[40:41], v7 offset:37888
v_mfma_f32_16x16x32_bf16 v[84:87], v[56:59], a[180:183], v[84:87]
ds_read_b64_tr_b16 v[42:43], v7 offset:38144
v_mfma_f32_16x16x32_bf16 v[88:91], v[56:59], a[184:187], v[88:91]
ds_read_b64_tr_b16 v[44:45], v8 offset:37888
v_mfma_f32_16x16x32_bf16 v[92:95], v[56:59], a[188:191], v[92:95]
ds_read_b64_tr_b16 v[46:47], v8 offset:38144
v_mfma_f32_16x16x32_bf16 v[96:99], v[60:63], a[176:179], v[96:99]
ds_read_b64_tr_b16 v[48:49], v7 offset:38400
v_mfma_f32_16x16x32_bf16 v[100:103], v[60:63], a[180:183], v[100:103]
ds_read_b64_tr_b16 v[50:51], v7 offset:38656
v_mfma_f32_16x16x32_bf16 v[104:107], v[60:63], a[184:187], v[104:107]
ds_read_b64_tr_b16 v[52:53], v8 offset:38400
v_mfma_f32_16x16x32_bf16 v[108:111], v[60:63], a[188:191], v[108:111]
ds_read_b64_tr_b16 v[54:55], v8 offset:38656
v_mfma_f32_16x16x32_bf16 v[112:115], v[64:67], a[176:179], v[112:115]
v_accvgpr_write_b32 a60, 0
v_accvgpr_write_b32 a61, 0
v_mfma_f32_16x16x32_bf16 v[116:119], v[64:67], a[180:183], v[116:119]
buffer_load_dwordx4 v3, s[8:11], s92 offen offset:2048 sc1 lds
v_mfma_f32_16x16x32_bf16 v[120:123], v[64:67], a[184:187], v[120:123]
v_accvgpr_write_b32 a62, 0
v_accvgpr_write_b32 a63, 0
v_mfma_f32_16x16x32_bf16 v[124:127], v[64:67], a[188:191], v[124:127]
buffer_load_dwordx4 v3, s[8:11], s92 offen offset:3072 sc1 lds
s_add_u32 s92, s92, 24576
s_cmp_eq_u32 s92, s52
s_cselect_b32 s92, s82, s92
s_cselect_b32 s52, -1, s52
s_xor_b32 s94, s94, 32768
v_mfma_f32_16x16x32_bf16 v[128:131], v[68:71], a[176:179], v[128:131]
v_accvgpr_write_b32 a64, 0
v_accvgpr_write_b32 a65, 0
v_mfma_f32_16x16x32_bf16 v[132:135], v[68:71], a[180:183], v[132:135]
v_accvgpr_write_b32 a66, 0
v_accvgpr_write_b32 a67, 0
v_mfma_f32_16x16x32_bf16 v[136:139], v[68:71], a[184:187], v[136:139]
v_accvgpr_write_b32 a68, 0
v_accvgpr_write_b32 a69, 0
v_mfma_f32_16x16x32_bf16 v[140:143], v[68:71], a[188:191], v[140:143]
v_accvgpr_write_b32 a70, 0
v_accvgpr_write_b32 a71, 0
s_waitcnt lgkmcnt(0)
v_mfma_f32_16x16x32_bf16 v[144:147], v[40:43], a[176:179], v[144:147]
ds_read_b64_tr_b16 v[56:57], v9 offset:36864
v_mfma_f32_16x16x32_bf16 v[148:151], v[40:43], a[180:183], v[148:151]
ds_read_b64_tr_b16 v[58:59], v9 offset:37120
v_mfma_f32_16x16x32_bf16 v[152:155], v[40:43], a[184:187], v[152:155]
ds_read_b64_tr_b16 v[60:61], v10 offset:36864
v_mfma_f32_16x16x32_bf16 v[156:159], v[40:43], a[188:191], v[156:159]
ds_read_b64_tr_b16 v[62:63], v10 offset:37120
v_mfma_f32_16x16x32_bf16 v[160:163], v[44:47], a[176:179], v[160:163]
ds_read_b64_tr_b16 v[64:65], v9 offset:37376
v_mfma_f32_16x16x32_bf16 v[164:167], v[44:47], a[180:183], v[164:167]
ds_read_b64_tr_b16 v[66:67], v9 offset:37632
v_mfma_f32_16x16x32_bf16 v[168:171], v[44:47], a[184:187], v[168:171]
ds_read_b64_tr_b16 v[68:69], v10 offset:37376
v_mfma_f32_16x16x32_bf16 v[172:175], v[44:47], a[188:191], v[172:175]
ds_read_b64_tr_b16 v[70:71], v10 offset:37632
v_mfma_f32_16x16x32_bf16 v[176:179], v[48:51], a[176:179], v[176:179]
s_mov_b32 m0, s94
v_accvgpr_write_b32 a72, 0
v_mfma_f32_16x16x32_bf16 v[180:183], v[48:51], a[180:183], v[180:183]
buffer_load_dwordx4 v3, s[8:11], s92 offen offset:0 sc1 lds
v_mfma_f32_16x16x32_bf16 v[184:187], v[48:51], a[184:187], v[184:187]
v_accvgpr_write_b32 a73, 0
v_accvgpr_write_b32 a74, 0
v_mfma_f32_16x16x32_bf16 v[188:191], v[48:51], a[188:191], v[188:191]
buffer_load_dwordx4 v3, s[8:11], s92 offen offset:1024 sc1 lds
v_mfma_f32_16x16x32_bf16 v[192:195], v[52:55], a[176:179], v[192:195]
v_accvgpr_write_b32 a75, 0
v_accvgpr_write_b32 a76, 0
v_mfma_f32_16x16x32_bf16 v[196:199], v[52:55], a[180:183], v[196:199]
v_accvgpr_write_b32 a77, 0
v_accvgpr_write_b32 a78, 0
v_mfma_f32_16x16x32_bf16 v[200:203], v[52:55], a[184:187], v[200:203]
v_accvgpr_write_b32 a79, 0
v_accvgpr_write_b32 a80, 0
v_mfma_f32_16x16x32_bf16 v[204:207], v[52:55], a[188:191], v[204:207]
v_accvgpr_write_b32 a81, 0
v_accvgpr_write_b32 a82, 0
s_waitcnt lgkmcnt(0)
v_mfma_f32_16x16x32_bf16 v[208:211], v[56:59], a[176:179], v[208:211]
ds_read_b64_tr_b16 v[40:41], v20 offset:0
v_mfma_f32_16x16x32_bf16 v[216:219], v[56:59], a[180:183], v[216:219]
ds_read_b64_tr_b16 v[42:43], v20 offset:256
v_mfma_f32_16x16x32_bf16 v[224:227], v[56:59], a[184:187], v[224:227]
ds_read_b64_tr_b16 v[44:45], v21 offset:0
v_mfma_f32_16x16x32_bf16 v[232:235], v[56:59], a[188:191], v[232:235]
ds_read_b64_tr_b16 v[46:47], v21 offset:256
v_mfma_f32_16x16x32_bf16 v[212:215], v[60:63], a[176:179], v[212:215]
ds_read_b64_tr_b16 v[48:49], v20 offset:512
v_mfma_f32_16x16x32_bf16 v[220:223], v[60:63], a[180:183], v[220:223]
ds_read_b64_tr_b16 v[50:51], v20 offset:768
v_mfma_f32_16x16x32_bf16 v[228:231], v[60:63], a[184:187], v[228:231]
ds_read_b64_tr_b16 v[52:53], v21 offset:512
v_mfma_f32_16x16x32_bf16 v[236:239], v[60:63], a[188:191], v[236:239]
ds_read_b64_tr_b16 v[54:55], v21 offset:768
v_mfma_f32_16x16x32_bf16 v[240:243], v[64:67], a[176:179], v[240:243]
v_accvgpr_write_b32 a83, 0
v_accvgpr_write_b32 a84, 0
v_mfma_f32_16x16x32_bf16 v[248:251], v[64:67], a[180:183], v[248:251]
buffer_load_dwordx4 v3, s[8:11], s92 offen offset:2048 sc1 lds
v_mfma_f32_16x16x32_bf16 v[244:247], v[68:71], a[176:179], v[244:247]
v_accvgpr_write_b32 a85, 0
v_accvgpr_write_b32 a86, 0
v_mfma_f32_16x16x32_bf16 v[252:255], v[68:71], a[180:183], v[252:255]
buffer_load_dwordx4 v3, s[8:11], s92 offen offset:3072 sc1 lds
s_add_u32 s92, s92, 8192
s_waitcnt vmcnt(36) lgkmcnt(0)
s_barrier
v_mfma_f32_16x16x32_bf16 v[80:83], v[40:43], a[192:195], v[80:83]
ds_read_b64_tr_b16 v[56:57], v20 offset:1024
v_mfma_f32_16x16x32_bf16 v[84:87], v[40:43], a[196:199], v[84:87]
ds_read_b64_tr_b16 v[58:59], v20 offset:1280
v_mfma_f32_16x16x32_bf16 v[88:91], v[40:43], a[200:203], v[88:91]
ds_read_b64_tr_b16 v[60:61], v21 offset:1024
v_mfma_f32_16x16x32_bf16 v[92:95], v[40:43], a[204:207], v[92:95]
ds_read_b64_tr_b16 v[62:63], v21 offset:1280
v_mfma_f32_16x16x32_bf16 v[96:99], v[44:47], a[192:195], v[96:99]
ds_read_b64_tr_b16 v[64:65], v20 offset:1536
v_mfma_f32_16x16x32_bf16 v[100:103], v[44:47], a[196:199], v[100:103]
ds_read_b64_tr_b16 v[66:67], v20 offset:1792
v_mfma_f32_16x16x32_bf16 v[104:107], v[44:47], a[200:203], v[104:107]
ds_read_b64_tr_b16 v[68:69], v21 offset:1536
v_mfma_f32_16x16x32_bf16 v[108:111], v[44:47], a[204:207], v[108:111]
ds_read_b64_tr_b16 v[70:71], v21 offset:1792
v_mfma_f32_16x16x32_bf16 v[112:115], v[48:51], a[192:195], v[112:115]
s_add_u32 m0, s94, 16384
v_accvgpr_write_b32 a87, 0
v_mfma_f32_16x16x32_bf16 v[116:119], v[48:51], a[196:199], v[116:119]
buffer_load_dwordx4 v3, s[8:11], s92 offen offset:0 sc1 lds
v_mfma_f32_16x16x32_bf16 v[120:123], v[48:51], a[200:203], v[120:123]
v_accvgpr_write_b32 a88, 0
v_accvgpr_write_b32 a89, 0
v_mfma_f32_16x16x32_bf16 v[124:127], v[48:51], a[204:207], v[124:127]
buffer_load_dwordx4 v3, s[8:11], s92 offen offset:1024 sc1 lds
v_mfma_f32_16x16x32_bf16 v[128:131], v[52:55], a[192:195], v[128:131]
v_accvgpr_write_b32 a90, 0
v_accvgpr_write_b32 a91, 0
v_mfma_f32_16x16x32_bf16 v[132:135], v[52:55], a[196:199], v[132:135]
v_accvgpr_write_b32 a92, 0
v_accvgpr_write_b32 a93, 0
v_mfma_f32_16x16x32_bf16 v[136:139], v[52:55], a[200:203], v[136:139]
v_accvgpr_write_b32 a94, 0
v_accvgpr_write_b32 a95, 0
v_mfma_f32_16x16x32_bf16 v[140:143], v[52:55], a[204:207], v[140:143]
v_accvgpr_write_b32 a96, 0
v_accvgpr_write_b32 a97, 0
s_waitcnt lgkmcnt(0)
v_mfma_f32_16x16x32_bf16 v[144:147], v[56:59], a[192:195], v[144:147]
ds_read_b64_tr_b16 v[40:41], v22 offset:0
v_mfma_f32_16x16x32_bf16 v[148:151], v[56:59], a[196:199], v[148:151]
ds_read_b64_tr_b16 v[42:43], v22 offset:256
v_mfma_f32_16x16x32_bf16 v[152:155], v[56:59], a[200:203], v[152:155]
ds_read_b64_tr_b16 v[44:45], v23 offset:0
v_mfma_f32_16x16x32_bf16 v[156:159], v[56:59], a[204:207], v[156:159]
ds_read_b64_tr_b16 v[46:47], v23 offset:256
v_mfma_f32_16x16x32_bf16 v[160:163], v[60:63], a[192:195], v[160:163]
ds_read_b64_tr_b16 v[48:49], v22 offset:512
v_mfma_f32_16x16x32_bf16 v[164:167], v[60:63], a[196:199], v[164:167]
ds_read_b64_tr_b16 v[50:51], v22 offset:768
v_mfma_f32_16x16x32_bf16 v[168:171], v[60:63], a[200:203], v[168:171]
ds_read_b64_tr_b16 v[52:53], v23 offset:512
v_mfma_f32_16x16x32_bf16 v[172:175], v[60:63], a[204:207], v[172:175]
ds_read_b64_tr_b16 v[54:55], v23 offset:768
v_mfma_f32_16x16x32_bf16 v[176:179], v[64:67], a[192:195], v[176:179]
v_accvgpr_write_b32 a98, 0
v_accvgpr_write_b32 a99, 0
v_mfma_f32_16x16x32_bf16 v[180:183], v[64:67], a[196:199], v[180:183]
buffer_load_dwordx4 v3, s[8:11], s92 offen offset:2048 sc1 lds
v_mfma_f32_16x16x32_bf16 v[184:187], v[64:67], a[200:203], v[184:187]
v_accvgpr_write_b32 a100, 0
v_accvgpr_write_b32 a101, 0
v_mfma_f32_16x16x32_bf16 v[188:191], v[64:67], a[204:207], v[188:191]
buffer_load_dwordx4 v3, s[8:11], s92 offen offset:3072 sc1 lds
s_add_u32 s92, s92, 24576
s_cmp_eq_u32 s92, s52
s_cselect_b32 s92, s82, s92
s_cselect_b32 s52, -1, s52
s_xor_b32 s94, s94, 32768
v_mfma_f32_16x16x32_bf16 v[192:195], v[68:71], a[192:195], v[192:195]
v_accvgpr_write_b32 a102, 0
v_accvgpr_write_b32 a103, 0
v_mfma_f32_16x16x32_bf16 v[196:199], v[68:71], a[196:199], v[196:199]
v_accvgpr_write_b32 a104, 0
v_accvgpr_write_b32 a105, 0
v_mfma_f32_16x16x32_bf16 v[200:203], v[68:71], a[200:203], v[200:203]
)ASM"
R"ASM(
v_accvgpr_write_b32 a106, 0
v_accvgpr_write_b32 a107, 0
v_mfma_f32_16x16x32_bf16 v[204:207], v[68:71], a[204:207], v[204:207]
v_accvgpr_write_b32 a108, 0
v_accvgpr_write_b32 a109, 0
s_waitcnt lgkmcnt(0)
v_mfma_f32_16x16x32_bf16 v[208:211], v[40:43], a[192:195], v[208:211]
ds_read_b64_tr_b16 v[56:57], v20 offset:12288
v_mfma_f32_16x16x32_bf16 v[216:219], v[40:43], a[196:199], v[216:219]
ds_read_b64_tr_b16 v[58:59], v20 offset:12544
v_mfma_f32_16x16x32_bf16 v[224:227], v[40:43], a[200:203], v[224:227]
ds_read_b64_tr_b16 v[60:61], v21 offset:12288
v_mfma_f32_16x16x32_bf16 v[232:235], v[40:43], a[204:207], v[232:235]
ds_read_b64_tr_b16 v[62:63], v21 offset:12544
v_mfma_f32_16x16x32_bf16 v[212:215], v[44:47], a[192:195], v[212:215]
ds_read_b64_tr_b16 v[64:65], v20 offset:12800
v_mfma_f32_16x16x32_bf16 v[220:223], v[44:47], a[196:199], v[220:223]
ds_read_b64_tr_b16 v[66:67], v20 offset:13056
v_mfma_f32_16x16x32_bf16 v[228:231], v[44:47], a[200:203], v[228:231]
ds_read_b64_tr_b16 v[68:69], v21 offset:12800
v_mfma_f32_16x16x32_bf16 v[236:239], v[44:47], a[204:207], v[236:239]
ds_read_b64_tr_b16 v[70:71], v21 offset:13056
v_mfma_f32_16x16x32_bf16 v[240:243], v[48:51], a[192:195], v[240:243]
s_mov_b32 m0, s95
v_accvgpr_write_b32 a110, 0
v_mfma_f32_16x16x32_bf16 v[248:251], v[48:51], a[196:199], v[248:251]
buffer_load_dwordx4 v3, s[8:11], s93 offen offset:0 sc1 lds
v_mfma_f32_16x16x32_bf16 v[244:247], v[52:55], a[192:195], v[244:247]
v_accvgpr_write_b32 a111, 0
v_accvgpr_write_b32 a112, 0
v_mfma_f32_16x16x32_bf16 v[252:255], v[52:55], a[196:199], v[252:255]
buffer_load_dwordx4 v3, s[8:11], s93 offen offset:1024 sc1 lds
s_waitcnt vmcnt(28) lgkmcnt(0)
s_barrier
v_mfma_f32_16x16x32_bf16 v[80:83], v[56:59], a[208:211], v[80:83]
ds_read_b64_tr_b16 v[40:41], v20 offset:13312
v_mfma_f32_16x16x32_bf16 v[84:87], v[56:59], a[212:215], v[84:87]
ds_read_b64_tr_b16 v[42:43], v20 offset:13568
v_mfma_f32_16x16x32_bf16 v[88:91], v[56:59], a[216:219], v[88:91]
ds_read_b64_tr_b16 v[44:45], v21 offset:13312
v_mfma_f32_16x16x32_bf16 v[92:95], v[56:59], a[220:223], v[92:95]
ds_read_b64_tr_b16 v[46:47], v21 offset:13568
v_mfma_f32_16x16x32_bf16 v[96:99], v[60:63], a[208:211], v[96:99]
ds_read_b64_tr_b16 v[48:49], v20 offset:13824
v_mfma_f32_16x16x32_bf16 v[100:103], v[60:63], a[212:215], v[100:103]
ds_read_b64_tr_b16 v[50:51], v20 offset:14080
v_mfma_f32_16x16x32_bf16 v[104:107], v[60:63], a[216:219], v[104:107]
ds_read_b64_tr_b16 v[52:53], v21 offset:13824
v_mfma_f32_16x16x32_bf16 v[108:111], v[60:63], a[220:223], v[108:111]
ds_read_b64_tr_b16 v[54:55], v21 offset:14080
v_mfma_f32_16x16x32_bf16 v[112:115], v[64:67], a[208:211], v[112:115]
v_accvgpr_write_b32 a113, 0
v_accvgpr_write_b32 a114, 0
v_mfma_f32_16x16x32_bf16 v[116:119], v[64:67], a[212:215], v[116:119]
buffer_load_dwordx4 v3, s[8:11], s93 offen offset:2048 sc1 lds
v_mfma_f32_16x16x32_bf16 v[120:123], v[64:67], a[216:219], v[120:123]
v_accvgpr_write_b32 a115, 0
v_accvgpr_write_b32 a116, 0
v_mfma_f32_16x16x32_bf16 v[124:127], v[64:67], a[220:223], v[124:127]
buffer_load_dwordx4 v3, s[8:11], s93 offen offset:3072 sc1 lds
s_add_u32 s93, s93, 8192
v_mfma_f32_16x16x32_bf16 v[128:131], v[68:71], a[208:211], v[128:131]
v_accvgpr_write_b32 a117, 0
v_accvgpr_write_b32 a118, 0
v_mfma_f32_16x16x32_bf16 v[132:135], v[68:71], a[212:215], v[132:135]
v_accvgpr_write_b32 a119, 0
v_accvgpr_write_b32 a120, 0
v_mfma_f32_16x16x32_bf16 v[136:139], v[68:71], a[216:219], v[136:139]
v_accvgpr_write_b32 a121, 0
v_accvgpr_write_b32 a122, 0
v_mfma_f32_16x16x32_bf16 v[140:143], v[68:71], a[220:223], v[140:143]
v_accvgpr_write_b32 a123, 0
v_accvgpr_write_b32 a124, 0
s_waitcnt lgkmcnt(0)
v_mfma_f32_16x16x32_bf16 v[144:147], v[40:43], a[208:211], v[144:147]
ds_read_b64_tr_b16 v[56:57], v22 offset:12288
v_mfma_f32_16x16x32_bf16 v[148:151], v[40:43], a[212:215], v[148:151]
ds_read_b64_tr_b16 v[58:59], v22 offset:12544
v_mfma_f32_16x16x32_bf16 v[152:155], v[40:43], a[216:219], v[152:155]
ds_read_b64_tr_b16 v[60:61], v23 offset:12288
v_mfma_f32_16x16x32_bf16 v[156:159], v[40:43], a[220:223], v[156:159]
ds_read_b64_tr_b16 v[62:63], v23 offset:12544
v_mfma_f32_16x16x32_bf16 v[160:163], v[44:47], a[208:211], v[160:163]
ds_read_b64_tr_b16 v[64:65], v22 offset:12800
v_mfma_f32_16x16x32_bf16 v[164:167], v[44:47], a[212:215], v[164:167]
ds_read_b64_tr_b16 v[66:67], v22 offset:13056
v_mfma_f32_16x16x32_bf16 v[168:171], v[44:47], a[216:219], v[168:171]
ds_read_b64_tr_b16 v[68:69], v23 offset:12800
v_mfma_f32_16x16x32_bf16 v[172:175], v[44:47], a[220:223], v[172:175]
ds_read_b64_tr_b16 v[70:71], v23 offset:13056
v_mfma_f32_16x16x32_bf16 v[176:179], v[48:51], a[208:211], v[176:179]
s_add_u32 m0, s95, 16384
v_accvgpr_write_b32 a125, 0
v_mfma_f32_16x16x32_bf16 v[180:183], v[48:51], a[212:215], v[180:183]
buffer_load_dwordx4 v3, s[8:11], s93 offen offset:0 sc1 lds
v_mfma_f32_16x16x32_bf16 v[184:187], v[48:51], a[216:219], v[184:187]
v_accvgpr_write_b32 a126, 0
v_accvgpr_write_b32 a127, 0
v_mfma_f32_16x16x32_bf16 v[188:191], v[48:51], a[220:223], v[188:191]
buffer_load_dwordx4 v3, s[8:11], s93 offen offset:1024 sc1 lds
v_mfma_f32_16x16x32_bf16 v[192:195], v[52:55], a[208:211], v[192:195]
v_mfma_f32_16x16x32_bf16 v[196:199], v[52:55], a[212:215], v[196:199]
v_mfma_f32_16x16x32_bf16 v[200:203], v[52:55], a[216:219], v[200:203]
v_mfma_f32_16x16x32_bf16 v[204:207], v[52:55], a[220:223], v[204:207]
s_waitcnt lgkmcnt(0)
v_mfma_f32_16x16x32_bf16 v[208:211], v[56:59], a[208:211], v[208:211]
ds_read_b64_tr_b16 v[40:41], v20 offset:24576
v_mfma_f32_16x16x32_bf16 v[216:219], v[56:59], a[212:215], v[216:219]
ds_read_b64_tr_b16 v[42:43], v20 offset:24832
v_mfma_f32_16x16x32_bf16 v[224:227], v[56:59], a[216:219], v[224:227]
ds_read_b64_tr_b16 v[44:45], v21 offset:24576
v_mfma_f32_16x16x32_bf16 v[232:235], v[56:59], a[220:223], v[232:235]
ds_read_b64_tr_b16 v[46:47], v21 offset:24832
v_mfma_f32_16x16x32_bf16 v[212:215], v[60:63], a[208:211], v[212:215]
ds_read_b64_tr_b16 v[48:49], v20 offset:25088
v_mfma_f32_16x16x32_bf16 v[220:223], v[60:63], a[212:215], v[220:223]
ds_read_b64_tr_b16 v[50:51], v20 offset:25344
v_mfma_f32_16x16x32_bf16 v[228:231], v[60:63], a[216:219], v[228:231]
ds_read_b64_tr_b16 v[52:53], v21 offset:25088
v_mfma_f32_16x16x32_bf16 v[236:239], v[60:63], a[220:223], v[236:239]
ds_read_b64_tr_b16 v[54:55], v21 offset:25344
v_mfma_f32_16x16x32_bf16 v[240:243], v[64:67], a[208:211], v[240:243]
v_mfma_f32_16x16x32_bf16 v[248:251], v[64:67], a[212:215], v[248:251]
buffer_load_dwordx4 v3, s[8:11], s93 offen offset:2048 sc1 lds
v_mfma_f32_16x16x32_bf16 v[244:247], v[68:71], a[208:211], v[244:247]
v_mfma_f32_16x16x32_bf16 v[252:255], v[68:71], a[212:215], v[252:255]
buffer_load_dwordx4 v3, s[8:11], s93 offen offset:3072 sc1 lds
s_add_u32 s93, s93, 24576
s_cmp_eq_u32 s93, s53
s_cselect_b32 s93, s14, s93
s_cselect_b32 s53, -1, s53
s_xor_b32 s95, s95, 32768
s_waitcnt vmcnt(26) lgkmcnt(0)
s_barrier
v_mfma_f32_16x16x32_bf16 v[80:83], v[40:43], a[224:227], v[80:83]
ds_read_b64_tr_b16 v[56:57], v20 offset:25600
v_mfma_f32_16x16x32_bf16 v[84:87], v[40:43], a[228:231], v[84:87]
ds_read_b64_tr_b16 v[58:59], v20 offset:25856
v_mfma_f32_16x16x32_bf16 v[88:91], v[40:43], a[232:235], v[88:91]
ds_read_b64_tr_b16 v[60:61], v21 offset:25600
v_mfma_f32_16x16x32_bf16 v[92:95], v[40:43], a[236:239], v[92:95]
ds_read_b64_tr_b16 v[62:63], v21 offset:25856
v_mfma_f32_16x16x32_bf16 v[96:99], v[44:47], a[224:227], v[96:99]
ds_read_b64_tr_b16 v[64:65], v20 offset:26112
v_mfma_f32_16x16x32_bf16 v[100:103], v[44:47], a[228:231], v[100:103]
ds_read_b64_tr_b16 v[66:67], v20 offset:26368
v_mfma_f32_16x16x32_bf16 v[104:107], v[44:47], a[232:235], v[104:107]
ds_read_b64_tr_b16 v[68:69], v21 offset:26112
v_mfma_f32_16x16x32_bf16 v[108:111], v[44:47], a[236:239], v[108:111]
ds_read_b64_tr_b16 v[70:71], v21 offset:26368
v_mfma_f32_16x16x32_bf16 v[112:115], v[48:51], a[224:227], v[112:115]
v_mfma_f32_16x16x32_bf16 v[116:119], v[48:51], a[228:231], v[116:119]
v_mfma_f32_16x16x32_bf16 v[120:123], v[48:51], a[232:235], v[120:123]
v_mfma_f32_16x16x32_bf16 v[124:127], v[48:51], a[236:239], v[124:127]
v_mfma_f32_16x16x32_bf16 v[128:131], v[52:55], a[224:227], v[128:131]
v_mfma_f32_16x16x32_bf16 v[132:135], v[52:55], a[228:231], v[132:135]
v_mfma_f32_16x16x32_bf16 v[136:139], v[52:55], a[232:235], v[136:139]
v_mfma_f32_16x16x32_bf16 v[140:143], v[52:55], a[236:239], v[140:143]
s_waitcnt lgkmcnt(0)
v_mfma_f32_16x16x32_bf16 v[144:147], v[56:59], a[224:227], v[144:147]
ds_read_b64_tr_b16 v[40:41], v22 offset:24576
v_mfma_f32_16x16x32_bf16 v[148:151], v[56:59], a[228:231], v[148:151]
ds_read_b64_tr_b16 v[42:43], v22 offset:24832
v_mfma_f32_16x16x32_bf16 v[152:155], v[56:59], a[232:235], v[152:155]
ds_read_b64_tr_b16 v[44:45], v23 offset:24576
v_mfma_f32_16x16x32_bf16 v[156:159], v[56:59], a[236:239], v[156:159]
ds_read_b64_tr_b16 v[46:47], v23 offset:24832
v_mfma_f32_16x16x32_bf16 v[160:163], v[60:63], a[224:227], v[160:163]
ds_read_b64_tr_b16 v[48:49], v22 offset:25088
v_mfma_f32_16x16x32_bf16 v[164:167], v[60:63], a[228:231], v[164:167]
ds_read_b64_tr_b16 v[50:51], v22 offset:25344
v_mfma_f32_16x16x32_bf16 v[168:171], v[60:63], a[232:235], v[168:171]
ds_read_b64_tr_b16 v[52:53], v23 offset:25088
v_mfma_f32_16x16x32_bf16 v[172:175], v[60:63], a[236:239], v[172:175]
ds_read_b64_tr_b16 v[54:55], v23 offset:25344
v_mfma_f32_16x16x32_bf16 v[176:179], v[64:67], a[224:227], v[176:179]
v_mfma_f32_16x16x32_bf16 v[180:183], v[64:67], a[228:231], v[180:183]
v_mfma_f32_16x16x32_bf16 v[184:187], v[64:67], a[232:235], v[184:187]
v_mfma_f32_16x16x32_bf16 v[188:191], v[64:67], a[236:239], v[188:191]
v_mfma_f32_16x16x32_bf16 v[192:195], v[68:71], a[224:227], v[192:195]
v_mfma_f32_16x16x32_bf16 v[196:199], v[68:71], a[228:231], v[196:199]
v_mfma_f32_16x16x32_bf16 v[200:203], v[68:71], a[232:235], v[200:203]
v_mfma_f32_16x16x32_bf16 v[204:207], v[68:71], a[236:239], v[204:207]
s_waitcnt lgkmcnt(0)
v_mfma_f32_16x16x32_bf16 v[208:211], v[40:43], a[224:227], v[208:211]
ds_read_b64_tr_b16 v[56:57], v20 offset:36864
v_mfma_f32_16x16x32_bf16 v[216:219], v[40:43], a[228:231], v[216:219]
ds_read_b64_tr_b16 v[58:59], v20 offset:37120
v_mfma_f32_16x16x32_bf16 v[224:227], v[40:43], a[232:235], v[224:227]
ds_read_b64_tr_b16 v[60:61], v21 offset:36864
v_mfma_f32_16x16x32_bf16 v[232:235], v[40:43], a[236:239], v[232:235]
ds_read_b64_tr_b16 v[62:63], v21 offset:37120
v_mfma_f32_16x16x32_bf16 v[212:215], v[44:47], a[224:227], v[212:215]
ds_read_b64_tr_b16 v[64:65], v20 offset:37376
v_mfma_f32_16x16x32_bf16 v[220:223], v[44:47], a[228:231], v[220:223]
ds_read_b64_tr_b16 v[66:67], v20 offset:37632
v_mfma_f32_16x16x32_bf16 v[228:231], v[44:47], a[232:235], v[228:231]
ds_read_b64_tr_b16 v[68:69], v21 offset:37376
v_mfma_f32_16x16x32_bf16 v[236:239], v[44:47], a[236:239], v[236:239]
ds_read_b64_tr_b16 v[70:71], v21 offset:37632
)ASM"
R"ASM(
v_mfma_f32_16x16x32_bf16 v[240:243], v[48:51], a[224:227], v[240:243]
v_mfma_f32_16x16x32_bf16 v[248:251], v[48:51], a[228:231], v[248:251]
v_mfma_f32_16x16x32_bf16 v[244:247], v[52:55], a[224:227], v[244:247]
v_mfma_f32_16x16x32_bf16 v[252:255], v[52:55], a[228:231], v[252:255]
s_waitcnt vmcnt(26) lgkmcnt(0)
s_barrier
v_mfma_f32_16x16x32_bf16 v[80:83], v[56:59], a[240:243], v[80:83]
ds_read_b64_tr_b16 v[40:41], v20 offset:37888
v_mfma_f32_16x16x32_bf16 v[84:87], v[56:59], a[244:247], v[84:87]
ds_read_b64_tr_b16 v[42:43], v20 offset:38144
v_mfma_f32_16x16x32_bf16 v[88:91], v[56:59], a[248:251], v[88:91]
ds_read_b64_tr_b16 v[44:45], v21 offset:37888
v_mfma_f32_16x16x32_bf16 v[92:95], v[56:59], a[252:255], v[92:95]
ds_read_b64_tr_b16 v[46:47], v21 offset:38144
v_mfma_f32_16x16x32_bf16 v[96:99], v[60:63], a[240:243], v[96:99]
ds_read_b64_tr_b16 v[48:49], v20 offset:38400
v_mfma_f32_16x16x32_bf16 v[100:103], v[60:63], a[244:247], v[100:103]
ds_read_b64_tr_b16 v[50:51], v20 offset:38656
v_mfma_f32_16x16x32_bf16 v[104:107], v[60:63], a[248:251], v[104:107]
ds_read_b64_tr_b16 v[52:53], v21 offset:38400
v_mfma_f32_16x16x32_bf16 v[108:111], v[60:63], a[252:255], v[108:111]
ds_read_b64_tr_b16 v[54:55], v21 offset:38656
v_mfma_f32_16x16x32_bf16 v[112:115], v[64:67], a[240:243], v[112:115]
v_mfma_f32_16x16x32_bf16 v[116:119], v[64:67], a[244:247], v[116:119]
v_mfma_f32_16x16x32_bf16 v[120:123], v[64:67], a[248:251], v[120:123]
v_mfma_f32_16x16x32_bf16 v[124:127], v[64:67], a[252:255], v[124:127]
v_mfma_f32_16x16x32_bf16 v[128:131], v[68:71], a[240:243], v[128:131]
v_mfma_f32_16x16x32_bf16 v[132:135], v[68:71], a[244:247], v[132:135]
v_mfma_f32_16x16x32_bf16 v[136:139], v[68:71], a[248:251], v[136:139]
v_mfma_f32_16x16x32_bf16 v[140:143], v[68:71], a[252:255], v[140:143]
s_waitcnt lgkmcnt(0)
v_mfma_f32_16x16x32_bf16 v[144:147], v[40:43], a[240:243], v[144:147]
ds_read_b64_tr_b16 v[56:57], v22 offset:36864
v_mfma_f32_16x16x32_bf16 v[148:151], v[40:43], a[244:247], v[148:151]
ds_read_b64_tr_b16 v[58:59], v22 offset:37120
v_mfma_f32_16x16x32_bf16 v[152:155], v[40:43], a[248:251], v[152:155]
ds_read_b64_tr_b16 v[60:61], v23 offset:36864
v_mfma_f32_16x16x32_bf16 v[156:159], v[40:43], a[252:255], v[156:159]
ds_read_b64_tr_b16 v[62:63], v23 offset:37120
v_mfma_f32_16x16x32_bf16 v[160:163], v[44:47], a[240:243], v[160:163]
ds_read_b64_tr_b16 v[64:65], v22 offset:37376
v_mfma_f32_16x16x32_bf16 v[164:167], v[44:47], a[244:247], v[164:167]
ds_read_b64_tr_b16 v[66:67], v22 offset:37632
v_mfma_f32_16x16x32_bf16 v[168:171], v[44:47], a[248:251], v[168:171]
ds_read_b64_tr_b16 v[68:69], v23 offset:37376
v_mfma_f32_16x16x32_bf16 v[172:175], v[44:47], a[252:255], v[172:175]
ds_read_b64_tr_b16 v[70:71], v23 offset:37632
v_mfma_f32_16x16x32_bf16 v[176:179], v[48:51], a[240:243], v[176:179]
v_mfma_f32_16x16x32_bf16 v[180:183], v[48:51], a[244:247], v[180:183]
v_mfma_f32_16x16x32_bf16 v[184:187], v[48:51], a[248:251], v[184:187]
v_mfma_f32_16x16x32_bf16 v[188:191], v[48:51], a[252:255], v[188:191]
v_mfma_f32_16x16x32_bf16 v[192:195], v[52:55], a[240:243], v[192:195]
v_mfma_f32_16x16x32_bf16 v[196:199], v[52:55], a[244:247], v[196:199]
v_mfma_f32_16x16x32_bf16 v[200:203], v[52:55], a[248:251], v[200:203]
v_mfma_f32_16x16x32_bf16 v[204:207], v[52:55], a[252:255], v[204:207]
s_waitcnt lgkmcnt(0)
s_barrier
v_mfma_f32_16x16x32_bf16 v[208:211], v[56:59], a[240:243], v[208:211]
v_mfma_f32_16x16x32_bf16 v[216:219], v[56:59], a[244:247], v[216:219]
v_mfma_f32_16x16x32_bf16 v[224:227], v[56:59], a[248:251], v[224:227]
v_mfma_f32_16x16x32_bf16 v[232:235], v[56:59], a[252:255], v[232:235]
v_mfma_f32_16x16x32_bf16 v[212:215], v[60:63], a[240:243], v[212:215]
v_mfma_f32_16x16x32_bf16 v[220:223], v[60:63], a[244:247], v[220:223]
v_mfma_f32_16x16x32_bf16 v[228:231], v[60:63], a[248:251], v[228:231]
v_mfma_f32_16x16x32_bf16 v[236:239], v[60:63], a[252:255], v[236:239]
v_mfma_f32_16x16x32_bf16 v[240:243], v[64:67], a[240:243], v[240:243]
v_mfma_f32_16x16x32_bf16 v[248:251], v[64:67], a[244:247], v[248:251]
v_mfma_f32_16x16x32_bf16 v[244:247], v[68:71], a[240:243], v[244:247]
v_mfma_f32_16x16x32_bf16 v[252:255], v[68:71], a[244:247], v[252:255]
s_mov_b32 s98, 0xbdd2d3e8
v_mov_b32 v36, 0xc0135761
v_cvt_pk_bf16_f32 v80, v80, v81
v_cvt_pk_bf16_f32 v81, v82, v83
v_cvt_pk_bf16_f32 v82, v96, v97
v_cvt_pk_bf16_f32 v83, v98, v99
v_cvt_pk_bf16_f32 v112, v112, v113
v_cvt_pk_bf16_f32 v113, v114, v115
v_cvt_pk_bf16_f32 v114, v128, v129
v_cvt_pk_bf16_f32 v115, v130, v131
v_cvt_pk_bf16_f32 v144, v144, v145
v_cvt_pk_bf16_f32 v145, v146, v147
v_cvt_pk_bf16_f32 v146, v160, v161
v_cvt_pk_bf16_f32 v147, v162, v163
v_cvt_pk_bf16_f32 v176, v176, v177
v_cvt_pk_bf16_f32 v177, v178, v179
v_cvt_pk_bf16_f32 v178, v192, v193
v_cvt_pk_bf16_f32 v179, v194, v195
v_cvt_pk_bf16_f32 v84, v84, v85
v_cvt_pk_bf16_f32 v85, v86, v87
v_cvt_pk_bf16_f32 v86, v100, v101
v_cvt_pk_bf16_f32 v87, v102, v103
v_cvt_pk_bf16_f32 v116, v116, v117
v_cvt_pk_bf16_f32 v117, v118, v119
v_cvt_pk_bf16_f32 v118, v132, v133
v_cvt_pk_bf16_f32 v119, v134, v135
v_cvt_pk_bf16_f32 v148, v148, v149
v_cvt_pk_bf16_f32 v149, v150, v151
v_cvt_pk_bf16_f32 v150, v164, v165
v_cvt_pk_bf16_f32 v151, v166, v167
v_cvt_pk_bf16_f32 v180, v180, v181
v_cvt_pk_bf16_f32 v181, v182, v183
v_cvt_pk_bf16_f32 v182, v196, v197
v_cvt_pk_bf16_f32 v183, v198, v199
v_cvt_pk_bf16_f32 v88, v88, v89
v_cvt_pk_bf16_f32 v89, v90, v91
v_cvt_pk_bf16_f32 v90, v104, v105
v_cvt_pk_bf16_f32 v91, v106, v107
v_cvt_pk_bf16_f32 v120, v120, v121
v_cvt_pk_bf16_f32 v121, v122, v123
v_cvt_pk_bf16_f32 v122, v136, v137
v_cvt_pk_bf16_f32 v123, v138, v139
v_cvt_pk_bf16_f32 v152, v152, v153
v_cvt_pk_bf16_f32 v153, v154, v155
v_cvt_pk_bf16_f32 v154, v168, v169
v_cvt_pk_bf16_f32 v155, v170, v171
v_cvt_pk_bf16_f32 v184, v184, v185
v_cvt_pk_bf16_f32 v185, v186, v187
v_cvt_pk_bf16_f32 v186, v200, v201
v_cvt_pk_bf16_f32 v187, v202, v203
v_cvt_pk_bf16_f32 v92, v92, v93
v_cvt_pk_bf16_f32 v93, v94, v95
v_cvt_pk_bf16_f32 v94, v108, v109
v_cvt_pk_bf16_f32 v95, v110, v111
v_cvt_pk_bf16_f32 v124, v124, v125
v_cvt_pk_bf16_f32 v125, v126, v127
v_cvt_pk_bf16_f32 v126, v140, v141
v_cvt_pk_bf16_f32 v127, v142, v143
v_cvt_pk_bf16_f32 v156, v156, v157
v_cvt_pk_bf16_f32 v157, v158, v159
v_cvt_pk_bf16_f32 v158, v172, v173
v_cvt_pk_bf16_f32 v159, v174, v175
v_cvt_pk_bf16_f32 v188, v188, v189
v_cvt_pk_bf16_f32 v189, v190, v191
v_cvt_pk_bf16_f32 v190, v204, v205
v_cvt_pk_bf16_f32 v191, v206, v207
ds_write_b128 v14, v[80:83] offset:0
v_lshlrev_b32 v40, 16, v28
v_and_b32 v41, 0xffff0000, v28
v_lshlrev_b32 v42, 16, v29
v_and_b32 v43, 0xffff0000, v29
v_lshlrev_b32 v44, 16, v30
v_and_b32 v45, 0xffff0000, v30
v_lshlrev_b32 v46, 16, v31
v_and_b32 v47, 0xffff0000, v31
v_lshlrev_b32 v48, 16, v32
v_and_b32 v49, 0xffff0000, v32
ds_write_b128 v14, v[112:115] offset:1024
v_lshlrev_b32 v50, 16, v33
v_and_b32 v51, 0xffff0000, v33
v_lshlrev_b32 v52, 16, v34
v_and_b32 v53, 0xffff0000, v34
v_lshlrev_b32 v54, 16, v35
v_and_b32 v55, 0xffff0000, v35
v_pk_fma_f32 v[208:209], v[208:209], v[72:73], v[40:41] op_sel_hi:[1,0,1]
v_pk_fma_f32 v[210:211], v[210:211], v[72:73], v[42:43] op_sel_hi:[1,0,1]
v_pk_fma_f32 v[212:213], v[212:213], v[72:73], v[44:45] op_sel_hi:[1,0,1]
v_pk_fma_f32 v[214:215], v[214:215], v[72:73], v[46:47] op_sel_hi:[1,0,1]
ds_write_b128 v14, v[144:147] offset:2048
v_pk_fma_f32 v[216:217], v[216:217], v[72:73], v[40:41] op_sel:[0,1,0] op_sel_hi:[1,1,1]
v_pk_fma_f32 v[218:219], v[218:219], v[72:73], v[42:43] op_sel:[0,1,0] op_sel_hi:[1,1,1]
v_pk_fma_f32 v[220:221], v[220:221], v[72:73], v[44:45] op_sel:[0,1,0] op_sel_hi:[1,1,1]
v_pk_fma_f32 v[222:223], v[222:223], v[72:73], v[46:47] op_sel:[0,1,0] op_sel_hi:[1,1,1]
v_pk_mul_f32 v[56:57], v[208:209], v[208:209]
v_pk_mul_f32 v[58:59], v[210:211], v[210:211]
v_pk_mul_f32 v[60:61], v[212:213], v[212:213]
v_pk_mul_f32 v[62:63], v[214:215], v[214:215]
v_pk_mul_f32 v[64:65], v[216:217], v[216:217]
v_pk_mul_f32 v[66:67], v[218:219], v[218:219]
v_pk_mul_f32 v[68:69], v[220:221], v[220:221]
ds_write_b128 v14, v[176:179] offset:3072
v_pk_mul_f32 v[70:71], v[222:223], v[222:223]
v_pk_fma_f32 v[56:57], v[56:57], s[98:99], v[36:37] op_sel_hi:[1,0,0]
v_pk_fma_f32 v[58:59], v[58:59], s[98:99], v[36:37] op_sel_hi:[1,0,0]
v_pk_fma_f32 v[60:61], v[60:61], s[98:99], v[36:37] op_sel_hi:[1,0,0]
v_pk_fma_f32 v[62:63], v[62:63], s[98:99], v[36:37] op_sel_hi:[1,0,0]
v_pk_fma_f32 v[64:65], v[64:65], s[98:99], v[36:37] op_sel_hi:[1,0,0]
v_pk_fma_f32 v[66:67], v[66:67], s[98:99], v[36:37] op_sel_hi:[1,0,0]
v_pk_fma_f32 v[68:69], v[68:69], s[98:99], v[36:37] op_sel_hi:[1,0,0]
v_pk_fma_f32 v[70:71], v[70:71], s[98:99], v[36:37] op_sel_hi:[1,0,0]
v_pk_mul_f32 v[56:57], v[56:57], v[208:209]
ds_write_b128 v14, v[84:87] offset:4096
v_pk_mul_f32 v[58:59], v[58:59], v[210:211]
v_pk_mul_f32 v[60:61], v[60:61], v[212:213]
v_pk_mul_f32 v[62:63], v[62:63], v[214:215]
v_pk_mul_f32 v[64:65], v[64:65], v[216:217]
v_pk_mul_f32 v[66:67], v[66:67], v[218:219]
v_pk_mul_f32 v[68:69], v[68:69], v[220:221]
v_pk_mul_f32 v[70:71], v[70:71], v[222:223]
v_exp_f32 v56, v56
v_exp_f32 v57, v57
v_exp_f32 v58, v58
v_exp_f32 v59, v59
ds_write_b128 v14, v[116:119] offset:5120
v_exp_f32 v60, v60
v_exp_f32 v61, v61
v_exp_f32 v62, v62
v_exp_f32 v63, v63
v_exp_f32 v64, v64
v_exp_f32 v65, v65
v_exp_f32 v66, v66
)ASM"
R"ASM(
v_exp_f32 v67, v67
v_exp_f32 v68, v68
v_exp_f32 v69, v69
ds_write_b128 v14, v[148:151] offset:6144
v_exp_f32 v70, v70
v_exp_f32 v71, v71
v_pk_add_f32 v[56:57], v[56:57], 1.0 op_sel_hi:[1,0]
v_pk_add_f32 v[58:59], v[58:59], 1.0 op_sel_hi:[1,0]
v_pk_add_f32 v[60:61], v[60:61], 1.0 op_sel_hi:[1,0]
v_pk_add_f32 v[62:63], v[62:63], 1.0 op_sel_hi:[1,0]
v_pk_add_f32 v[64:65], v[64:65], 1.0 op_sel_hi:[1,0]
v_pk_add_f32 v[66:67], v[66:67], 1.0 op_sel_hi:[1,0]
v_pk_add_f32 v[68:69], v[68:69], 1.0 op_sel_hi:[1,0]
v_pk_add_f32 v[70:71], v[70:71], 1.0 op_sel_hi:[1,0]
v_rcp_f32 v56, v56
ds_write_b128 v14, v[180:183] offset:7168
v_rcp_f32 v57, v57
v_rcp_f32 v58, v58
v_rcp_f32 v59, v59
v_rcp_f32 v60, v60
v_rcp_f32 v61, v61
v_rcp_f32 v62, v62
v_rcp_f32 v63, v63
v_rcp_f32 v64, v64
v_rcp_f32 v65, v65
v_rcp_f32 v66, v66
ds_write_b128 v14, v[88:91] offset:8192
v_rcp_f32 v67, v67
v_rcp_f32 v68, v68
v_rcp_f32 v69, v69
v_rcp_f32 v70, v70
v_rcp_f32 v71, v71
v_pk_mul_f32 v[208:209], v[208:209], v[56:57]
v_pk_mul_f32 v[210:211], v[210:211], v[58:59]
v_pk_mul_f32 v[212:213], v[212:213], v[60:61]
v_pk_mul_f32 v[214:215], v[214:215], v[62:63]
v_pk_mul_f32 v[216:217], v[216:217], v[64:65]
ds_write_b128 v14, v[120:123] offset:9216
v_pk_mul_f32 v[218:219], v[218:219], v[66:67]
v_pk_mul_f32 v[220:221], v[220:221], v[68:69]
v_pk_mul_f32 v[222:223], v[222:223], v[70:71]
v_cvt_pk_bf16_f32 v208, v208, v209
v_cvt_pk_bf16_f32 v209, v210, v211
v_cvt_pk_bf16_f32 v210, v212, v213
v_cvt_pk_bf16_f32 v211, v214, v215
v_cvt_pk_bf16_f32 v216, v216, v217
v_cvt_pk_bf16_f32 v217, v218, v219
v_cvt_pk_bf16_f32 v218, v220, v221
v_cvt_pk_bf16_f32 v219, v222, v223
ds_write_b128 v14, v[152:155] offset:10240
buffer_store_dwordx4 v[208:211], v2, s[8:11], s58 offen sc1
s_add_u32 s58, s58, 175104
buffer_store_dwordx4 v[216:219], v2, s[8:11], s58 offen sc1
v_pk_fma_f32 v[224:225], v[224:225], v[74:75], v[40:41] op_sel_hi:[1,0,1]
v_pk_fma_f32 v[226:227], v[226:227], v[74:75], v[42:43] op_sel_hi:[1,0,1]
v_pk_fma_f32 v[228:229], v[228:229], v[74:75], v[44:45] op_sel_hi:[1,0,1]
v_pk_fma_f32 v[230:231], v[230:231], v[74:75], v[46:47] op_sel_hi:[1,0,1]
v_pk_fma_f32 v[232:233], v[232:233], v[74:75], v[40:41] op_sel:[0,1,0] op_sel_hi:[1,1,1]
v_pk_fma_f32 v[234:235], v[234:235], v[74:75], v[42:43] op_sel:[0,1,0] op_sel_hi:[1,1,1]
v_pk_fma_f32 v[236:237], v[236:237], v[74:75], v[44:45] op_sel:[0,1,0] op_sel_hi:[1,1,1]
ds_write_b128 v14, v[184:187] offset:11264
v_pk_fma_f32 v[238:239], v[238:239], v[74:75], v[46:47] op_sel:[0,1,0] op_sel_hi:[1,1,1]
v_pk_mul_f32 v[56:57], v[224:225], v[224:225]
v_pk_mul_f32 v[58:59], v[226:227], v[226:227]
v_pk_mul_f32 v[60:61], v[228:229], v[228:229]
v_pk_mul_f32 v[62:63], v[230:231], v[230:231]
v_pk_mul_f32 v[64:65], v[232:233], v[232:233]
v_pk_mul_f32 v[66:67], v[234:235], v[234:235]
v_pk_mul_f32 v[68:69], v[236:237], v[236:237]
v_pk_mul_f32 v[70:71], v[238:239], v[238:239]
v_pk_fma_f32 v[56:57], v[56:57], s[98:99], v[36:37] op_sel_hi:[1,0,0]
v_pk_fma_f32 v[58:59], v[58:59], s[98:99], v[36:37] op_sel_hi:[1,0,0]
ds_write_b128 v14, v[92:95] offset:12288
v_pk_fma_f32 v[60:61], v[60:61], s[98:99], v[36:37] op_sel_hi:[1,0,0]
v_pk_fma_f32 v[62:63], v[62:63], s[98:99], v[36:37] op_sel_hi:[1,0,0]
v_pk_fma_f32 v[64:65], v[64:65], s[98:99], v[36:37] op_sel_hi:[1,0,0]
v_pk_fma_f32 v[66:67], v[66:67], s[98:99], v[36:37] op_sel_hi:[1,0,0]
v_pk_fma_f32 v[68:69], v[68:69], s[98:99], v[36:37] op_sel_hi:[1,0,0]
v_pk_fma_f32 v[70:71], v[70:71], s[98:99], v[36:37] op_sel_hi:[1,0,0]
v_pk_mul_f32 v[56:57], v[56:57], v[224:225]
v_pk_mul_f32 v[58:59], v[58:59], v[226:227]
v_pk_mul_f32 v[60:61], v[60:61], v[228:229]
v_pk_mul_f32 v[62:63], v[62:63], v[230:231]
ds_write_b128 v14, v[124:127] offset:13312
v_pk_mul_f32 v[64:65], v[64:65], v[232:233]
v_pk_mul_f32 v[66:67], v[66:67], v[234:235]
v_pk_mul_f32 v[68:69], v[68:69], v[236:237]
v_pk_mul_f32 v[70:71], v[70:71], v[238:239]
v_exp_f32 v56, v56
v_exp_f32 v57, v57
v_exp_f32 v58, v58
v_exp_f32 v59, v59
v_exp_f32 v60, v60
v_exp_f32 v61, v61
v_exp_f32 v62, v62
ds_write_b128 v14, v[156:159] offset:14336
v_exp_f32 v63, v63
v_exp_f32 v64, v64
v_exp_f32 v65, v65
v_exp_f32 v66, v66
v_exp_f32 v67, v67
v_exp_f32 v68, v68
v_exp_f32 v69, v69
v_exp_f32 v70, v70
v_exp_f32 v71, v71
v_pk_add_f32 v[56:57], v[56:57], 1.0 op_sel_hi:[1,0]
ds_write_b128 v14, v[188:191] offset:15360
v_pk_add_f32 v[58:59], v[58:59], 1.0 op_sel_hi:[1,0]
v_pk_add_f32 v[60:61], v[60:61], 1.0 op_sel_hi:[1,0]
v_pk_add_f32 v[62:63], v[62:63], 1.0 op_sel_hi:[1,0]
v_pk_add_f32 v[64:65], v[64:65], 1.0 op_sel_hi:[1,0]
v_pk_add_f32 v[66:67], v[66:67], 1.0 op_sel_hi:[1,0]
v_pk_add_f32 v[68:69], v[68:69], 1.0 op_sel_hi:[1,0]
v_pk_add_f32 v[70:71], v[70:71], 1.0 op_sel_hi:[1,0]
v_rcp_f32 v56, v56
v_rcp_f32 v57, v57
v_rcp_f32 v58, v58
v_rcp_f32 v59, v59
s_waitcnt vmcnt(10) lgkmcnt(0)
s_barrier
v_and_b32 v37, 8192, v15
v_or_b32 v14, v14, v37
ds_read_b128 a[128:131], v14 offset:0
v_rcp_f32 v60, v60
v_rcp_f32 v61, v61
ds_read_b128 a[132:135], v14 offset:1024
v_rcp_f32 v62, v62
v_rcp_f32 v63, v63
ds_read_b128 a[136:139], v14 offset:2048
v_rcp_f32 v64, v64
v_rcp_f32 v65, v65
v_rcp_f32 v66, v66
ds_read_b128 a[140:143], v14 offset:3072
v_rcp_f32 v67, v67
v_rcp_f32 v68, v68
ds_read_b128 a[144:147], v14 offset:4096
v_rcp_f32 v69, v69
v_rcp_f32 v70, v70
v_rcp_f32 v71, v71
ds_read_b128 a[148:151], v14 offset:5120
v_pk_mul_f32 v[224:225], v[224:225], v[56:57]
v_pk_mul_f32 v[226:227], v[226:227], v[58:59]
ds_read_b128 a[152:155], v14 offset:6144
v_pk_mul_f32 v[228:229], v[228:229], v[60:61]
v_pk_mul_f32 v[230:231], v[230:231], v[62:63]
v_pk_mul_f32 v[232:233], v[232:233], v[64:65]
ds_read_b128 a[156:159], v14 offset:7168
v_pk_mul_f32 v[234:235], v[234:235], v[66:67]
v_pk_mul_f32 v[236:237], v[236:237], v[68:69]
ds_read_b128 a[160:163], v15 offset:0
v_pk_mul_f32 v[238:239], v[238:239], v[70:71]
v_cvt_pk_bf16_f32 v224, v224, v225
ds_read_b128 a[164:167], v15 offset:1024
v_cvt_pk_bf16_f32 v225, v226, v227
v_cvt_pk_bf16_f32 v226, v228, v229
v_cvt_pk_bf16_f32 v227, v230, v231
ds_read_b128 a[168:171], v15 offset:2048
v_cvt_pk_bf16_f32 v232, v232, v233
v_cvt_pk_bf16_f32 v233, v234, v235
ds_read_b128 a[172:175], v15 offset:3072
v_cvt_pk_bf16_f32 v234, v236, v237
v_cvt_pk_bf16_f32 v235, v238, v239
buffer_store_dwordx4 v[224:227], v2, s[8:11], s59 offen sc1
ds_read_b128 a[176:179], v15 offset:4096
s_add_u32 s59, s59, 175104
buffer_store_dwordx4 v[232:235], v2, s[8:11], s59 offen sc1
ds_read_b128 a[180:183], v15 offset:5120
v_pk_fma_f32 v[240:241], v[240:241], v[72:73], v[48:49] op_sel_hi:[1,0,1]
v_pk_fma_f32 v[242:243], v[242:243], v[72:73], v[50:51] op_sel_hi:[1,0,1]
v_pk_fma_f32 v[244:245], v[244:245], v[72:73], v[52:53] op_sel_hi:[1,0,1]
ds_read_b128 a[184:187], v15 offset:6144
v_pk_fma_f32 v[246:247], v[246:247], v[72:73], v[54:55] op_sel_hi:[1,0,1]
v_pk_fma_f32 v[248:249], v[248:249], v[72:73], v[48:49] op_sel:[0,1,0] op_sel_hi:[1,1,1]
ds_read_b128 a[188:191], v15 offset:7168
v_pk_fma_f32 v[250:251], v[250:251], v[72:73], v[50:51] op_sel:[0,1,0] op_sel_hi:[1,1,1]
v_pk_fma_f32 v[252:253], v[252:253], v[72:73], v[52:53] op_sel:[0,1,0] op_sel_hi:[1,1,1]
v_pk_fma_f32 v[254:255], v[254:255], v[72:73], v[54:55] op_sel:[0,1,0] op_sel_hi:[1,1,1]
v_xor_b32 v14, 8192, v14
v_pk_mul_f32 v[56:57], v[240:241], v[240:241]
v_pk_mul_f32 v[58:59], v[242:243], v[242:243]
v_xor_b32 v15, 8192, v15
v_pk_mul_f32 v[60:61], v[244:245], v[244:245]
v_pk_mul_f32 v[62:63], v[246:247], v[246:247]
ds_read_b128 v[144:147], v16 offset:0
v_pk_mul_f32 v[64:65], v[248:249], v[248:249]
v_pk_mul_f32 v[66:67], v[250:251], v[250:251]
v_pk_mul_f32 v[68:69], v[252:253], v[252:253]
ds_read_b128 v[148:151], v16 offset:1024
v_pk_mul_f32 v[70:71], v[254:255], v[254:255]
v_pk_fma_f32 v[56:57], v[56:57], s[98:99], v[36:37] op_sel_hi:[1,0,0]
ds_read_b128 v[152:155], v16 offset:2048
v_pk_fma_f32 v[58:59], v[58:59], s[98:99], v[36:37] op_sel_hi:[1,0,0]
v_pk_fma_f32 v[60:61], v[60:61], s[98:99], v[36:37] op_sel_hi:[1,0,0]
v_pk_fma_f32 v[62:63], v[62:63], s[98:99], v[36:37] op_sel_hi:[1,0,0]
ds_read_b128 v[156:159], v16 offset:3072
v_pk_fma_f32 v[64:65], v[64:65], s[98:99], v[36:37] op_sel_hi:[1,0,0]
v_pk_fma_f32 v[66:67], v[66:67], s[98:99], v[36:37] op_sel_hi:[1,0,0]
ds_read_b128 v[160:163], v16 offset:4096
v_pk_fma_f32 v[68:69], v[68:69], s[98:99], v[36:37] op_sel_hi:[1,0,0]
v_pk_fma_f32 v[70:71], v[70:71], s[98:99], v[36:37] op_sel_hi:[1,0,0]
)ASM"
R"ASM(
v_pk_mul_f32 v[56:57], v[56:57], v[240:241]
ds_read_b128 v[164:167], v16 offset:5120
v_pk_mul_f32 v[58:59], v[58:59], v[242:243]
v_pk_mul_f32 v[60:61], v[60:61], v[244:245]
ds_read_b128 v[168:171], v16 offset:6144
v_pk_mul_f32 v[62:63], v[62:63], v[246:247]
v_pk_mul_f32 v[64:65], v[64:65], v[248:249]
v_pk_mul_f32 v[66:67], v[66:67], v[250:251]
ds_read_b128 v[172:175], v16 offset:7168
v_pk_mul_f32 v[68:69], v[68:69], v[252:253]
v_pk_mul_f32 v[70:71], v[70:71], v[254:255]
ds_read_b128 v[176:179], v16 offset:16384
v_exp_f32 v56, v56
v_exp_f32 v57, v57
ds_read_b128 v[180:183], v16 offset:17408
v_exp_f32 v58, v58
v_exp_f32 v59, v59
v_exp_f32 v60, v60
ds_read_b128 v[184:187], v16 offset:18432
v_exp_f32 v61, v61
v_exp_f32 v62, v62
ds_read_b128 v[188:191], v16 offset:19456
v_exp_f32 v63, v63
v_exp_f32 v64, v64
v_exp_f32 v65, v65
ds_read_b128 v[192:195], v16 offset:20480
v_exp_f32 v66, v66
v_exp_f32 v67, v67
ds_read_b128 v[196:199], v16 offset:21504
v_exp_f32 v68, v68
v_exp_f32 v69, v69
v_exp_f32 v70, v70
ds_read_b128 v[200:203], v16 offset:22528
v_exp_f32 v71, v71
v_pk_add_f32 v[56:57], v[56:57], 1.0 op_sel_hi:[1,0]
ds_read_b128 v[204:207], v16 offset:23552
v_pk_add_f32 v[58:59], v[58:59], 1.0 op_sel_hi:[1,0]
v_pk_add_f32 v[60:61], v[60:61], 1.0 op_sel_hi:[1,0]
ds_read_b128 a[192:195], v14 offset:0
v_pk_add_f32 v[62:63], v[62:63], 1.0 op_sel_hi:[1,0]
v_pk_add_f32 v[64:65], v[64:65], 1.0 op_sel_hi:[1,0]
v_pk_add_f32 v[66:67], v[66:67], 1.0 op_sel_hi:[1,0]
ds_read_b128 a[196:199], v14 offset:1024
v_pk_add_f32 v[68:69], v[68:69], 1.0 op_sel_hi:[1,0]
v_pk_add_f32 v[70:71], v[70:71], 1.0 op_sel_hi:[1,0]
ds_read_b128 a[200:203], v14 offset:2048
v_rcp_f32 v56, v56
v_rcp_f32 v57, v57
v_rcp_f32 v58, v58
ds_read_b128 a[204:207], v14 offset:3072
v_rcp_f32 v59, v59
v_rcp_f32 v60, v60
ds_read_b128 a[208:211], v14 offset:4096
v_rcp_f32 v61, v61
v_rcp_f32 v62, v62
v_rcp_f32 v63, v63
ds_read_b128 a[212:215], v14 offset:5120
v_rcp_f32 v64, v64
v_rcp_f32 v65, v65
ds_read_b128 a[216:219], v14 offset:6144
v_rcp_f32 v66, v66
v_rcp_f32 v67, v67
v_rcp_f32 v68, v68
ds_read_b128 a[220:223], v14 offset:7168
v_rcp_f32 v69, v69
v_rcp_f32 v70, v70
ds_read_b128 a[224:227], v15 offset:0
v_rcp_f32 v71, v71
v_pk_mul_f32 v[240:241], v[240:241], v[56:57]
ds_read_b128 a[228:231], v15 offset:1024
v_pk_mul_f32 v[242:243], v[242:243], v[58:59]
v_pk_mul_f32 v[244:245], v[244:245], v[60:61]
v_pk_mul_f32 v[246:247], v[246:247], v[62:63]
ds_read_b128 a[232:235], v15 offset:2048
v_pk_mul_f32 v[248:249], v[248:249], v[64:65]
v_pk_mul_f32 v[250:251], v[250:251], v[66:67]
ds_read_b128 a[236:239], v15 offset:3072
v_pk_mul_f32 v[252:253], v[252:253], v[68:69]
v_pk_mul_f32 v[254:255], v[254:255], v[70:71]
v_cvt_pk_bf16_f32 v240, v240, v241
ds_read_b128 a[240:243], v15 offset:4096
v_cvt_pk_bf16_f32 v241, v242, v243
v_cvt_pk_bf16_f32 v242, v244, v245
ds_read_b128 a[244:247], v15 offset:5120
v_cvt_pk_bf16_f32 v243, v246, v247
v_cvt_pk_bf16_f32 v248, v248, v249
v_cvt_pk_bf16_f32 v249, v250, v251
ds_read_b128 a[248:251], v15 offset:6144
v_cvt_pk_bf16_f32 v250, v252, v253
v_cvt_pk_bf16_f32 v251, v254, v255
ds_read_b128 a[252:255], v15 offset:7168
buffer_store_dwordx4 v[240:243], v2, s[8:11], s60 offen sc1
s_add_u32 s60, s60, 175104
buffer_store_dwordx4 v[248:251], v2, s[8:11], s60 offen sc1
s_nop 1
v_mov_b32 v240, v76
v_add_u32 v241, 174080, v76
v_mov_b32 v242, v77
v_add_u32 v243, 14336, v77
v_mov_b32 v244, v78
v_readlane_b32 s64, v79, 0
v_readlane_b32 s65, v79, 1
v_readlane_b32 s66, v79, 2
v_readlane_b32 s67, v79, 3
v_readlane_b32 s68, v79, 4
v_readlane_b32 s69, v79, 5
v_readlane_b32 s70, v79, 6
v_readlane_b32 s71, v79, 7
v_readlane_b32 s72, v79, 8
v_readlane_b32 s73, v79, 9
v_readlane_b32 s74, v79, 10
v_readlane_b32 s75, v79, 11
v_readlane_b32 s76, v79, 12
v_readlane_b32 s77, v79, 13
v_readlane_b32 s78, v79, 14
v_readlane_b32 s79, v79, 15
v_add_u32 v19, 65536, v16
v_xor_b32 v16, 32768, v16
v_xor_b32 v18, 8192, v17
v_mov_b32 v28, 0x17800000
v_cmp_gt_i32 s[16:17], 0, v13
v_cmp_gt_i32 s[18:19], 1, v13
v_cmp_gt_i32 s[20:21], 2, v13
v_cmp_gt_i32 s[22:23], 3, v13
v_mov_b32 v44, 0x3f803f80
v_mov_b32 v45, 0x3f803f80
v_mov_b32 v46, 0x3f803f80
v_mov_b32 v47, 0x3f803f80
v_cndmask_b32 v40, v28, 1.0, s[16:17]
v_cndmask_b32 v41, v28, 1.0, s[18:19]
v_cndmask_b32 v42, v28, 1.0, s[20:21]
v_cndmask_b32 v43, v28, 1.0, s[22:23]
v_mov_b32 v64, 0
v_mov_b32 v65, 0
v_mov_b32 v66, 0
v_mov_b32 v67, 0
v_mov_b32 v68, 0
v_mov_b32 v69, 0
v_mov_b32 v70, 0
v_mov_b32 v71, 0
v_mov_b32 v72, 0
v_mov_b32 v73, 0
v_mov_b32 v74, 0
v_mov_b32 v75, 0
v_mov_b32 v76, 0
v_mov_b32 v77, 0
v_mov_b32 v78, 0
v_mov_b32 v79, 0
s_cmp_eq_u32 s54, 0
s_cselect_b32 s16, s64, 0
s_cselect_b32 s17, s65, 0
s_cselect_b32 s18, s66, 0
s_cselect_b32 s19, s67, 0
s_cselect_b32 s20, s68, 0
s_cselect_b32 s21, s69, 0
s_cselect_b32 s22, s70, 0
s_cselect_b32 s23, s71, 0
s_waitcnt vmcnt(6) lgkmcnt(0)
s_barrier
v_mfma_f32_16x16x32_bf16 v[80:83], v[144:147], a[128:131], 0
v_mfma_f32_16x16x32_bf16 v[88:91], v[144:147], a[144:147], 0
s_mov_b32 m0, s95
v_mfma_f32_16x16x32_bf16 v[96:99], v[144:147], a[160:163], 0
buffer_load_dwordx4 v3, s[8:11], s93 offen offset:0 sc1 lds
v_mfma_f32_16x16x32_bf16 v[104:107], v[144:147], a[176:179], 0
v_mfma_f32_16x16x32_bf16 v[84:87], v[148:151], a[128:131], 0
v_mfma_f32_16x16x32_bf16 v[92:95], v[148:151], a[144:147], 0
buffer_load_dwordx4 v3, s[8:11], s93 offen offset:1024 sc1 lds
v_mfma_f32_16x16x32_bf16 v[100:103], v[148:151], a[160:163], 0
v_mfma_f32_16x16x32_bf16 v[108:111], v[148:151], a[176:179], 0
v_mfma_f32_16x16x32_bf16 v[80:83], v[152:155], a[132:135], v[80:83]
buffer_load_dwordx4 v3, s[8:11], s93 offen offset:2048 sc1 lds
v_mfma_f32_16x16x32_bf16 v[88:91], v[152:155], a[148:151], v[88:91]
v_mfma_f32_16x16x32_bf16 v[96:99], v[152:155], a[164:167], v[96:99]
v_mfma_f32_16x16x32_bf16 v[104:107], v[152:155], a[180:183], v[104:107]
buffer_load_dwordx4 v3, s[8:11], s93 offen offset:3072 sc1 lds
v_mfma_f32_16x16x32_bf16 v[84:87], v[156:159], a[132:135], v[84:87]
s_add_u32 s93, s93, 8192
v_mfma_f32_16x16x32_bf16 v[92:95], v[156:159], a[148:151], v[92:95]
v_mfma_f32_16x16x32_bf16 v[100:103], v[156:159], a[164:167], v[100:103]
s_add_u32 m0, s95, 16384
v_mfma_f32_16x16x32_bf16 v[108:111], v[156:159], a[180:183], v[108:111]
buffer_load_dwordx4 v3, s[8:11], s93 offen offset:0 sc1 lds
v_mfma_f32_16x16x32_bf16 v[80:83], v[160:163], a[136:139], v[80:83]
v_mfma_f32_16x16x32_bf16 v[88:91], v[160:163], a[152:155], v[88:91]
v_mfma_f32_16x16x32_bf16 v[96:99], v[160:163], a[168:171], v[96:99]
buffer_load_dwordx4 v3, s[8:11], s93 offen offset:1024 sc1 lds
v_mfma_f32_16x16x32_bf16 v[104:107], v[160:163], a[184:187], v[104:107]
v_mfma_f32_16x16x32_bf16 v[84:87], v[164:167], a[136:139], v[84:87]
v_mfma_f32_16x16x32_bf16 v[92:95], v[164:167], a[152:155], v[92:95]
buffer_load_dwordx4 v3, s[8:11], s93 offen offset:2048 sc1 lds
v_mfma_f32_16x16x32_bf16 v[100:103], v[164:167], a[168:171], v[100:103]
v_mfma_f32_16x16x32_bf16 v[108:111], v[164:167], a[184:187], v[108:111]
v_mfma_f32_16x16x32_bf16 v[80:83], v[168:171], a[140:143], v[80:83]
buffer_load_dwordx4 v3, s[8:11], s93 offen offset:3072 sc1 lds
v_mfma_f32_16x16x32_bf16 v[88:91], v[168:171], a[156:159], v[88:91]
s_add_u32 s93, s93, 24576
s_cmp_eq_u32 s93, s53
v_mfma_f32_16x16x32_bf16 v[96:99], v[168:171], a[172:175], v[96:99]
s_cselect_b32 s93, s14, s93
)ASM"
R"ASM(
s_cselect_b32 s53, -1, s53
v_mfma_f32_16x16x32_bf16 v[104:107], v[168:171], a[188:191], v[104:107]
s_xor_b32 s95, s95, 32768
v_mfma_f32_16x16x32_bf16 v[84:87], v[172:175], a[140:143], v[84:87]
v_mfma_f32_16x16x32_bf16 v[92:95], v[172:175], a[156:159], v[92:95]
v_mfma_f32_16x16x32_bf16 v[100:103], v[172:175], a[172:175], v[100:103]
v_mfma_f32_16x16x32_bf16 v[108:111], v[172:175], a[188:191], v[108:111]
s_nop 4
v_fma_f32 v80, v40, s16, v80
v_fma_f32 v81, v41, s16, v81
v_fma_f32 v82, v42, s16, v82
v_fma_f32 v83, v43, s16, v83
v_fma_f32 v84, v40, s17, v84
v_fma_f32 v85, v41, s17, v85
v_fma_f32 v86, v42, s17, v86
v_fma_f32 v87, v43, s17, v87
v_fma_f32 v88, v40, s18, v88
v_fma_f32 v89, v41, s18, v89
v_fma_f32 v90, v42, s18, v90
v_fma_f32 v91, v43, s18, v91
v_fma_f32 v92, v40, s19, v92
v_fma_f32 v93, v41, s19, v93
v_fma_f32 v94, v42, s19, v94
v_fma_f32 v95, v43, s19, v95
v_fma_f32 v96, v40, s20, v96
v_fma_f32 v97, v41, s20, v97
v_fma_f32 v98, v42, s20, v98
v_fma_f32 v99, v43, s20, v99
v_fma_f32 v100, v40, s21, v100
v_fma_f32 v101, v41, s21, v101
v_fma_f32 v102, v42, s21, v102
v_fma_f32 v103, v43, s21, v103
v_fma_f32 v104, v40, s22, v104
v_fma_f32 v105, v41, s22, v105
v_fma_f32 v106, v42, s22, v106
v_fma_f32 v107, v43, s22, v107
v_fma_f32 v108, v40, s23, v108
v_fma_f32 v109, v41, s23, v109
v_fma_f32 v110, v42, s23, v110
v_fma_f32 v111, v43, s23, v111
v_max3_f32 v20, v80, v81, v82
v_max3_f32 v21, v88, v89, v90
v_max3_f32 v22, v96, v97, v98
v_max3_f32 v23, v104, v105, v106
v_max3_f32 v20, v20, v83, v84
v_max3_f32 v21, v21, v91, v92
v_max3_f32 v22, v22, v99, v100
v_max3_f32 v23, v23, v107, v108
v_max3_f32 v20, v20, v85, v86
v_max3_f32 v21, v21, v93, v94
v_max3_f32 v22, v22, v101, v102
v_max3_f32 v23, v23, v109, v110
v_max_f32 v20, v20, v87
v_max_f32 v21, v21, v95
v_max_f32 v22, v22, v103
v_max_f32 v23, v23, v111
v_mov_b32 v28, v20
v_mov_b32 v29, v21
v_mov_b32 v30, v22
v_mov_b32 v31, v23
v_permlane16_swap_b32 v20, v28
v_permlane16_swap_b32 v21, v29
v_permlane16_swap_b32 v22, v30
v_permlane16_swap_b32 v23, v31
v_max_f32 v20, v20, v28
v_max_f32 v21, v21, v29
v_max_f32 v22, v22, v30
v_max_f32 v23, v23, v31
v_mov_b32 v28, v20
v_mov_b32 v29, v21
v_mov_b32 v30, v22
v_mov_b32 v31, v23
v_permlane32_swap_b32 v20, v28
v_permlane32_swap_b32 v21, v29
v_permlane32_swap_b32 v22, v30
v_permlane32_swap_b32 v23, v31
v_max_f32 v20, v20, v28
v_max_f32 v21, v21, v29
v_max_f32 v22, v22, v30
v_max_f32 v23, v23, v31
v_sub_f32 v48, 0, v20
v_sub_f32 v49, 0, v20
v_sub_f32 v50, 0, v20
v_sub_f32 v51, 0, v20
v_sub_f32 v52, 0, v21
v_sub_f32 v53, 0, v21
v_sub_f32 v54, 0, v21
v_sub_f32 v55, 0, v21
v_sub_f32 v56, 0, v22
v_sub_f32 v57, 0, v22
v_sub_f32 v58, 0, v22
v_sub_f32 v59, 0, v22
v_sub_f32 v60, 0, v23
v_sub_f32 v61, 0, v23
v_sub_f32 v62, 0, v23
v_sub_f32 v63, 0, v23
s_nop 0
v_mfma_f32_16x16x32_bf16 v[112:115], v[176:179], a[128:131], v[48:51]
v_sub_f32 v80, v80, v20
v_sub_f32 v81, v81, v20
ds_read_b128 v[144:147], v19 offset:0
v_mfma_f32_16x16x32_bf16 v[120:123], v[176:179], a[144:147], v[52:55]
v_exp_f32 v80, v80
v_sub_f32 v82, v82, v20
ds_read_b128 v[148:151], v19 offset:1024
v_mfma_f32_16x16x32_bf16 v[128:131], v[176:179], a[160:163], v[56:59]
v_exp_f32 v81, v81
v_sub_f32 v83, v83, v20
ds_read_b128 v[152:155], v19 offset:2048
v_mfma_f32_16x16x32_bf16 v[136:139], v[176:179], a[176:179], v[60:63]
v_exp_f32 v82, v82
v_sub_f32 v84, v84, v20
ds_read_b128 v[156:159], v19 offset:3072
v_mfma_f32_16x16x32_bf16 v[116:119], v[180:183], a[128:131], v[48:51]
v_exp_f32 v83, v83
v_sub_f32 v85, v85, v20
v_cvt_pk_bf16_f32 v80, v80, v81
ds_read_b128 v[160:163], v19 offset:4096
v_mfma_f32_16x16x32_bf16 v[124:127], v[180:183], a[144:147], v[52:55]
v_exp_f32 v84, v84
v_sub_f32 v86, v86, v20
ds_read_b128 v[164:167], v19 offset:5120
v_mfma_f32_16x16x32_bf16 v[132:135], v[180:183], a[160:163], v[56:59]
v_exp_f32 v85, v85
v_sub_f32 v87, v87, v20
v_cvt_pk_bf16_f32 v81, v82, v83
ds_read_b128 v[168:171], v19 offset:6144
v_mfma_f32_16x16x32_bf16 v[140:143], v[180:183], a[176:179], v[60:63]
v_exp_f32 v86, v86
v_sub_f32 v88, v88, v21
ds_read_b128 v[172:175], v19 offset:7168
v_mfma_f32_16x16x32_bf16 v[112:115], v[184:187], a[132:135], v[112:115]
v_exp_f32 v87, v87
v_sub_f32 v89, v89, v21
v_cvt_pk_bf16_f32 v82, v84, v85
v_mfma_f32_16x16x32_bf16 v[120:123], v[184:187], a[148:151], v[120:123]
v_exp_f32 v88, v88
v_sub_f32 v90, v90, v21
s_mov_b32 m0, s94
v_mfma_f32_16x16x32_bf16 v[128:131], v[184:187], a[164:167], v[128:131]
v_exp_f32 v89, v89
buffer_load_dwordx4 v3, s[8:11], s92 offen offset:0 sc1 lds
v_mfma_f32_16x16x32_bf16 v[136:139], v[184:187], a[180:183], v[136:139]
v_sub_f32 v91, v91, v21
v_cvt_pk_bf16_f32 v83, v86, v87
v_exp_f32 v90, v90
v_sub_f32 v92, v92, v21
v_mfma_f32_16x16x32_bf16 v[116:119], v[188:191], a[132:135], v[116:119]
v_exp_f32 v91, v91
v_sub_f32 v93, v93, v21
v_cvt_pk_bf16_f32 v88, v88, v89
v_mfma_f32_16x16x32_bf16 v[124:127], v[188:191], a[148:151], v[124:127]
v_exp_f32 v92, v92
v_sub_f32 v94, v94, v21
v_exp_f32 v93, v93
v_mfma_f32_16x16x32_bf16 v[132:135], v[188:191], a[164:167], v[132:135]
v_sub_f32 v95, v95, v21
v_cvt_pk_bf16_f32 v89, v90, v91
v_exp_f32 v94, v94
v_sub_f32 v96, v96, v22
v_mfma_f32_16x16x32_bf16 v[140:143], v[188:191], a[180:183], v[140:143]
buffer_load_dwordx4 v3, s[8:11], s92 offen offset:1024 sc1 lds
v_mfma_f32_16x16x32_bf16 v[112:115], v[192:195], a[136:139], v[112:115]
v_exp_f32 v95, v95
v_sub_f32 v97, v97, v22
v_cvt_pk_bf16_f32 v90, v92, v93
v_mfma_f32_16x16x32_bf16 v[120:123], v[192:195], a[152:155], v[120:123]
v_exp_f32 v96, v96
v_sub_f32 v98, v98, v22
v_exp_f32 v97, v97
v_mfma_f32_16x16x32_bf16 v[128:131], v[192:195], a[168:171], v[128:131]
v_sub_f32 v99, v99, v22
v_cvt_pk_bf16_f32 v91, v94, v95
v_exp_f32 v98, v98
v_sub_f32 v100, v100, v22
v_mfma_f32_16x16x32_bf16 v[136:139], v[192:195], a[184:187], v[136:139]
v_exp_f32 v99, v99
v_sub_f32 v101, v101, v22
v_cvt_pk_bf16_f32 v96, v96, v97
v_mfma_f32_16x16x32_bf16 v[116:119], v[196:199], a[136:139], v[116:119]
v_exp_f32 v100, v100
buffer_load_dwordx4 v3, s[8:11], s92 offen offset:2048 sc1 lds
v_mfma_f32_16x16x32_bf16 v[124:127], v[196:199], a[152:155], v[124:127]
v_sub_f32 v102, v102, v22
v_exp_f32 v101, v101
v_sub_f32 v103, v103, v22
v_mfma_f32_16x16x32_bf16 v[132:135], v[196:199], a[168:171], v[132:135]
v_cvt_pk_bf16_f32 v97, v98, v99
v_exp_f32 v102, v102
v_sub_f32 v104, v104, v23
v_mfma_f32_16x16x32_bf16 v[140:143], v[196:199], a[184:187], v[140:143]
v_exp_f32 v103, v103
v_sub_f32 v105, v105, v23
v_cvt_pk_bf16_f32 v98, v100, v101
v_mfma_f32_16x16x32_bf16 v[112:115], v[200:203], a[140:143], v[112:115]
v_exp_f32 v104, v104
v_sub_f32 v106, v106, v23
v_exp_f32 v105, v105
v_mfma_f32_16x16x32_bf16 v[120:123], v[200:203], a[156:159], v[120:123]
v_sub_f32 v107, v107, v23
)ASM"
R"ASM(
buffer_load_dwordx4 v3, s[8:11], s92 offen offset:3072 sc1 lds
v_mfma_f32_16x16x32_bf16 v[128:131], v[200:203], a[172:175], v[128:131]
v_cvt_pk_bf16_f32 v99, v102, v103
v_exp_f32 v106, v106
v_sub_f32 v108, v108, v23
s_add_u32 s92, s92, 8192
v_mfma_f32_16x16x32_bf16 v[136:139], v[200:203], a[188:191], v[136:139]
v_exp_f32 v107, v107
v_sub_f32 v109, v109, v23
v_cvt_pk_bf16_f32 v104, v104, v105
v_mfma_f32_16x16x32_bf16 v[116:119], v[204:207], a[140:143], v[116:119]
v_exp_f32 v108, v108
v_sub_f32 v110, v110, v23
v_mfma_f32_16x16x32_bf16 v[124:127], v[204:207], a[156:159], v[124:127]
v_exp_f32 v109, v109
v_sub_f32 v111, v111, v23
v_cvt_pk_bf16_f32 v105, v106, v107
v_mfma_f32_16x16x32_bf16 v[132:135], v[204:207], a[172:175], v[132:135]
v_exp_f32 v110, v110
v_exp_f32 v111, v111
v_cvt_pk_bf16_f32 v106, v108, v109
v_cvt_pk_bf16_f32 v107, v110, v111
v_mfma_f32_16x16x32_bf16 v[140:143], v[204:207], a[188:191], v[140:143]
s_waitcnt vmcnt(16) lgkmcnt(0)
s_barrier
s_cmp_eq_u32 s54, 0
s_cbranch_scc1 .LT1_%=
s_sub_u32 s96, s54, 1
s_cmp_eq_u32 s96, 0
s_cbranch_scc1 .LpA_%=
.LlA_%=:
v_mfma_f32_16x16x32_bf16 a[0:3], v[144:147], v[80:83], a[0:3]
ds_read_b128 v[176:179], v16 offset:0
v_mfma_f32_16x16x32_bf16 a[4:7], v[144:147], v[88:91], a[4:7]
ds_read_b128 v[180:183], v16 offset:1024
v_mfma_f32_16x16x32_bf16 a[8:11], v[144:147], v[96:99], a[8:11]
ds_read_b128 v[184:187], v16 offset:2048
v_mfma_f32_16x16x32_bf16 a[12:15], v[144:147], v[104:107], a[12:15]
ds_read_b128 v[188:191], v16 offset:3072
v_mfma_f32_16x16x32_bf16 a[16:19], v[148:151], v[80:83], a[16:19]
v_exp_f32 v112, v112
ds_read_b128 v[192:195], v16 offset:4096
v_mfma_f32_16x16x32_bf16 a[20:23], v[148:151], v[88:91], a[20:23]
ds_read_b128 v[196:199], v16 offset:5120
v_mfma_f32_16x16x32_bf16 a[24:27], v[148:151], v[96:99], a[24:27]
v_exp_f32 v113, v113
ds_read_b128 v[200:203], v16 offset:6144
v_mfma_f32_16x16x32_bf16 a[28:31], v[148:151], v[104:107], a[28:31]
ds_read_b128 v[204:207], v16 offset:7168
v_mfma_f32_16x16x32_bf16 a[32:35], v[152:155], v[80:83], a[32:35]
v_exp_f32 v114, v114
v_mfma_f32_16x16x32_bf16 a[36:39], v[152:155], v[88:91], a[36:39]
v_exp_f32 v115, v115
s_mov_b32 m0, s95
v_mfma_f32_16x16x32_bf16 a[40:43], v[152:155], v[96:99], a[40:43]
buffer_load_dwordx4 v3, s[8:11], s93 offen offset:0 sc1 lds
v_mfma_f32_16x16x32_bf16 a[44:47], v[152:155], v[104:107], a[44:47]
v_cvt_pk_bf16_f32 v112, v112, v113
v_mfma_f32_16x16x32_bf16 a[48:51], v[156:159], v[80:83], a[48:51]
v_exp_f32 v116, v116
v_mfma_f32_16x16x32_bf16 a[52:55], v[156:159], v[88:91], a[52:55]
v_exp_f32 v117, v117
v_mfma_f32_16x16x32_bf16 a[56:59], v[156:159], v[96:99], a[56:59]
v_cvt_pk_bf16_f32 v113, v114, v115
v_mfma_f32_16x16x32_bf16 a[60:63], v[156:159], v[104:107], a[60:63]
v_exp_f32 v118, v118
v_mfma_f32_16x16x32_bf16 a[64:67], v[160:163], v[80:83], a[64:67]
buffer_load_dwordx4 v3, s[8:11], s93 offen offset:1024 sc1 lds
v_mfma_f32_16x16x32_bf16 a[68:71], v[160:163], v[88:91], a[68:71]
v_exp_f32 v119, v119
v_mfma_f32_16x16x32_bf16 a[72:75], v[160:163], v[96:99], a[72:75]
v_cvt_pk_bf16_f32 v114, v116, v117
v_mfma_f32_16x16x32_bf16 a[76:79], v[160:163], v[104:107], a[76:79]
v_exp_f32 v120, v120
v_mfma_f32_16x16x32_bf16 a[80:83], v[164:167], v[80:83], a[80:83]
v_exp_f32 v121, v121
v_cvt_pk_bf16_f32 v115, v118, v119
v_mfma_f32_16x16x32_bf16 a[84:87], v[164:167], v[88:91], a[84:87]
v_exp_f32 v122, v122
v_mfma_f32_16x16x32_bf16 a[88:91], v[164:167], v[96:99], a[88:91]
buffer_load_dwordx4 v3, s[8:11], s93 offen offset:2048 sc1 lds
v_mfma_f32_16x16x32_bf16 a[92:95], v[164:167], v[104:107], a[92:95]
v_mfma_f32_16x16x32_bf16 a[96:99], v[168:171], v[80:83], a[96:99]
v_exp_f32 v123, v123
v_cvt_pk_bf16_f32 v120, v120, v121
v_mfma_f32_16x16x32_bf16 a[100:103], v[168:171], v[88:91], a[100:103]
v_exp_f32 v124, v124
v_mfma_f32_16x16x32_bf16 a[104:107], v[168:171], v[96:99], a[104:107]
v_exp_f32 v125, v125
v_mfma_f32_16x16x32_bf16 a[108:111], v[168:171], v[104:107], a[108:111]
v_cvt_pk_bf16_f32 v121, v122, v123
v_mfma_f32_16x16x32_bf16 a[112:115], v[172:175], v[80:83], a[112:115]
buffer_load_dwordx4 v3, s[8:11], s93 offen offset:3072 sc1 lds
v_mfma_f32_16x16x32_bf16 a[116:119], v[172:175], v[88:91], a[116:119]
s_add_u32 s93, s93, 8192
v_mfma_f32_16x16x32_bf16 a[120:123], v[172:175], v[96:99], a[120:123]
v_exp_f32 v126, v126
v_mfma_f32_16x16x32_bf16 a[124:127], v[172:175], v[104:107], a[124:127]
v_exp_f32 v127, v127
v_cvt_pk_bf16_f32 v122, v124, v125
v_mfma_f32_16x16x32_bf16 v[64:67], v[44:47], v[80:83], v[64:67]
v_exp_f32 v128, v128
v_mfma_f32_16x16x32_bf16 v[68:71], v[44:47], v[88:91], v[68:71]
v_exp_f32 v129, v129
v_mfma_f32_16x16x32_bf16 v[72:75], v[44:47], v[96:99], v[72:75]
v_cvt_pk_bf16_f32 v123, v126, v127
v_mfma_f32_16x16x32_bf16 v[76:79], v[44:47], v[104:107], v[76:79]
v_exp_f32 v130, v130
s_waitcnt lgkmcnt(0)
v_mfma_f32_16x16x32_bf16 v[80:83], v[176:179], a[128:131], v[48:51]
ds_read_b128 v[144:147], v19 offset:16384
v_mfma_f32_16x16x32_bf16 v[88:91], v[176:179], a[144:147], v[52:55]
v_exp_f32 v131, v131
ds_read_b128 v[148:151], v19 offset:17408
v_mfma_f32_16x16x32_bf16 v[96:99], v[176:179], a[160:163], v[56:59]
v_cvt_pk_bf16_f32 v128, v128, v129
ds_read_b128 v[152:155], v19 offset:18432
v_mfma_f32_16x16x32_bf16 v[104:107], v[176:179], a[176:179], v[60:63]
ds_read_b128 v[156:159], v19 offset:19456
v_mfma_f32_16x16x32_bf16 v[84:87], v[180:183], a[128:131], v[48:51]
v_exp_f32 v132, v132
ds_read_b128 v[160:163], v19 offset:20480
v_mfma_f32_16x16x32_bf16 v[92:95], v[180:183], a[144:147], v[52:55]
ds_read_b128 v[164:167], v19 offset:21504
v_mfma_f32_16x16x32_bf16 v[100:103], v[180:183], a[160:163], v[56:59]
v_exp_f32 v133, v133
ds_read_b128 v[168:171], v19 offset:22528
v_mfma_f32_16x16x32_bf16 v[108:111], v[180:183], a[176:179], v[60:63]
v_cvt_pk_bf16_f32 v129, v130, v131
ds_read_b128 v[172:175], v19 offset:23552
v_mfma_f32_16x16x32_bf16 v[80:83], v[184:187], a[132:135], v[80:83]
v_xor_b32 v19, 32768, v19
v_mfma_f32_16x16x32_bf16 v[88:91], v[184:187], a[148:151], v[88:91]
v_exp_f32 v134, v134
v_mfma_f32_16x16x32_bf16 v[96:99], v[184:187], a[164:167], v[96:99]
v_exp_f32 v135, v135
v_cvt_pk_bf16_f32 v130, v132, v133
v_mfma_f32_16x16x32_bf16 v[104:107], v[184:187], a[180:183], v[104:107]
s_add_u32 m0, s94, 16384
v_mfma_f32_16x16x32_bf16 v[84:87], v[188:191], a[132:135], v[84:87]
buffer_load_dwordx4 v3, s[8:11], s92 offen offset:0 sc1 lds
v_mfma_f32_16x16x32_bf16 v[92:95], v[188:191], a[148:151], v[92:95]
v_exp_f32 v136, v136
v_mfma_f32_16x16x32_bf16 v[100:103], v[188:191], a[164:167], v[100:103]
v_exp_f32 v137, v137
v_mfma_f32_16x16x32_bf16 v[108:111], v[188:191], a[180:183], v[108:111]
v_cvt_pk_bf16_f32 v131, v134, v135
v_mfma_f32_16x16x32_bf16 v[80:83], v[192:195], a[136:139], v[80:83]
v_exp_f32 v138, v138
v_mfma_f32_16x16x32_bf16 v[88:91], v[192:195], a[152:155], v[88:91]
buffer_load_dwordx4 v3, s[8:11], s92 offen offset:1024 sc1 lds
v_mfma_f32_16x16x32_bf16 v[96:99], v[192:195], a[168:171], v[96:99]
v_exp_f32 v139, v139
v_mfma_f32_16x16x32_bf16 v[104:107], v[192:195], a[184:187], v[104:107]
v_cvt_pk_bf16_f32 v136, v136, v137
v_mfma_f32_16x16x32_bf16 v[84:87], v[196:199], a[136:139], v[84:87]
v_exp_f32 v140, v140
v_mfma_f32_16x16x32_bf16 v[92:95], v[196:199], a[152:155], v[92:95]
v_exp_f32 v141, v141
v_cvt_pk_bf16_f32 v137, v138, v139
v_mfma_f32_16x16x32_bf16 v[100:103], v[196:199], a[168:171], v[100:103]
buffer_load_dwordx4 v3, s[8:11], s92 offen offset:2048 sc1 lds
v_mfma_f32_16x16x32_bf16 v[108:111], v[196:199], a[184:187], v[108:111]
v_mfma_f32_16x16x32_bf16 v[80:83], v[200:203], a[140:143], v[80:83]
v_exp_f32 v142, v142
v_mfma_f32_16x16x32_bf16 v[88:91], v[200:203], a[156:159], v[88:91]
v_exp_f32 v143, v143
v_cvt_pk_bf16_f32 v138, v140, v141
v_mfma_f32_16x16x32_bf16 v[96:99], v[200:203], a[172:175], v[96:99]
v_cvt_pk_bf16_f32 v139, v142, v143
v_mfma_f32_16x16x32_bf16 v[104:107], v[200:203], a[188:191], v[104:107]
buffer_load_dwordx4 v3, s[8:11], s92 offen offset:3072 sc1 lds
v_mfma_f32_16x16x32_bf16 v[84:87], v[204:207], a[140:143], v[84:87]
s_add_u32 s92, s92, 24576
s_cmp_eq_u32 s92, s52
v_mfma_f32_16x16x32_bf16 v[92:95], v[204:207], a[156:159], v[92:95]
s_cselect_b32 s92, s82, s92
s_cselect_b32 s52, -1, s52
s_xor_b32 s94, s94, 32768
v_mfma_f32_16x16x32_bf16 v[100:103], v[204:207], a[172:175], v[100:103]
v_mfma_f32_16x16x32_bf16 v[108:111], v[204:207], a[188:191], v[108:111]
s_waitcnt vmcnt(16) lgkmcnt(0)
s_barrier
v_mfma_f32_16x16x32_bf16 a[0:3], v[144:147], v[112:115], a[0:3]
ds_read_b128 v[176:179], v16 offset:16384
v_mfma_f32_16x16x32_bf16 a[4:7], v[144:147], v[120:123], a[4:7]
ds_read_b128 v[180:183], v16 offset:17408
v_mfma_f32_16x16x32_bf16 a[8:11], v[144:147], v[128:131], a[8:11]
ds_read_b128 v[184:187], v16 offset:18432
v_mfma_f32_16x16x32_bf16 a[12:15], v[144:147], v[136:139], a[12:15]
ds_read_b128 v[188:191], v16 offset:19456
v_mfma_f32_16x16x32_bf16 a[16:19], v[148:151], v[112:115], a[16:19]
v_exp_f32 v80, v80
ds_read_b128 v[192:195], v16 offset:20480
v_mfma_f32_16x16x32_bf16 a[20:23], v[148:151], v[120:123], a[20:23]
ds_read_b128 v[196:199], v16 offset:21504
v_mfma_f32_16x16x32_bf16 a[24:27], v[148:151], v[128:131], a[24:27]
v_exp_f32 v81, v81
ds_read_b128 v[200:203], v16 offset:22528
v_mfma_f32_16x16x32_bf16 a[28:31], v[148:151], v[136:139], a[28:31]
)ASM"
R"ASM(
ds_read_b128 v[204:207], v16 offset:23552
v_mfma_f32_16x16x32_bf16 a[32:35], v[152:155], v[112:115], a[32:35]
v_exp_f32 v82, v82
v_xor_b32 v16, 32768, v16
v_mfma_f32_16x16x32_bf16 a[36:39], v[152:155], v[120:123], a[36:39]
s_add_u32 m0, s95, 16384
v_mfma_f32_16x16x32_bf16 a[40:43], v[152:155], v[128:131], a[40:43]
buffer_load_dwordx4 v3, s[8:11], s93 offen offset:0 sc1 lds
v_mfma_f32_16x16x32_bf16 a[44:47], v[152:155], v[136:139], a[44:47]
v_exp_f32 v83, v83
v_mfma_f32_16x16x32_bf16 a[48:51], v[156:159], v[112:115], a[48:51]
v_cvt_pk_bf16_f32 v80, v80, v81
v_mfma_f32_16x16x32_bf16 a[52:55], v[156:159], v[120:123], a[52:55]
v_exp_f32 v84, v84
v_mfma_f32_16x16x32_bf16 a[56:59], v[156:159], v[128:131], a[56:59]
v_exp_f32 v85, v85
v_cvt_pk_bf16_f32 v81, v82, v83
v_mfma_f32_16x16x32_bf16 a[60:63], v[156:159], v[136:139], a[60:63]
v_exp_f32 v86, v86
v_mfma_f32_16x16x32_bf16 a[64:67], v[160:163], v[112:115], a[64:67]
buffer_load_dwordx4 v3, s[8:11], s93 offen offset:1024 sc1 lds
v_mfma_f32_16x16x32_bf16 a[68:71], v[160:163], v[120:123], a[68:71]
v_mfma_f32_16x16x32_bf16 a[72:75], v[160:163], v[128:131], a[72:75]
v_exp_f32 v87, v87
v_cvt_pk_bf16_f32 v82, v84, v85
v_mfma_f32_16x16x32_bf16 a[76:79], v[160:163], v[136:139], a[76:79]
v_exp_f32 v88, v88
v_mfma_f32_16x16x32_bf16 a[80:83], v[164:167], v[112:115], a[80:83]
v_exp_f32 v89, v89
v_mfma_f32_16x16x32_bf16 a[84:87], v[164:167], v[120:123], a[84:87]
v_cvt_pk_bf16_f32 v83, v86, v87
v_mfma_f32_16x16x32_bf16 a[88:91], v[164:167], v[128:131], a[88:91]
buffer_load_dwordx4 v3, s[8:11], s93 offen offset:2048 sc1 lds
v_mfma_f32_16x16x32_bf16 a[92:95], v[164:167], v[136:139], a[92:95]
v_exp_f32 v90, v90
v_mfma_f32_16x16x32_bf16 a[96:99], v[168:171], v[112:115], a[96:99]
v_exp_f32 v91, v91
v_mfma_f32_16x16x32_bf16 a[100:103], v[168:171], v[120:123], a[100:103]
v_cvt_pk_bf16_f32 v88, v88, v89
v_mfma_f32_16x16x32_bf16 a[104:107], v[168:171], v[128:131], a[104:107]
v_exp_f32 v92, v92
v_mfma_f32_16x16x32_bf16 a[108:111], v[168:171], v[136:139], a[108:111]
v_exp_f32 v93, v93
v_cvt_pk_bf16_f32 v89, v90, v91
v_mfma_f32_16x16x32_bf16 a[112:115], v[172:175], v[112:115], a[112:115]
buffer_load_dwordx4 v3, s[8:11], s93 offen offset:3072 sc1 lds
v_mfma_f32_16x16x32_bf16 a[116:119], v[172:175], v[120:123], a[116:119]
s_add_u32 s93, s93, 24576
s_cmp_eq_u32 s93, s53
v_mfma_f32_16x16x32_bf16 a[120:123], v[172:175], v[128:131], a[120:123]
s_cselect_b32 s93, s14, s93
s_cselect_b32 s53, -1, s53
v_mfma_f32_16x16x32_bf16 a[124:127], v[172:175], v[136:139], a[124:127]
s_xor_b32 s95, s95, 32768
v_mfma_f32_16x16x32_bf16 v[64:67], v[44:47], v[112:115], v[64:67]
v_exp_f32 v94, v94
v_mfma_f32_16x16x32_bf16 v[68:71], v[44:47], v[120:123], v[68:71]
v_exp_f32 v95, v95
v_mfma_f32_16x16x32_bf16 v[72:75], v[44:47], v[128:131], v[72:75]
v_cvt_pk_bf16_f32 v90, v92, v93
v_exp_f32 v96, v96
v_mfma_f32_16x16x32_bf16 v[76:79], v[44:47], v[136:139], v[76:79]
s_waitcnt lgkmcnt(0)
v_mfma_f32_16x16x32_bf16 v[112:115], v[176:179], a[128:131], v[48:51]
v_exp_f32 v97, v97
ds_read_b128 v[144:147], v19 offset:0
v_mfma_f32_16x16x32_bf16 v[120:123], v[176:179], a[144:147], v[52:55]
v_cvt_pk_bf16_f32 v91, v94, v95
ds_read_b128 v[148:151], v19 offset:1024
v_mfma_f32_16x16x32_bf16 v[128:131], v[176:179], a[160:163], v[56:59]
ds_read_b128 v[152:155], v19 offset:2048
v_mfma_f32_16x16x32_bf16 v[136:139], v[176:179], a[176:179], v[60:63]
v_exp_f32 v98, v98
ds_read_b128 v[156:159], v19 offset:3072
v_mfma_f32_16x16x32_bf16 v[116:119], v[180:183], a[128:131], v[48:51]
ds_read_b128 v[160:163], v19 offset:4096
v_mfma_f32_16x16x32_bf16 v[124:127], v[180:183], a[144:147], v[52:55]
v_exp_f32 v99, v99
ds_read_b128 v[164:167], v19 offset:5120
v_mfma_f32_16x16x32_bf16 v[132:135], v[180:183], a[160:163], v[56:59]
v_cvt_pk_bf16_f32 v96, v96, v97
ds_read_b128 v[168:171], v19 offset:6144
v_mfma_f32_16x16x32_bf16 v[140:143], v[180:183], a[176:179], v[60:63]
ds_read_b128 v[172:175], v19 offset:7168
v_mfma_f32_16x16x32_bf16 v[112:115], v[184:187], a[132:135], v[112:115]
v_exp_f32 v100, v100
v_mfma_f32_16x16x32_bf16 v[120:123], v[184:187], a[148:151], v[120:123]
v_exp_f32 v101, v101
v_cvt_pk_bf16_f32 v97, v98, v99
v_mfma_f32_16x16x32_bf16 v[128:131], v[184:187], a[164:167], v[128:131]
v_exp_f32 v102, v102
v_mfma_f32_16x16x32_bf16 v[136:139], v[184:187], a[180:183], v[136:139]
s_mov_b32 m0, s94
v_mfma_f32_16x16x32_bf16 v[116:119], v[188:191], a[132:135], v[116:119]
buffer_load_dwordx4 v3, s[8:11], s92 offen offset:0 sc1 lds
v_mfma_f32_16x16x32_bf16 v[124:127], v[188:191], a[148:151], v[124:127]
v_exp_f32 v103, v103
v_mfma_f32_16x16x32_bf16 v[132:135], v[188:191], a[164:167], v[132:135]
v_cvt_pk_bf16_f32 v98, v100, v101
v_mfma_f32_16x16x32_bf16 v[140:143], v[188:191], a[180:183], v[140:143]
v_exp_f32 v104, v104
v_mfma_f32_16x16x32_bf16 v[112:115], v[192:195], a[136:139], v[112:115]
v_exp_f32 v105, v105
v_cvt_pk_bf16_f32 v99, v102, v103
v_mfma_f32_16x16x32_bf16 v[120:123], v[192:195], a[152:155], v[120:123]
buffer_load_dwordx4 v3, s[8:11], s92 offen offset:1024 sc1 lds
v_mfma_f32_16x16x32_bf16 v[128:131], v[192:195], a[168:171], v[128:131]
v_mfma_f32_16x16x32_bf16 v[136:139], v[192:195], a[184:187], v[136:139]
v_exp_f32 v106, v106
v_mfma_f32_16x16x32_bf16 v[116:119], v[196:199], a[136:139], v[116:119]
v_exp_f32 v107, v107
v_cvt_pk_bf16_f32 v104, v104, v105
v_mfma_f32_16x16x32_bf16 v[124:127], v[196:199], a[152:155], v[124:127]
v_exp_f32 v108, v108
v_mfma_f32_16x16x32_bf16 v[132:135], v[196:199], a[168:171], v[132:135]
buffer_load_dwordx4 v3, s[8:11], s92 offen offset:2048 sc1 lds
v_mfma_f32_16x16x32_bf16 v[140:143], v[196:199], a[184:187], v[140:143]
v_mfma_f32_16x16x32_bf16 v[112:115], v[200:203], a[140:143], v[112:115]
v_exp_f32 v109, v109
v_cvt_pk_bf16_f32 v105, v106, v107
v_mfma_f32_16x16x32_bf16 v[120:123], v[200:203], a[156:159], v[120:123]
v_exp_f32 v110, v110
v_mfma_f32_16x16x32_bf16 v[128:131], v[200:203], a[172:175], v[128:131]
v_exp_f32 v111, v111
v_mfma_f32_16x16x32_bf16 v[136:139], v[200:203], a[188:191], v[136:139]
buffer_load_dwordx4 v3, s[8:11], s92 offen offset:3072 sc1 lds
v_mfma_f32_16x16x32_bf16 v[116:119], v[204:207], a[140:143], v[116:119]
s_add_u32 s92, s92, 8192
v_mfma_f32_16x16x32_bf16 v[124:127], v[204:207], a[156:159], v[124:127]
v_cvt_pk_bf16_f32 v106, v108, v109
v_cvt_pk_bf16_f32 v107, v110, v111
v_mfma_f32_16x16x32_bf16 v[132:135], v[204:207], a[172:175], v[132:135]
s_sub_u32 s96, s96, 1
s_cmp_lg_u32 s96, 0
v_mfma_f32_16x16x32_bf16 v[140:143], v[204:207], a[188:191], v[140:143]
s_waitcnt vmcnt(16) lgkmcnt(0)
s_barrier
s_cbranch_scc1 .LlA_%=
.LpA_%=:
v_mfma_f32_16x16x32_bf16 a[0:3], v[144:147], v[80:83], a[0:3]
ds_read_b128 v[176:179], v16 offset:0
v_mfma_f32_16x16x32_bf16 a[4:7], v[144:147], v[88:91], a[4:7]
ds_read_b128 v[180:183], v16 offset:1024
v_mfma_f32_16x16x32_bf16 a[8:11], v[144:147], v[96:99], a[8:11]
ds_read_b128 v[184:187], v16 offset:2048
v_mfma_f32_16x16x32_bf16 a[12:15], v[144:147], v[104:107], a[12:15]
ds_read_b128 v[188:191], v16 offset:3072
v_mfma_f32_16x16x32_bf16 a[16:19], v[148:151], v[80:83], a[16:19]
v_exp_f32 v112, v112
ds_read_b128 v[192:195], v16 offset:4096
v_mfma_f32_16x16x32_bf16 a[20:23], v[148:151], v[88:91], a[20:23]
ds_read_b128 v[196:199], v16 offset:5120
v_mfma_f32_16x16x32_bf16 a[24:27], v[148:151], v[96:99], a[24:27]
v_exp_f32 v113, v113
ds_read_b128 v[200:203], v16 offset:6144
v_mfma_f32_16x16x32_bf16 a[28:31], v[148:151], v[104:107], a[28:31]
ds_read_b128 v[204:207], v16 offset:7168
v_mfma_f32_16x16x32_bf16 a[32:35], v[152:155], v[80:83], a[32:35]
v_exp_f32 v114, v114
v_mfma_f32_16x16x32_bf16 a[36:39], v[152:155], v[88:91], a[36:39]
v_exp_f32 v115, v115
s_mov_b32 m0, s95
v_mfma_f32_16x16x32_bf16 a[40:43], v[152:155], v[96:99], a[40:43]
buffer_load_dwordx4 v3, s[8:11], s93 offen offset:0 sc1 lds
v_mfma_f32_16x16x32_bf16 a[44:47], v[152:155], v[104:107], a[44:47]
v_cvt_pk_bf16_f32 v112, v112, v113
v_mfma_f32_16x16x32_bf16 a[48:51], v[156:159], v[80:83], a[48:51]
v_exp_f32 v116, v116
v_mfma_f32_16x16x32_bf16 a[52:55], v[156:159], v[88:91], a[52:55]
v_exp_f32 v117, v117
v_mfma_f32_16x16x32_bf16 a[56:59], v[156:159], v[96:99], a[56:59]
v_cvt_pk_bf16_f32 v113, v114, v115
v_mfma_f32_16x16x32_bf16 a[60:63], v[156:159], v[104:107], a[60:63]
v_exp_f32 v118, v118
v_mfma_f32_16x16x32_bf16 a[64:67], v[160:163], v[80:83], a[64:67]
buffer_load_dwordx4 v3, s[8:11], s93 offen offset:1024 sc1 lds
v_mfma_f32_16x16x32_bf16 a[68:71], v[160:163], v[88:91], a[68:71]
v_exp_f32 v119, v119
v_mfma_f32_16x16x32_bf16 a[72:75], v[160:163], v[96:99], a[72:75]
v_cvt_pk_bf16_f32 v114, v116, v117
v_mfma_f32_16x16x32_bf16 a[76:79], v[160:163], v[104:107], a[76:79]
v_exp_f32 v120, v120
v_mfma_f32_16x16x32_bf16 a[80:83], v[164:167], v[80:83], a[80:83]
v_exp_f32 v121, v121
v_cvt_pk_bf16_f32 v115, v118, v119
v_mfma_f32_16x16x32_bf16 a[84:87], v[164:167], v[88:91], a[84:87]
v_exp_f32 v122, v122
v_mfma_f32_16x16x32_bf16 a[88:91], v[164:167], v[96:99], a[88:91]
buffer_load_dwordx4 v3, s[8:11], s93 offen offset:2048 sc1 lds
v_mfma_f32_16x16x32_bf16 a[92:95], v[164:167], v[104:107], a[92:95]
v_mfma_f32_16x16x32_bf16 a[96:99], v[168:171], v[80:83], a[96:99]
v_exp_f32 v123, v123
v_cvt_pk_bf16_f32 v120, v120, v121
v_mfma_f32_16x16x32_bf16 a[100:103], v[168:171], v[88:91], a[100:103]
v_exp_f32 v124, v124
v_mfma_f32_16x16x32_bf16 a[104:107], v[168:171], v[96:99], a[104:107]
v_exp_f32 v125, v125
v_mfma_f32_16x16x32_bf16 a[108:111], v[168:171], v[104:107], a[108:111]
v_cvt_pk_bf16_f32 v121, v122, v123
v_mfma_f32_16x16x32_bf16 a[112:115], v[172:175], v[80:83], a[112:115]
)ASM"
R"ASM(
buffer_load_dwordx4 v3, s[8:11], s93 offen offset:3072 sc1 lds
v_mfma_f32_16x16x32_bf16 a[116:119], v[172:175], v[88:91], a[116:119]
s_add_u32 s93, s93, 8192
v_mfma_f32_16x16x32_bf16 a[120:123], v[172:175], v[96:99], a[120:123]
v_exp_f32 v126, v126
v_mfma_f32_16x16x32_bf16 a[124:127], v[172:175], v[104:107], a[124:127]
v_exp_f32 v127, v127
v_cvt_pk_bf16_f32 v122, v124, v125
v_mfma_f32_16x16x32_bf16 v[64:67], v[44:47], v[80:83], v[64:67]
v_exp_f32 v128, v128
v_mfma_f32_16x16x32_bf16 v[68:71], v[44:47], v[88:91], v[68:71]
v_exp_f32 v129, v129
v_mfma_f32_16x16x32_bf16 v[72:75], v[44:47], v[96:99], v[72:75]
v_cvt_pk_bf16_f32 v123, v126, v127
v_mfma_f32_16x16x32_bf16 v[76:79], v[44:47], v[104:107], v[76:79]
v_exp_f32 v130, v130
s_waitcnt lgkmcnt(0)
v_mfma_f32_16x16x32_bf16 v[80:83], v[176:179], a[128:131], v[48:51]
ds_read_b128 v[144:147], v19 offset:16384
v_mfma_f32_16x16x32_bf16 v[88:91], v[176:179], a[144:147], v[52:55]
v_exp_f32 v131, v131
ds_read_b128 v[148:151], v19 offset:17408
v_mfma_f32_16x16x32_bf16 v[96:99], v[176:179], a[160:163], v[56:59]
v_cvt_pk_bf16_f32 v128, v128, v129
ds_read_b128 v[152:155], v19 offset:18432
v_mfma_f32_16x16x32_bf16 v[104:107], v[176:179], a[176:179], v[60:63]
ds_read_b128 v[156:159], v19 offset:19456
v_mfma_f32_16x16x32_bf16 v[84:87], v[180:183], a[128:131], v[48:51]
v_exp_f32 v132, v132
ds_read_b128 v[160:163], v19 offset:20480
v_mfma_f32_16x16x32_bf16 v[92:95], v[180:183], a[144:147], v[52:55]
ds_read_b128 v[164:167], v19 offset:21504
v_mfma_f32_16x16x32_bf16 v[100:103], v[180:183], a[160:163], v[56:59]
v_exp_f32 v133, v133
ds_read_b128 v[168:171], v19 offset:22528
v_mfma_f32_16x16x32_bf16 v[108:111], v[180:183], a[176:179], v[60:63]
v_cvt_pk_bf16_f32 v129, v130, v131
ds_read_b128 v[172:175], v19 offset:23552
v_mfma_f32_16x16x32_bf16 v[80:83], v[184:187], a[132:135], v[80:83]
v_xor_b32 v19, 32768, v19
v_mfma_f32_16x16x32_bf16 v[88:91], v[184:187], a[148:151], v[88:91]
v_exp_f32 v134, v134
v_mfma_f32_16x16x32_bf16 v[96:99], v[184:187], a[164:167], v[96:99]
v_exp_f32 v135, v135
v_cvt_pk_bf16_f32 v130, v132, v133
v_mfma_f32_16x16x32_bf16 v[104:107], v[184:187], a[180:183], v[104:107]
s_add_u32 m0, s94, 16384
v_mfma_f32_16x16x32_bf16 v[84:87], v[188:191], a[132:135], v[84:87]
buffer_load_dwordx4 v3, s[8:11], s92 offen offset:0 sc1 lds
v_mfma_f32_16x16x32_bf16 v[92:95], v[188:191], a[148:151], v[92:95]
v_exp_f32 v136, v136
v_mfma_f32_16x16x32_bf16 v[100:103], v[188:191], a[164:167], v[100:103]
v_exp_f32 v137, v137
v_mfma_f32_16x16x32_bf16 v[108:111], v[188:191], a[180:183], v[108:111]
v_cvt_pk_bf16_f32 v131, v134, v135
v_mfma_f32_16x16x32_bf16 v[80:83], v[192:195], a[136:139], v[80:83]
v_exp_f32 v138, v138
v_mfma_f32_16x16x32_bf16 v[88:91], v[192:195], a[152:155], v[88:91]
buffer_load_dwordx4 v3, s[8:11], s92 offen offset:1024 sc1 lds
v_mfma_f32_16x16x32_bf16 v[96:99], v[192:195], a[168:171], v[96:99]
v_exp_f32 v139, v139
v_mfma_f32_16x16x32_bf16 v[104:107], v[192:195], a[184:187], v[104:107]
v_cvt_pk_bf16_f32 v136, v136, v137
v_mfma_f32_16x16x32_bf16 v[84:87], v[196:199], a[136:139], v[84:87]
v_exp_f32 v140, v140
v_mfma_f32_16x16x32_bf16 v[92:95], v[196:199], a[152:155], v[92:95]
v_exp_f32 v141, v141
v_cvt_pk_bf16_f32 v137, v138, v139
v_mfma_f32_16x16x32_bf16 v[100:103], v[196:199], a[168:171], v[100:103]
buffer_load_dwordx4 v3, s[8:11], s92 offen offset:2048 sc1 lds
v_mfma_f32_16x16x32_bf16 v[108:111], v[196:199], a[184:187], v[108:111]
v_mfma_f32_16x16x32_bf16 v[80:83], v[200:203], a[140:143], v[80:83]
v_exp_f32 v142, v142
v_mfma_f32_16x16x32_bf16 v[88:91], v[200:203], a[156:159], v[88:91]
v_exp_f32 v143, v143
v_cvt_pk_bf16_f32 v138, v140, v141
v_mfma_f32_16x16x32_bf16 v[96:99], v[200:203], a[172:175], v[96:99]
v_cvt_pk_bf16_f32 v139, v142, v143
v_mfma_f32_16x16x32_bf16 v[104:107], v[200:203], a[188:191], v[104:107]
buffer_load_dwordx4 v3, s[8:11], s92 offen offset:3072 sc1 lds
v_mfma_f32_16x16x32_bf16 v[84:87], v[204:207], a[140:143], v[84:87]
s_add_u32 s92, s92, 24576
s_cmp_eq_u32 s92, s52
v_mfma_f32_16x16x32_bf16 v[92:95], v[204:207], a[156:159], v[92:95]
s_cselect_b32 s92, s82, s92
s_cselect_b32 s52, -1, s52
s_xor_b32 s94, s94, 32768
v_mfma_f32_16x16x32_bf16 v[100:103], v[204:207], a[172:175], v[100:103]
v_mfma_f32_16x16x32_bf16 v[108:111], v[204:207], a[188:191], v[108:111]
s_waitcnt vmcnt(16) lgkmcnt(0)
s_barrier
v_mfma_f32_16x16x32_bf16 a[0:3], v[144:147], v[112:115], a[0:3]
ds_read_b128 v[176:179], v16 offset:16384
v_mfma_f32_16x16x32_bf16 a[4:7], v[144:147], v[120:123], a[4:7]
ds_read_b128 v[180:183], v16 offset:17408
v_mfma_f32_16x16x32_bf16 a[8:11], v[144:147], v[128:131], a[8:11]
ds_read_b128 v[184:187], v16 offset:18432
v_mfma_f32_16x16x32_bf16 a[12:15], v[144:147], v[136:139], a[12:15]
v_fma_f32 v80, v40, s64, v80
ds_read_b128 v[188:191], v16 offset:19456
v_mfma_f32_16x16x32_bf16 a[16:19], v[148:151], v[112:115], a[16:19]
v_fma_f32 v81, v41, s64, v81
ds_read_b128 v[192:195], v16 offset:20480
v_mfma_f32_16x16x32_bf16 a[20:23], v[148:151], v[120:123], a[20:23]
v_exp_f32 v80, v80
ds_read_b128 v[196:199], v16 offset:21504
v_mfma_f32_16x16x32_bf16 a[24:27], v[148:151], v[128:131], a[24:27]
v_fma_f32 v82, v42, s64, v82
ds_read_b128 v[200:203], v16 offset:22528
v_mfma_f32_16x16x32_bf16 a[28:31], v[148:151], v[136:139], a[28:31]
v_exp_f32 v81, v81
ds_read_b128 v[204:207], v16 offset:23552
v_mfma_f32_16x16x32_bf16 a[32:35], v[152:155], v[112:115], a[32:35]
v_fma_f32 v83, v43, s64, v83
v_xor_b32 v16, 32768, v16
v_mfma_f32_16x16x32_bf16 a[36:39], v[152:155], v[120:123], a[36:39]
v_exp_f32 v82, v82
s_add_u32 m0, s95, 16384
v_mfma_f32_16x16x32_bf16 a[40:43], v[152:155], v[128:131], a[40:43]
buffer_load_dwordx4 v3, s[8:11], s93 offen offset:0 sc1 lds
v_mfma_f32_16x16x32_bf16 a[44:47], v[152:155], v[136:139], a[44:47]
v_fma_f32 v84, v40, s65, v84
v_mfma_f32_16x16x32_bf16 a[48:51], v[156:159], v[112:115], a[48:51]
v_exp_f32 v83, v83
v_fma_f32 v85, v41, s65, v85
v_mfma_f32_16x16x32_bf16 a[52:55], v[156:159], v[120:123], a[52:55]
v_cvt_pk_bf16_f32 v80, v80, v81
v_exp_f32 v84, v84
v_mfma_f32_16x16x32_bf16 a[56:59], v[156:159], v[128:131], a[56:59]
v_fma_f32 v86, v42, s65, v86
v_exp_f32 v85, v85
v_mfma_f32_16x16x32_bf16 a[60:63], v[156:159], v[136:139], a[60:63]
v_fma_f32 v87, v43, s65, v87
v_cvt_pk_bf16_f32 v81, v82, v83
v_mfma_f32_16x16x32_bf16 a[64:67], v[160:163], v[112:115], a[64:67]
buffer_load_dwordx4 v3, s[8:11], s93 offen offset:1024 sc1 lds
v_mfma_f32_16x16x32_bf16 a[68:71], v[160:163], v[120:123], a[68:71]
v_exp_f32 v86, v86
v_mfma_f32_16x16x32_bf16 a[72:75], v[160:163], v[128:131], a[72:75]
v_fma_f32 v88, v40, s66, v88
v_exp_f32 v87, v87
v_mfma_f32_16x16x32_bf16 a[76:79], v[160:163], v[136:139], a[76:79]
v_fma_f32 v89, v41, s66, v89
v_cvt_pk_bf16_f32 v82, v84, v85
v_mfma_f32_16x16x32_bf16 a[80:83], v[164:167], v[112:115], a[80:83]
v_exp_f32 v88, v88
v_fma_f32 v90, v42, s66, v90
v_mfma_f32_16x16x32_bf16 a[84:87], v[164:167], v[120:123], a[84:87]
v_exp_f32 v89, v89
v_mfma_f32_16x16x32_bf16 a[88:91], v[164:167], v[128:131], a[88:91]
buffer_load_dwordx4 v3, s[8:11], s93 offen offset:2048 sc1 lds
v_mfma_f32_16x16x32_bf16 a[92:95], v[164:167], v[136:139], a[92:95]
v_fma_f32 v91, v43, s66, v91
v_cvt_pk_bf16_f32 v83, v86, v87
v_mfma_f32_16x16x32_bf16 a[96:99], v[168:171], v[112:115], a[96:99]
v_exp_f32 v90, v90
v_fma_f32 v92, v40, s67, v92
v_mfma_f32_16x16x32_bf16 a[100:103], v[168:171], v[120:123], a[100:103]
v_exp_f32 v91, v91
v_mfma_f32_16x16x32_bf16 a[104:107], v[168:171], v[128:131], a[104:107]
v_fma_f32 v93, v41, s67, v93
v_cvt_pk_bf16_f32 v88, v88, v89
v_mfma_f32_16x16x32_bf16 a[108:111], v[168:171], v[136:139], a[108:111]
v_exp_f32 v92, v92
v_fma_f32 v94, v42, s67, v94
v_mfma_f32_16x16x32_bf16 a[112:115], v[172:175], v[112:115], a[112:115]
buffer_load_dwordx4 v3, s[8:11], s93 offen offset:3072 sc1 lds
v_mfma_f32_16x16x32_bf16 a[116:119], v[172:175], v[120:123], a[116:119]
s_add_u32 s93, s93, 24576
s_cmp_eq_u32 s93, s53
v_mfma_f32_16x16x32_bf16 a[120:123], v[172:175], v[128:131], a[120:123]
s_cselect_b32 s93, s14, s93
s_cselect_b32 s53, -1, s53
v_mfma_f32_16x16x32_bf16 a[124:127], v[172:175], v[136:139], a[124:127]
v_exp_f32 v93, v93
s_xor_b32 s95, s95, 32768
v_mfma_f32_16x16x32_bf16 v[64:67], v[44:47], v[112:115], v[64:67]
v_fma_f32 v95, v43, s67, v95
v_cvt_pk_bf16_f32 v89, v90, v91
v_mfma_f32_16x16x32_bf16 v[68:71], v[44:47], v[120:123], v[68:71]
v_exp_f32 v94, v94
v_fma_f32 v96, v40, s68, v96
v_mfma_f32_16x16x32_bf16 v[72:75], v[44:47], v[128:131], v[72:75]
v_exp_f32 v95, v95
v_fma_f32 v97, v41, s68, v97
v_mfma_f32_16x16x32_bf16 v[76:79], v[44:47], v[136:139], v[76:79]
v_cvt_pk_bf16_f32 v90, v92, v93
s_waitcnt lgkmcnt(0)
v_mfma_f32_16x16x32_bf16 v[112:115], v[176:179], a[128:131], v[48:51]
ds_read_b128 v[144:147], v19 offset:0
v_mfma_f32_16x16x32_bf16 v[120:123], v[176:179], a[144:147], v[52:55]
v_exp_f32 v96, v96
ds_read_b128 v[148:151], v19 offset:1024
v_mfma_f32_16x16x32_bf16 v[128:131], v[176:179], a[160:163], v[56:59]
v_fma_f32 v98, v42, s68, v98
ds_read_b128 v[152:155], v19 offset:2048
v_mfma_f32_16x16x32_bf16 v[136:139], v[176:179], a[176:179], v[60:63]
v_exp_f32 v97, v97
ds_read_b128 v[156:159], v19 offset:3072
v_mfma_f32_16x16x32_bf16 v[116:119], v[180:183], a[128:131], v[48:51]
)ASM"
R"ASM(
v_fma_f32 v99, v43, s68, v99
v_cvt_pk_bf16_f32 v91, v94, v95
ds_read_b128 v[160:163], v19 offset:4096
v_mfma_f32_16x16x32_bf16 v[124:127], v[180:183], a[144:147], v[52:55]
v_exp_f32 v98, v98
ds_read_b128 v[164:167], v19 offset:5120
v_mfma_f32_16x16x32_bf16 v[132:135], v[180:183], a[160:163], v[56:59]
v_fma_f32 v100, v40, s69, v100
ds_read_b128 v[168:171], v19 offset:6144
v_mfma_f32_16x16x32_bf16 v[140:143], v[180:183], a[176:179], v[60:63]
v_exp_f32 v99, v99
ds_read_b128 v[172:175], v19 offset:7168
v_mfma_f32_16x16x32_bf16 v[112:115], v[184:187], a[132:135], v[112:115]
v_fma_f32 v101, v41, s69, v101
v_cvt_pk_bf16_f32 v96, v96, v97
v_mfma_f32_16x16x32_bf16 v[120:123], v[184:187], a[148:151], v[120:123]
v_exp_f32 v100, v100
v_fma_f32 v102, v42, s69, v102
v_mfma_f32_16x16x32_bf16 v[128:131], v[184:187], a[164:167], v[128:131]
v_exp_f32 v101, v101
v_mfma_f32_16x16x32_bf16 v[136:139], v[184:187], a[180:183], v[136:139]
v_fma_f32 v103, v43, s69, v103
v_cvt_pk_bf16_f32 v97, v98, v99
s_mov_b32 m0, s94
v_mfma_f32_16x16x32_bf16 v[116:119], v[188:191], a[132:135], v[116:119]
buffer_load_dwordx4 v3, s[8:11], s92 offen offset:0 sc1 lds
v_mfma_f32_16x16x32_bf16 v[124:127], v[188:191], a[148:151], v[124:127]
v_exp_f32 v102, v102
v_mfma_f32_16x16x32_bf16 v[132:135], v[188:191], a[164:167], v[132:135]
v_fma_f32 v104, v40, s70, v104
v_mfma_f32_16x16x32_bf16 v[140:143], v[188:191], a[180:183], v[140:143]
v_exp_f32 v103, v103
v_fma_f32 v105, v41, s70, v105
v_mfma_f32_16x16x32_bf16 v[112:115], v[192:195], a[136:139], v[112:115]
v_cvt_pk_bf16_f32 v98, v100, v101
v_exp_f32 v104, v104
v_mfma_f32_16x16x32_bf16 v[120:123], v[192:195], a[152:155], v[120:123]
buffer_load_dwordx4 v3, s[8:11], s92 offen offset:1024 sc1 lds
v_mfma_f32_16x16x32_bf16 v[128:131], v[192:195], a[168:171], v[128:131]
v_fma_f32 v106, v42, s70, v106
v_mfma_f32_16x16x32_bf16 v[136:139], v[192:195], a[184:187], v[136:139]
v_exp_f32 v105, v105
v_fma_f32 v107, v43, s70, v107
v_mfma_f32_16x16x32_bf16 v[116:119], v[196:199], a[136:139], v[116:119]
v_cvt_pk_bf16_f32 v99, v102, v103
v_exp_f32 v106, v106
v_mfma_f32_16x16x32_bf16 v[124:127], v[196:199], a[152:155], v[124:127]
v_fma_f32 v108, v40, s71, v108
v_exp_f32 v107, v107
v_mfma_f32_16x16x32_bf16 v[132:135], v[196:199], a[168:171], v[132:135]
buffer_load_dwordx4 v3, s[8:11], s92 offen offset:2048 sc1 lds
v_mfma_f32_16x16x32_bf16 v[140:143], v[196:199], a[184:187], v[140:143]
v_fma_f32 v109, v41, s71, v109
v_cvt_pk_bf16_f32 v104, v104, v105
v_mfma_f32_16x16x32_bf16 v[112:115], v[200:203], a[140:143], v[112:115]
v_exp_f32 v108, v108
v_mfma_f32_16x16x32_bf16 v[120:123], v[200:203], a[156:159], v[120:123]
v_fma_f32 v110, v42, s71, v110
v_exp_f32 v109, v109
v_mfma_f32_16x16x32_bf16 v[128:131], v[200:203], a[172:175], v[128:131]
v_fma_f32 v111, v43, s71, v111
v_cvt_pk_bf16_f32 v105, v106, v107
v_mfma_f32_16x16x32_bf16 v[136:139], v[200:203], a[188:191], v[136:139]
buffer_load_dwordx4 v3, s[8:11], s92 offen offset:3072 sc1 lds
v_mfma_f32_16x16x32_bf16 v[116:119], v[204:207], a[140:143], v[116:119]
s_add_u32 s92, s92, 8192
v_mfma_f32_16x16x32_bf16 v[124:127], v[204:207], a[156:159], v[124:127]
v_exp_f32 v110, v110
v_mfma_f32_16x16x32_bf16 v[132:135], v[204:207], a[172:175], v[132:135]
v_exp_f32 v111, v111
v_cvt_pk_bf16_f32 v106, v108, v109
v_cvt_pk_bf16_f32 v107, v110, v111
v_mfma_f32_16x16x32_bf16 v[140:143], v[204:207], a[188:191], v[140:143]
s_waitcnt vmcnt(16) lgkmcnt(0)
s_barrier
.LT1_%=:
v_mfma_f32_16x16x32_bf16 a[0:3], v[144:147], v[80:83], a[0:3]
ds_read_b128 v[176:179], v16 offset:0
v_mfma_f32_16x16x32_bf16 a[4:7], v[144:147], v[88:91], a[4:7]
ds_read_b128 v[180:183], v16 offset:1024
v_mfma_f32_16x16x32_bf16 a[8:11], v[144:147], v[96:99], a[8:11]
ds_read_b128 v[184:187], v16 offset:2048
v_mfma_f32_16x16x32_bf16 a[12:15], v[144:147], v[104:107], a[12:15]
v_fma_f32 v112, v40, s72, v112
ds_read_b128 v[188:191], v16 offset:3072
v_mfma_f32_16x16x32_bf16 a[16:19], v[148:151], v[80:83], a[16:19]
v_fma_f32 v113, v41, s72, v113
ds_read_b128 v[192:195], v16 offset:4096
v_mfma_f32_16x16x32_bf16 a[20:23], v[148:151], v[88:91], a[20:23]
v_exp_f32 v112, v112
ds_read_b128 v[196:199], v16 offset:5120
v_mfma_f32_16x16x32_bf16 a[24:27], v[148:151], v[96:99], a[24:27]
v_fma_f32 v114, v42, s72, v114
ds_read_b128 v[200:203], v16 offset:6144
v_mfma_f32_16x16x32_bf16 a[28:31], v[148:151], v[104:107], a[28:31]
v_exp_f32 v113, v113
ds_read_b128 v[204:207], v16 offset:7168
v_mfma_f32_16x16x32_bf16 a[32:35], v[152:155], v[80:83], a[32:35]
v_fma_f32 v115, v43, s72, v115
v_exp_f32 v114, v114
v_mfma_f32_16x16x32_bf16 a[36:39], v[152:155], v[88:91], a[36:39]
v_fma_f32 v116, v40, s73, v116
s_mov_b32 m0, s95
v_mfma_f32_16x16x32_bf16 a[40:43], v[152:155], v[96:99], a[40:43]
buffer_load_dwordx4 v3, s[8:11], s93 offen offset:0 sc1 lds
v_mfma_f32_16x16x32_bf16 a[44:47], v[152:155], v[104:107], a[44:47]
v_exp_f32 v115, v115
v_mfma_f32_16x16x32_bf16 a[48:51], v[156:159], v[80:83], a[48:51]
v_fma_f32 v117, v41, s73, v117
v_cvt_pk_bf16_f32 v112, v112, v113
v_mfma_f32_16x16x32_bf16 a[52:55], v[156:159], v[88:91], a[52:55]
v_exp_f32 v116, v116
v_fma_f32 v118, v42, s73, v118
v_mfma_f32_16x16x32_bf16 a[56:59], v[156:159], v[96:99], a[56:59]
v_exp_f32 v117, v117
v_fma_f32 v119, v43, s73, v119
v_mfma_f32_16x16x32_bf16 a[60:63], v[156:159], v[104:107], a[60:63]
v_cvt_pk_bf16_f32 v113, v114, v115
v_mfma_f32_16x16x32_bf16 a[64:67], v[160:163], v[80:83], a[64:67]
buffer_load_dwordx4 v3, s[8:11], s93 offen offset:1024 sc1 lds
v_mfma_f32_16x16x32_bf16 a[68:71], v[160:163], v[88:91], a[68:71]
v_exp_f32 v118, v118
v_fma_f32 v120, v40, s74, v120
v_mfma_f32_16x16x32_bf16 a[72:75], v[160:163], v[96:99], a[72:75]
v_exp_f32 v119, v119
v_fma_f32 v121, v41, s74, v121
v_mfma_f32_16x16x32_bf16 a[76:79], v[160:163], v[104:107], a[76:79]
v_cvt_pk_bf16_f32 v114, v116, v117
v_mfma_f32_16x16x32_bf16 a[80:83], v[164:167], v[80:83], a[80:83]
v_exp_f32 v120, v120
v_fma_f32 v122, v42, s74, v122
v_mfma_f32_16x16x32_bf16 a[84:87], v[164:167], v[88:91], a[84:87]
v_exp_f32 v121, v121
v_fma_f32 v123, v43, s74, v123
v_mfma_f32_16x16x32_bf16 a[88:91], v[164:167], v[96:99], a[88:91]
buffer_load_dwordx4 v3, s[8:11], s93 offen offset:2048 sc1 lds
v_mfma_f32_16x16x32_bf16 a[92:95], v[164:167], v[104:107], a[92:95]
v_cvt_pk_bf16_f32 v115, v118, v119
v_mfma_f32_16x16x32_bf16 a[96:99], v[168:171], v[80:83], a[96:99]
v_exp_f32 v122, v122
v_fma_f32 v124, v40, s75, v124
v_mfma_f32_16x16x32_bf16 a[100:103], v[168:171], v[88:91], a[100:103]
v_exp_f32 v123, v123
v_fma_f32 v125, v41, s75, v125
v_mfma_f32_16x16x32_bf16 a[104:107], v[168:171], v[96:99], a[104:107]
v_cvt_pk_bf16_f32 v120, v120, v121
v_exp_f32 v124, v124
v_mfma_f32_16x16x32_bf16 a[108:111], v[168:171], v[104:107], a[108:111]
v_fma_f32 v126, v42, s75, v126
v_mfma_f32_16x16x32_bf16 a[112:115], v[172:175], v[80:83], a[112:115]
buffer_load_dwordx4 v3, s[8:11], s93 offen offset:3072 sc1 lds
v_mfma_f32_16x16x32_bf16 a[116:119], v[172:175], v[88:91], a[116:119]
v_exp_f32 v125, v125
s_add_u32 s93, s93, 8192
v_mfma_f32_16x16x32_bf16 a[120:123], v[172:175], v[96:99], a[120:123]
v_fma_f32 v127, v43, s75, v127
v_cvt_pk_bf16_f32 v121, v122, v123
v_mfma_f32_16x16x32_bf16 a[124:127], v[172:175], v[104:107], a[124:127]
v_exp_f32 v126, v126
v_fma_f32 v128, v40, s76, v128
v_mfma_f32_16x16x32_bf16 v[64:67], v[44:47], v[80:83], v[64:67]
v_exp_f32 v127, v127
v_fma_f32 v129, v41, s76, v129
v_mfma_f32_16x16x32_bf16 v[68:71], v[44:47], v[88:91], v[68:71]
v_cvt_pk_bf16_f32 v122, v124, v125
v_mfma_f32_16x16x32_bf16 v[72:75], v[44:47], v[96:99], v[72:75]
v_exp_f32 v128, v128
v_fma_f32 v130, v42, s76, v130
v_mfma_f32_16x16x32_bf16 v[76:79], v[44:47], v[104:107], v[76:79]
v_exp_f32 v129, v129
s_waitcnt lgkmcnt(0)
v_mfma_f32_16x16x32_bf16 v[80:83], v[176:179], a[192:195], 0
v_fma_f32 v131, v43, s76, v131
ds_read_b128 v[144:147], v19 offset:16384
v_mfma_f32_16x16x32_bf16 v[88:91], v[176:179], a[208:211], 0
v_cvt_pk_bf16_f32 v123, v126, v127
ds_read_b128 v[148:151], v19 offset:17408
v_mfma_f32_16x16x32_bf16 v[96:99], v[176:179], a[224:227], 0
v_exp_f32 v130, v130
ds_read_b128 v[152:155], v19 offset:18432
v_mfma_f32_16x16x32_bf16 v[104:107], v[176:179], a[240:243], 0
v_fma_f32 v132, v40, s77, v132
ds_read_b128 v[156:159], v19 offset:19456
v_mfma_f32_16x16x32_bf16 v[84:87], v[180:183], a[192:195], 0
v_exp_f32 v131, v131
ds_read_b128 v[160:163], v19 offset:20480
v_mfma_f32_16x16x32_bf16 v[92:95], v[180:183], a[208:211], 0
v_fma_f32 v133, v41, s77, v133
v_cvt_pk_bf16_f32 v128, v128, v129
ds_read_b128 v[164:167], v19 offset:21504
v_mfma_f32_16x16x32_bf16 v[100:103], v[180:183], a[224:227], 0
ds_read_b128 v[168:171], v19 offset:22528
v_mfma_f32_16x16x32_bf16 v[108:111], v[180:183], a[240:243], 0
v_exp_f32 v132, v132
ds_read_b128 v[172:175], v19 offset:23552
v_mfma_f32_16x16x32_bf16 v[80:83], v[184:187], a[196:199], v[80:83]
v_fma_f32 v134, v42, s77, v134
v_xor_b32 v19, 32768, v19
v_mfma_f32_16x16x32_bf16 v[88:91], v[184:187], a[212:215], v[88:91]
v_exp_f32 v133, v133
)ASM"
R"ASM(
v_fma_f32 v135, v43, s77, v135
v_mfma_f32_16x16x32_bf16 v[96:99], v[184:187], a[228:231], v[96:99]
v_cvt_pk_bf16_f32 v129, v130, v131
v_exp_f32 v134, v134
v_mfma_f32_16x16x32_bf16 v[104:107], v[184:187], a[244:247], v[104:107]
v_fma_f32 v136, v40, s78, v136
s_add_u32 m0, s94, 16384
v_mfma_f32_16x16x32_bf16 v[84:87], v[188:191], a[196:199], v[84:87]
buffer_load_dwordx4 v3, s[8:11], s92 offen offset:0 sc1 lds
v_mfma_f32_16x16x32_bf16 v[92:95], v[188:191], a[212:215], v[92:95]
v_exp_f32 v135, v135
v_fma_f32 v137, v41, s78, v137
v_mfma_f32_16x16x32_bf16 v[100:103], v[188:191], a[228:231], v[100:103]
v_cvt_pk_bf16_f32 v130, v132, v133
v_mfma_f32_16x16x32_bf16 v[108:111], v[188:191], a[244:247], v[108:111]
v_exp_f32 v136, v136
v_fma_f32 v138, v42, s78, v138
v_mfma_f32_16x16x32_bf16 v[80:83], v[192:195], a[200:203], v[80:83]
v_exp_f32 v137, v137
v_fma_f32 v139, v43, s78, v139
v_mfma_f32_16x16x32_bf16 v[88:91], v[192:195], a[216:219], v[88:91]
buffer_load_dwordx4 v3, s[8:11], s92 offen offset:1024 sc1 lds
v_mfma_f32_16x16x32_bf16 v[96:99], v[192:195], a[232:235], v[96:99]
v_cvt_pk_bf16_f32 v131, v134, v135
v_mfma_f32_16x16x32_bf16 v[104:107], v[192:195], a[248:251], v[104:107]
v_exp_f32 v138, v138
v_fma_f32 v140, v40, s79, v140
v_mfma_f32_16x16x32_bf16 v[84:87], v[196:199], a[200:203], v[84:87]
v_exp_f32 v139, v139
v_fma_f32 v141, v41, s79, v141
v_mfma_f32_16x16x32_bf16 v[92:95], v[196:199], a[216:219], v[92:95]
v_cvt_pk_bf16_f32 v136, v136, v137
v_exp_f32 v140, v140
v_mfma_f32_16x16x32_bf16 v[100:103], v[196:199], a[232:235], v[100:103]
buffer_load_dwordx4 v3, s[8:11], s92 offen offset:2048 sc1 lds
v_mfma_f32_16x16x32_bf16 v[108:111], v[196:199], a[248:251], v[108:111]
v_fma_f32 v142, v42, s79, v142
v_mfma_f32_16x16x32_bf16 v[80:83], v[200:203], a[204:207], v[80:83]
v_exp_f32 v141, v141
v_fma_f32 v143, v43, s79, v143
v_mfma_f32_16x16x32_bf16 v[88:91], v[200:203], a[220:223], v[88:91]
v_cvt_pk_bf16_f32 v137, v138, v139
v_exp_f32 v142, v142
v_mfma_f32_16x16x32_bf16 v[96:99], v[200:203], a[236:239], v[96:99]
v_exp_f32 v143, v143
v_mfma_f32_16x16x32_bf16 v[104:107], v[200:203], a[252:255], v[104:107]
buffer_load_dwordx4 v3, s[8:11], s92 offen offset:3072 sc1 lds
v_mfma_f32_16x16x32_bf16 v[84:87], v[204:207], a[204:207], v[84:87]
s_add_u32 s92, s92, 24576
s_cmp_eq_u32 s92, s52
v_mfma_f32_16x16x32_bf16 v[92:95], v[204:207], a[220:223], v[92:95]
s_cselect_b32 s92, s82, s92
s_cselect_b32 s52, -1, s52
s_xor_b32 s94, s94, 32768
v_mfma_f32_16x16x32_bf16 v[100:103], v[204:207], a[236:239], v[100:103]
v_cvt_pk_bf16_f32 v138, v140, v141
v_cvt_pk_bf16_f32 v139, v142, v143
v_mfma_f32_16x16x32_bf16 v[108:111], v[204:207], a[252:255], v[108:111]
s_waitcnt vmcnt(16) lgkmcnt(0)
s_barrier
v_mfma_f32_16x16x32_bf16 a[0:3], v[144:147], v[112:115], a[0:3]
ds_read_b128 v[176:179], v16 offset:16384
v_mfma_f32_16x16x32_bf16 a[4:7], v[144:147], v[120:123], a[4:7]
ds_read_b128 v[180:183], v16 offset:17408
v_mfma_f32_16x16x32_bf16 a[8:11], v[144:147], v[128:131], a[8:11]
ds_read_b128 v[184:187], v16 offset:18432
v_mfma_f32_16x16x32_bf16 a[12:15], v[144:147], v[136:139], a[12:15]
v_mov_b32 v24, v20
v_mov_b32 v25, v21
ds_read_b128 v[188:191], v16 offset:19456
v_mfma_f32_16x16x32_bf16 a[16:19], v[148:151], v[112:115], a[16:19]
v_mov_b32 v26, v22
v_mov_b32 v27, v23
v_max3_f32 v20, v80, v81, v82
ds_read_b128 v[192:195], v16 offset:20480
v_mfma_f32_16x16x32_bf16 a[20:23], v[148:151], v[120:123], a[20:23]
v_max3_f32 v21, v88, v89, v90
v_max3_f32 v22, v96, v97, v98
ds_read_b128 v[196:199], v16 offset:21504
v_mfma_f32_16x16x32_bf16 a[24:27], v[148:151], v[128:131], a[24:27]
v_max3_f32 v23, v104, v105, v106
v_max3_f32 v20, v20, v83, v84
ds_read_b128 v[200:203], v16 offset:22528
v_mfma_f32_16x16x32_bf16 a[28:31], v[148:151], v[136:139], a[28:31]
v_max3_f32 v21, v21, v91, v92
v_max3_f32 v22, v22, v99, v100
v_max3_f32 v23, v23, v107, v108
ds_read_b128 v[204:207], v16 offset:23552
v_mfma_f32_16x16x32_bf16 a[32:35], v[152:155], v[112:115], a[32:35]
v_max3_f32 v20, v20, v85, v86
v_max3_f32 v21, v21, v93, v94
v_xor_b32 v16, 32768, v16
v_mfma_f32_16x16x32_bf16 a[36:39], v[152:155], v[120:123], a[36:39]
v_max3_f32 v22, v22, v101, v102
v_max3_f32 v23, v23, v109, v110
s_add_u32 m0, s95, 16384
v_mfma_f32_16x16x32_bf16 a[40:43], v[152:155], v[128:131], a[40:43]
buffer_load_dwordx4 v3, s[8:11], s93 offen offset:0 sc1 lds
v_mfma_f32_16x16x32_bf16 a[44:47], v[152:155], v[136:139], a[44:47]
v_max_f32 v20, v20, v87
v_max_f32 v21, v21, v95
v_max_f32 v22, v22, v103
v_max_f32 v23, v23, v111
v_mfma_f32_16x16x32_bf16 a[48:51], v[156:159], v[112:115], a[48:51]
v_mov_b32 v28, v20
v_mov_b32 v29, v21
v_mov_b32 v30, v22
v_mfma_f32_16x16x32_bf16 a[52:55], v[156:159], v[120:123], a[52:55]
v_mov_b32 v31, v23
v_permlane16_swap_b32 v20, v28
v_permlane16_swap_b32 v21, v29
v_mfma_f32_16x16x32_bf16 a[56:59], v[156:159], v[128:131], a[56:59]
v_permlane16_swap_b32 v22, v30
v_permlane16_swap_b32 v23, v31
v_max_f32 v20, v20, v28
v_max_f32 v21, v21, v29
v_mfma_f32_16x16x32_bf16 a[60:63], v[156:159], v[136:139], a[60:63]
v_max_f32 v22, v22, v30
v_max_f32 v23, v23, v31
v_mov_b32 v28, v20
v_mfma_f32_16x16x32_bf16 a[64:67], v[160:163], v[112:115], a[64:67]
buffer_load_dwordx4 v3, s[8:11], s93 offen offset:1024 sc1 lds
v_mfma_f32_16x16x32_bf16 a[68:71], v[160:163], v[120:123], a[68:71]
v_mov_b32 v29, v21
v_mov_b32 v30, v22
v_mov_b32 v31, v23
v_permlane32_swap_b32 v20, v28
v_mfma_f32_16x16x32_bf16 a[72:75], v[160:163], v[128:131], a[72:75]
v_permlane32_swap_b32 v21, v29
v_permlane32_swap_b32 v22, v30
v_permlane32_swap_b32 v23, v31
v_mfma_f32_16x16x32_bf16 a[76:79], v[160:163], v[136:139], a[76:79]
v_max_f32 v20, v20, v28
v_max_f32 v21, v21, v29
v_max_f32 v22, v22, v30
v_mfma_f32_16x16x32_bf16 a[80:83], v[164:167], v[112:115], a[80:83]
v_max_f32 v23, v23, v31
v_sub_f32 v48, 0, v20
v_sub_f32 v49, 0, v20
v_mfma_f32_16x16x32_bf16 a[84:87], v[164:167], v[120:123], a[84:87]
v_sub_f32 v50, 0, v20
v_sub_f32 v51, 0, v20
v_sub_f32 v52, 0, v21
v_sub_f32 v53, 0, v21
v_mfma_f32_16x16x32_bf16 a[88:91], v[164:167], v[128:131], a[88:91]
buffer_load_dwordx4 v3, s[8:11], s93 offen offset:2048 sc1 lds
v_mfma_f32_16x16x32_bf16 a[92:95], v[164:167], v[136:139], a[92:95]
v_sub_f32 v54, 0, v21
v_sub_f32 v55, 0, v21
v_sub_f32 v56, 0, v22
v_mfma_f32_16x16x32_bf16 a[96:99], v[168:171], v[112:115], a[96:99]
v_sub_f32 v57, 0, v22
v_sub_f32 v58, 0, v22
v_sub_f32 v59, 0, v22
v_sub_f32 v60, 0, v23
v_mfma_f32_16x16x32_bf16 a[100:103], v[168:171], v[120:123], a[100:103]
v_sub_f32 v61, 0, v23
v_sub_f32 v62, 0, v23
v_sub_f32 v63, 0, v23
v_mfma_f32_16x16x32_bf16 a[104:107], v[168:171], v[128:131], a[104:107]
v_sub_f32 v80, v80, v20
v_sub_f32 v81, v81, v20
v_exp_f32 v80, v80
v_mfma_f32_16x16x32_bf16 a[108:111], v[168:171], v[136:139], a[108:111]
v_sub_f32 v82, v82, v20
v_exp_f32 v81, v81
v_mfma_f32_16x16x32_bf16 a[112:115], v[172:175], v[112:115], a[112:115]
v_sub_f32 v83, v83, v20
buffer_load_dwordx4 v3, s[8:11], s93 offen offset:3072 sc1 lds
v_mfma_f32_16x16x32_bf16 a[116:119], v[172:175], v[120:123], a[116:119]
v_exp_f32 v82, v82
s_add_u32 s93, s93, 24576
s_cmp_eq_u32 s93, s53
v_mfma_f32_16x16x32_bf16 a[120:123], v[172:175], v[128:131], a[120:123]
v_sub_f32 v84, v84, v20
s_cselect_b32 s93, s14, s93
s_cselect_b32 s53, -1, s53
v_mfma_f32_16x16x32_bf16 a[124:127], v[172:175], v[136:139], a[124:127]
v_exp_f32 v83, v83
v_sub_f32 v85, v85, v20
s_xor_b32 s95, s95, 32768
v_mfma_f32_16x16x32_bf16 v[64:67], v[44:47], v[112:115], v[64:67]
v_cvt_pk_bf16_f32 v80, v80, v81
v_exp_f32 v84, v84
v_mfma_f32_16x16x32_bf16 v[68:71], v[44:47], v[120:123], v[68:71]
s_add_u32 s98, s100, 2
v_mov_b32 v32, s98
v_mov_b32 v33, 0
buffer_store_dword v32, v33, s[8:11], s84 offen sc1
v_mfma_f32_16x16x32_bf16 v[72:75], v[44:47], v[128:131], v[72:75]
v_sub_f32 v86, v86, v20
v_mfma_f32_16x16x32_bf16 v[76:79], v[44:47], v[136:139], v[76:79]
v_exp_f32 v85, v85
v_sub_f32 v87, v87, v20
s_waitcnt lgkmcnt(0)
v_mfma_f32_16x16x32_bf16 v[112:115], v[176:179], a[192:195], v[48:51]
v_cvt_pk_bf16_f32 v81, v82, v83
v_exp_f32 v86, v86
ds_read_b128 v[144:147], v19 offset:0
v_mfma_f32_16x16x32_bf16 v[120:123], v[176:179], a[208:211], v[52:55]
)ASM"
R"ASM(
v_sub_f32 v88, v88, v21
v_exp_f32 v87, v87
ds_read_b128 v[148:151], v19 offset:1024
v_mfma_f32_16x16x32_bf16 v[128:131], v[176:179], a[224:227], v[56:59]
v_sub_f32 v89, v89, v21
v_cvt_pk_bf16_f32 v82, v84, v85
ds_read_b128 v[152:155], v19 offset:2048
v_mfma_f32_16x16x32_bf16 v[136:139], v[176:179], a[240:243], v[60:63]
v_exp_f32 v88, v88
v_sub_f32 v90, v90, v21
ds_read_b128 v[156:159], v19 offset:3072
v_mfma_f32_16x16x32_bf16 v[116:119], v[180:183], a[192:195], v[48:51]
v_exp_f32 v89, v89
v_sub_f32 v91, v91, v21
ds_read_b128 v[160:163], v19 offset:4096
v_mfma_f32_16x16x32_bf16 v[124:127], v[180:183], a[208:211], v[52:55]
v_cvt_pk_bf16_f32 v83, v86, v87
ds_read_b128 v[164:167], v19 offset:5120
v_mfma_f32_16x16x32_bf16 v[132:135], v[180:183], a[224:227], v[56:59]
v_exp_f32 v90, v90
v_sub_f32 v92, v92, v21
ds_read_b128 v[168:171], v19 offset:6144
v_mfma_f32_16x16x32_bf16 v[140:143], v[180:183], a[240:243], v[60:63]
v_exp_f32 v91, v91
v_sub_f32 v93, v93, v21
ds_read_b128 v[172:175], v19 offset:7168
v_mfma_f32_16x16x32_bf16 v[112:115], v[184:187], a[196:199], v[112:115]
v_cvt_pk_bf16_f32 v88, v88, v89
v_exp_f32 v92, v92
v_sub_f32 v94, v94, v21
v_mfma_f32_16x16x32_bf16 v[120:123], v[184:187], a[212:215], v[120:123]
v_exp_f32 v93, v93
v_sub_f32 v95, v95, v21
v_cvt_pk_bf16_f32 v89, v90, v91
v_mfma_f32_16x16x32_bf16 v[128:131], v[184:187], a[228:231], v[128:131]
v_exp_f32 v94, v94
v_sub_f32 v96, v96, v22
v_mfma_f32_16x16x32_bf16 v[136:139], v[184:187], a[244:247], v[136:139]
v_exp_f32 v95, v95
v_sub_f32 v97, v97, v22
s_mov_b32 m0, s94
v_mfma_f32_16x16x32_bf16 v[116:119], v[188:191], a[196:199], v[116:119]
v_cvt_pk_bf16_f32 v90, v92, v93
buffer_load_dwordx4 v3, s[8:11], s92 offen offset:0 sc1 lds
v_mfma_f32_16x16x32_bf16 v[124:127], v[188:191], a[212:215], v[124:127]
v_exp_f32 v96, v96
v_sub_f32 v98, v98, v22
v_mfma_f32_16x16x32_bf16 v[132:135], v[188:191], a[228:231], v[132:135]
v_exp_f32 v97, v97
v_sub_f32 v99, v99, v22
v_cvt_pk_bf16_f32 v91, v94, v95
v_mfma_f32_16x16x32_bf16 v[140:143], v[188:191], a[244:247], v[140:143]
v_exp_f32 v98, v98
v_sub_f32 v100, v100, v22
v_mfma_f32_16x16x32_bf16 v[112:115], v[192:195], a[200:203], v[112:115]
v_exp_f32 v99, v99
v_sub_f32 v101, v101, v22
v_cvt_pk_bf16_f32 v96, v96, v97
v_mfma_f32_16x16x32_bf16 v[120:123], v[192:195], a[216:219], v[120:123]
buffer_load_dwordx4 v3, s[8:11], s92 offen offset:1024 sc1 lds
v_mfma_f32_16x16x32_bf16 v[128:131], v[192:195], a[232:235], v[128:131]
v_exp_f32 v100, v100
v_sub_f32 v102, v102, v22
v_mfma_f32_16x16x32_bf16 v[136:139], v[192:195], a[248:251], v[136:139]
v_exp_f32 v101, v101
v_sub_f32 v103, v103, v22
v_cvt_pk_bf16_f32 v97, v98, v99
v_mfma_f32_16x16x32_bf16 v[116:119], v[196:199], a[200:203], v[116:119]
v_exp_f32 v102, v102
v_sub_f32 v104, v104, v23
v_exp_f32 v103, v103
v_mfma_f32_16x16x32_bf16 v[124:127], v[196:199], a[216:219], v[124:127]
v_sub_f32 v105, v105, v23
v_cvt_pk_bf16_f32 v98, v100, v101
v_mfma_f32_16x16x32_bf16 v[132:135], v[196:199], a[232:235], v[132:135]
v_exp_f32 v104, v104
buffer_load_dwordx4 v3, s[8:11], s92 offen offset:2048 sc1 lds
v_mfma_f32_16x16x32_bf16 v[140:143], v[196:199], a[248:251], v[140:143]
v_sub_f32 v106, v106, v23
v_exp_f32 v105, v105
v_sub_f32 v107, v107, v23
v_mfma_f32_16x16x32_bf16 v[112:115], v[200:203], a[204:207], v[112:115]
v_cvt_pk_bf16_f32 v99, v102, v103
v_exp_f32 v106, v106
v_mfma_f32_16x16x32_bf16 v[120:123], v[200:203], a[220:223], v[120:123]
v_sub_f32 v108, v108, v23
v_exp_f32 v107, v107
v_sub_f32 v109, v109, v23
v_mfma_f32_16x16x32_bf16 v[128:131], v[200:203], a[236:239], v[128:131]
v_cvt_pk_bf16_f32 v104, v104, v105
v_exp_f32 v108, v108
v_mfma_f32_16x16x32_bf16 v[136:139], v[200:203], a[252:255], v[136:139]
v_sub_f32 v110, v110, v23
buffer_load_dwordx4 v3, s[8:11], s92 offen offset:3072 sc1 lds
v_mfma_f32_16x16x32_bf16 v[116:119], v[204:207], a[204:207], v[116:119]
v_exp_f32 v109, v109
v_sub_f32 v111, v111, v23
s_add_u32 s92, s92, 8192
v_mfma_f32_16x16x32_bf16 v[124:127], v[204:207], a[220:223], v[124:127]
v_cvt_pk_bf16_f32 v105, v106, v107
v_exp_f32 v110, v110
v_mfma_f32_16x16x32_bf16 v[132:135], v[204:207], a[236:239], v[132:135]
v_exp_f32 v111, v111
v_cvt_pk_bf16_f32 v106, v108, v109
v_cvt_pk_bf16_f32 v107, v110, v111
v_mfma_f32_16x16x32_bf16 v[140:143], v[204:207], a[252:255], v[140:143]
s_waitcnt vmcnt(16) lgkmcnt(0)
s_barrier
v_mov_b32 v28, v26
v_mov_b32 v29, v27
v_mov_b32 v30, v72
v_mov_b32 v31, v76
ds_write_b128 v17, v[28:31]
s_waitcnt lgkmcnt(0)
s_barrier
ds_read_b128 v[32:35], v18
v_accvgpr_read_b32 v176, a0
v_accvgpr_read_b32 v177, a1
v_accvgpr_read_b32 v178, a2
v_accvgpr_read_b32 v179, a3
v_accvgpr_read_b32 v180, a4
v_accvgpr_read_b32 v181, a5
v_accvgpr_read_b32 v182, a6
v_accvgpr_read_b32 v183, a7
v_accvgpr_read_b32 v184, a16
v_accvgpr_read_b32 v185, a17
v_accvgpr_read_b32 v186, a18
v_accvgpr_read_b32 v187, a19
v_accvgpr_read_b32 v188, a20
v_accvgpr_read_b32 v189, a21
v_accvgpr_read_b32 v190, a22
v_accvgpr_read_b32 v191, a23
s_waitcnt lgkmcnt(0)
s_barrier
s_add_u32 s99, s56, 175104
ds_write_b128 v17, a[8:11] offset:0
v_max_f32 v28, v24, v32
v_max_f32 v29, v25, v33
v_sub_f32 v36, v24, v28
v_sub_f32 v38, v32, v28
v_sub_f32 v37, v25, v29
v_sub_f32 v39, v33, v29
v_exp_f32 v36, v36
ds_write_b128 v17, a[12:15] offset:1024
v_exp_f32 v37, v37
v_exp_f32 v38, v38
v_exp_f32 v39, v39
v_add_f32 v30, v64, v34
v_add_f32 v31, v68, v35
v_mul_f32 v28, v64, v36
v_mul_f32 v29, v68, v37
v_fma_f32 v28, v34, v38, v28
ds_write_b128 v17, a[24:27] offset:2048
v_fma_f32 v29, v35, v39, v29
v_add_f32 v30, v30, v31
s_mov_b32 s98, 0x4e800000
v_rcp_f32 v28, v28
v_rcp_f32 v29, v29
v_cmp_nge_f32 vcc, s98, v30
v_mul_f32 v36, v36, v28
ds_write_b128 v17, a[28:31] offset:3072
v_mul_f32 v38, v38, v28
v_mul_f32 v37, v37, v29
v_mul_f32 v39, v39, v29
s_cmp_lg_u64 vcc, 0
s_cselect_b32 s98, 1, 0
s_or_b32 s97, s97, s98
v_accvgpr_read_b32 v192, a32
v_accvgpr_read_b32 v193, a33
ds_write_b128 v17, a[40:43] offset:4096
v_accvgpr_read_b32 v194, a34
v_accvgpr_read_b32 v195, a35
v_accvgpr_read_b32 v196, a36
v_accvgpr_read_b32 v197, a37
v_accvgpr_read_b32 v198, a38
v_accvgpr_read_b32 v199, a39
v_accvgpr_read_b32 v200, a48
ds_write_b128 v17, a[44:47] offset:5120
v_accvgpr_read_b32 v201, a49
v_accvgpr_read_b32 v202, a50
v_accvgpr_read_b32 v203, a51
v_accvgpr_read_b32 v204, a52
v_accvgpr_read_b32 v205, a53
v_accvgpr_read_b32 v206, a54
v_accvgpr_read_b32 v207, a55
v_pk_mul_f32 v[176:177], v[176:177], v[36:37] op_sel_hi:[1,0]
ds_write_b128 v17, a[56:59] offset:6144
v_pk_mul_f32 v[178:179], v[178:179], v[36:37] op_sel_hi:[1,0]
v_pk_mul_f32 v[180:181], v[180:181], v[36:37] op_sel:[0,1] op_sel_hi:[1,1]
v_pk_mul_f32 v[182:183], v[182:183], v[36:37] op_sel:[0,1] op_sel_hi:[1,1]
v_pk_mul_f32 v[184:185], v[184:185], v[36:37] op_sel_hi:[1,0]
v_pk_mul_f32 v[186:187], v[186:187], v[36:37] op_sel_hi:[1,0]
v_pk_mul_f32 v[188:189], v[188:189], v[36:37] op_sel:[0,1] op_sel_hi:[1,1]
v_pk_mul_f32 v[190:191], v[190:191], v[36:37] op_sel:[0,1] op_sel_hi:[1,1]
ds_write_b128 v17, a[60:63] offset:7168
v_pk_mul_f32 v[192:193], v[192:193], v[36:37] op_sel_hi:[1,0]
v_pk_mul_f32 v[194:195], v[194:195], v[36:37] op_sel_hi:[1,0]
v_pk_mul_f32 v[196:197], v[196:197], v[36:37] op_sel:[0,1] op_sel_hi:[1,1]
v_pk_mul_f32 v[198:199], v[198:199], v[36:37] op_sel:[0,1] op_sel_hi:[1,1]
v_pk_mul_f32 v[200:201], v[200:201], v[36:37] op_sel_hi:[1,0]
)ASM"
R"ASM(
v_pk_mul_f32 v[202:203], v[202:203], v[36:37] op_sel_hi:[1,0]
v_pk_mul_f32 v[204:205], v[204:205], v[36:37] op_sel:[0,1] op_sel_hi:[1,1]
v_pk_mul_f32 v[206:207], v[206:207], v[36:37] op_sel:[0,1] op_sel_hi:[1,1]
s_waitcnt lgkmcnt(0)
s_barrier
ds_read_b128 v[208:211], v18 offset:0
ds_read_b128 v[212:215], v18 offset:1024
ds_read_b128 v[216:219], v18 offset:2048
ds_read_b128 v[220:223], v18 offset:3072
ds_read_b128 v[224:227], v18 offset:4096
ds_read_b128 v[228:231], v18 offset:5120
ds_read_b128 v[232:235], v18 offset:6144
ds_read_b128 v[236:239], v18 offset:7168
s_waitcnt lgkmcnt(0)
s_barrier
ds_write_b128 v17, a[72:75] offset:0
v_pk_fma_f32 v[176:177], v[208:209], v[38:39], v[176:177] op_sel_hi:[1,0,1]
v_pk_fma_f32 v[178:179], v[210:211], v[38:39], v[178:179] op_sel_hi:[1,0,1]
v_pk_fma_f32 v[180:181], v[212:213], v[38:39], v[180:181] op_sel:[0,1,0] op_sel_hi:[1,1,1]
v_pk_fma_f32 v[182:183], v[214:215], v[38:39], v[182:183] op_sel:[0,1,0] op_sel_hi:[1,1,1]
ds_write_b128 v17, a[76:79] offset:1024
v_pk_fma_f32 v[184:185], v[216:217], v[38:39], v[184:185] op_sel_hi:[1,0,1]
v_pk_fma_f32 v[186:187], v[218:219], v[38:39], v[186:187] op_sel_hi:[1,0,1]
v_pk_fma_f32 v[188:189], v[220:221], v[38:39], v[188:189] op_sel:[0,1,0] op_sel_hi:[1,1,1]
v_pk_fma_f32 v[190:191], v[222:223], v[38:39], v[190:191] op_sel:[0,1,0] op_sel_hi:[1,1,1]
v_pk_fma_f32 v[192:193], v[224:225], v[38:39], v[192:193] op_sel_hi:[1,0,1]
ds_write_b128 v17, a[88:91] offset:2048
v_pk_fma_f32 v[194:195], v[226:227], v[38:39], v[194:195] op_sel_hi:[1,0,1]
v_pk_fma_f32 v[196:197], v[228:229], v[38:39], v[196:197] op_sel:[0,1,0] op_sel_hi:[1,1,1]
v_pk_fma_f32 v[198:199], v[230:231], v[38:39], v[198:199] op_sel:[0,1,0] op_sel_hi:[1,1,1]
v_pk_fma_f32 v[200:201], v[232:233], v[38:39], v[200:201] op_sel_hi:[1,0,1]
ds_write_b128 v17, a[92:95] offset:3072
v_pk_fma_f32 v[202:203], v[234:235], v[38:39], v[202:203] op_sel_hi:[1,0,1]
v_pk_fma_f32 v[204:205], v[236:237], v[38:39], v[204:205] op_sel:[0,1,0] op_sel_hi:[1,1,1]
v_pk_fma_f32 v[206:207], v[238:239], v[38:39], v[206:207] op_sel:[0,1,0] op_sel_hi:[1,1,1]
v_cvt_pk_bf16_f32 v176, v176, v177
v_cvt_pk_bf16_f32 v177, v178, v179
ds_write_b128 v17, a[104:107] offset:4096
v_cvt_pk_bf16_f32 v178, v184, v185
v_cvt_pk_bf16_f32 v179, v186, v187
buffer_store_dwordx4 v[176:179], v2, s[8:11], s56 offen offset:0 sc1
v_cvt_pk_bf16_f32 v180, v180, v181
ds_write_b128 v17, a[108:111] offset:5120
v_cvt_pk_bf16_f32 v181, v182, v183
v_cvt_pk_bf16_f32 v182, v188, v189
v_cvt_pk_bf16_f32 v183, v190, v191
buffer_store_dwordx4 v[180:183], v2, s[8:11], s99 offen offset:0 sc1
v_cvt_pk_bf16_f32 v192, v192, v193
ds_write_b128 v17, a[120:123] offset:6144
v_cvt_pk_bf16_f32 v193, v194, v195
v_cvt_pk_bf16_f32 v194, v200, v201
v_cvt_pk_bf16_f32 v195, v202, v203
buffer_store_dwordx4 v[192:195], v2, s[8:11], s56 offen offset:1024 sc1
ds_write_b128 v17, a[124:127] offset:7168
v_cvt_pk_bf16_f32 v196, v196, v197
v_cvt_pk_bf16_f32 v197, v198, v199
v_cvt_pk_bf16_f32 v198, v204, v205
v_cvt_pk_bf16_f32 v199, v206, v207
buffer_store_dwordx4 v[196:199], v2, s[8:11], s99 offen offset:1024 sc1
s_nop 1
v_accvgpr_read_b32 v176, a64
v_accvgpr_read_b32 v177, a65
v_accvgpr_read_b32 v178, a66
v_accvgpr_read_b32 v179, a67
v_accvgpr_read_b32 v180, a68
v_accvgpr_read_b32 v181, a69
v_accvgpr_read_b32 v182, a70
v_accvgpr_read_b32 v183, a71
v_accvgpr_read_b32 v184, a80
v_accvgpr_read_b32 v185, a81
v_accvgpr_read_b32 v186, a82
v_accvgpr_read_b32 v187, a83
v_accvgpr_read_b32 v188, a84
v_accvgpr_read_b32 v189, a85
v_accvgpr_read_b32 v190, a86
v_accvgpr_read_b32 v191, a87
v_accvgpr_read_b32 v192, a96
v_accvgpr_read_b32 v193, a97
v_accvgpr_read_b32 v194, a98
v_accvgpr_read_b32 v195, a99
v_accvgpr_read_b32 v196, a100
v_accvgpr_read_b32 v197, a101
v_accvgpr_read_b32 v198, a102
v_accvgpr_read_b32 v199, a103
v_accvgpr_read_b32 v200, a112
v_accvgpr_read_b32 v201, a113
v_accvgpr_read_b32 v202, a114
v_accvgpr_read_b32 v203, a115
v_accvgpr_read_b32 v204, a116
v_accvgpr_read_b32 v205, a117
v_accvgpr_read_b32 v206, a118
v_accvgpr_read_b32 v207, a119
v_pk_mul_f32 v[176:177], v[176:177], v[36:37] op_sel_hi:[1,0]
v_pk_mul_f32 v[178:179], v[178:179], v[36:37] op_sel_hi:[1,0]
v_pk_mul_f32 v[180:181], v[180:181], v[36:37] op_sel:[0,1] op_sel_hi:[1,1]
v_pk_mul_f32 v[182:183], v[182:183], v[36:37] op_sel:[0,1] op_sel_hi:[1,1]
v_pk_mul_f32 v[184:185], v[184:185], v[36:37] op_sel_hi:[1,0]
v_pk_mul_f32 v[186:187], v[186:187], v[36:37] op_sel_hi:[1,0]
v_pk_mul_f32 v[188:189], v[188:189], v[36:37] op_sel:[0,1] op_sel_hi:[1,1]
v_pk_mul_f32 v[190:191], v[190:191], v[36:37] op_sel:[0,1] op_sel_hi:[1,1]
v_pk_mul_f32 v[192:193], v[192:193], v[36:37] op_sel_hi:[1,0]
v_pk_mul_f32 v[194:195], v[194:195], v[36:37] op_sel_hi:[1,0]
v_pk_mul_f32 v[196:197], v[196:197], v[36:37] op_sel:[0,1] op_sel_hi:[1,1]
v_pk_mul_f32 v[198:199], v[198:199], v[36:37] op_sel:[0,1] op_sel_hi:[1,1]
v_pk_mul_f32 v[200:201], v[200:201], v[36:37] op_sel_hi:[1,0]
v_pk_mul_f32 v[202:203], v[202:203], v[36:37] op_sel_hi:[1,0]
v_pk_mul_f32 v[204:205], v[204:205], v[36:37] op_sel:[0,1] op_sel_hi:[1,1]
v_pk_mul_f32 v[206:207], v[206:207], v[36:37] op_sel:[0,1] op_sel_hi:[1,1]
s_waitcnt lgkmcnt(0)
s_barrier
ds_read_b128 v[208:211], v18 offset:0
ds_read_b128 v[212:215], v18 offset:1024
ds_read_b128 v[216:219], v18 offset:2048
ds_read_b128 v[220:223], v18 offset:3072
ds_read_b128 v[224:227], v18 offset:4096
ds_read_b128 v[228:231], v18 offset:5120
ds_read_b128 v[232:235], v18 offset:6144
ds_read_b128 v[236:239], v18 offset:7168
s_waitcnt lgkmcnt(0)
v_pk_fma_f32 v[176:177], v[208:209], v[38:39], v[176:177] op_sel_hi:[1,0,1]
v_pk_fma_f32 v[178:179], v[210:211], v[38:39], v[178:179] op_sel_hi:[1,0,1]
v_pk_fma_f32 v[180:181], v[212:213], v[38:39], v[180:181] op_sel:[0,1,0] op_sel_hi:[1,1,1]
v_pk_fma_f32 v[182:183], v[214:215], v[38:39], v[182:183] op_sel:[0,1,0] op_sel_hi:[1,1,1]
v_pk_fma_f32 v[184:185], v[216:217], v[38:39], v[184:185] op_sel_hi:[1,0,1]
v_pk_fma_f32 v[186:187], v[218:219], v[38:39], v[186:187] op_sel_hi:[1,0,1]
v_pk_fma_f32 v[188:189], v[220:221], v[38:39], v[188:189] op_sel:[0,1,0] op_sel_hi:[1,1,1]
v_pk_fma_f32 v[190:191], v[222:223], v[38:39], v[190:191] op_sel:[0,1,0] op_sel_hi:[1,1,1]
v_pk_fma_f32 v[192:193], v[224:225], v[38:39], v[192:193] op_sel_hi:[1,0,1]
v_pk_fma_f32 v[194:195], v[226:227], v[38:39], v[194:195] op_sel_hi:[1,0,1]
v_pk_fma_f32 v[196:197], v[228:229], v[38:39], v[196:197] op_sel:[0,1,0] op_sel_hi:[1,1,1]
v_pk_fma_f32 v[198:199], v[230:231], v[38:39], v[198:199] op_sel:[0,1,0] op_sel_hi:[1,1,1]
v_pk_fma_f32 v[200:201], v[232:233], v[38:39], v[200:201] op_sel_hi:[1,0,1]
v_pk_fma_f32 v[202:203], v[234:235], v[38:39], v[202:203] op_sel_hi:[1,0,1]
v_pk_fma_f32 v[204:205], v[236:237], v[38:39], v[204:205] op_sel:[0,1,0] op_sel_hi:[1,1,1]
v_pk_fma_f32 v[206:207], v[238:239], v[38:39], v[206:207] op_sel:[0,1,0] op_sel_hi:[1,1,1]
v_cvt_pk_bf16_f32 v176, v176, v177
v_cvt_pk_bf16_f32 v177, v178, v179
v_cvt_pk_bf16_f32 v178, v184, v185
v_cvt_pk_bf16_f32 v179, v186, v187
buffer_store_dwordx4 v[176:179], v2, s[8:11], s56 offen offset:2048 sc1
v_cvt_pk_bf16_f32 v180, v180, v181
v_cvt_pk_bf16_f32 v181, v182, v183
v_cvt_pk_bf16_f32 v182, v188, v189
v_cvt_pk_bf16_f32 v183, v190, v191
buffer_store_dwordx4 v[180:183], v2, s[8:11], s99 offen offset:2048 sc1
v_cvt_pk_bf16_f32 v192, v192, v193
v_cvt_pk_bf16_f32 v193, v194, v195
v_cvt_pk_bf16_f32 v194, v200, v201
v_cvt_pk_bf16_f32 v195, v202, v203
buffer_store_dwordx4 v[192:195], v2, s[8:11], s56 offen offset:3072 sc1
v_cvt_pk_bf16_f32 v196, v196, v197
v_cvt_pk_bf16_f32 v197, v198, v199
v_cvt_pk_bf16_f32 v198, v204, v205
v_cvt_pk_bf16_f32 v199, v206, v207
buffer_store_dwordx4 v[196:199], v2, s[8:11], s99 offen offset:3072 sc1
v_mfma_f32_16x16x32_bf16 a[0:3], v[144:147], v[80:83], 0
ds_read_b128 v[176:179], v16 offset:0
v_mfma_f32_16x16x32_bf16 a[4:7], v[144:147], v[88:91], 0
ds_read_b128 v[180:183], v16 offset:1024
v_mfma_f32_16x16x32_bf16 a[8:11], v[144:147], v[96:99], 0
ds_read_b128 v[184:187], v16 offset:2048
v_mfma_f32_16x16x32_bf16 a[12:15], v[144:147], v[104:107], 0
ds_read_b128 v[188:191], v16 offset:3072
v_mfma_f32_16x16x32_bf16 a[16:19], v[148:151], v[80:83], 0
v_exp_f32 v112, v112
ds_read_b128 v[192:195], v16 offset:4096
v_mfma_f32_16x16x32_bf16 a[20:23], v[148:151], v[88:91], 0
ds_read_b128 v[196:199], v16 offset:5120
v_mfma_f32_16x16x32_bf16 a[24:27], v[148:151], v[96:99], 0
v_exp_f32 v113, v113
ds_read_b128 v[200:203], v16 offset:6144
v_mfma_f32_16x16x32_bf16 a[28:31], v[148:151], v[104:107], 0
ds_read_b128 v[204:207], v16 offset:7168
v_mfma_f32_16x16x32_bf16 a[32:35], v[152:155], v[80:83], 0
v_exp_f32 v114, v114
v_mfma_f32_16x16x32_bf16 a[36:39], v[152:155], v[88:91], 0
v_exp_f32 v115, v115
s_mov_b32 m0, s95
v_mfma_f32_16x16x32_bf16 a[40:43], v[152:155], v[96:99], 0
buffer_load_dwordx4 v3, s[8:11], s93 offen offset:0 sc1 lds
v_mfma_f32_16x16x32_bf16 a[44:47], v[152:155], v[104:107], 0
v_cvt_pk_bf16_f32 v112, v112, v113
v_mfma_f32_16x16x32_bf16 a[48:51], v[156:159], v[80:83], 0
v_exp_f32 v116, v116
v_mfma_f32_16x16x32_bf16 a[52:55], v[156:159], v[88:91], 0
v_exp_f32 v117, v117
v_mfma_f32_16x16x32_bf16 a[56:59], v[156:159], v[96:99], 0
v_cvt_pk_bf16_f32 v113, v114, v115
v_mfma_f32_16x16x32_bf16 a[60:63], v[156:159], v[104:107], 0
v_exp_f32 v118, v118
v_mfma_f32_16x16x32_bf16 a[64:67], v[160:163], v[80:83], 0
buffer_load_dwordx4 v3, s[8:11], s93 offen offset:1024 sc1 lds
v_mfma_f32_16x16x32_bf16 a[68:71], v[160:163], v[88:91], 0
v_exp_f32 v119, v119
v_mfma_f32_16x16x32_bf16 a[72:75], v[160:163], v[96:99], 0
v_cvt_pk_bf16_f32 v114, v116, v117
v_mfma_f32_16x16x32_bf16 a[76:79], v[160:163], v[104:107], 0
v_exp_f32 v120, v120
v_mfma_f32_16x16x32_bf16 a[80:83], v[164:167], v[80:83], 0
v_exp_f32 v121, v121
)ASM"
R"ASM(
v_cvt_pk_bf16_f32 v115, v118, v119
v_mfma_f32_16x16x32_bf16 a[84:87], v[164:167], v[88:91], 0
v_exp_f32 v122, v122
v_mfma_f32_16x16x32_bf16 a[88:91], v[164:167], v[96:99], 0
buffer_load_dwordx4 v3, s[8:11], s93 offen offset:2048 sc1 lds
v_mfma_f32_16x16x32_bf16 a[92:95], v[164:167], v[104:107], 0
v_mfma_f32_16x16x32_bf16 a[96:99], v[168:171], v[80:83], 0
v_exp_f32 v123, v123
v_cvt_pk_bf16_f32 v120, v120, v121
v_mfma_f32_16x16x32_bf16 a[100:103], v[168:171], v[88:91], 0
v_exp_f32 v124, v124
v_mfma_f32_16x16x32_bf16 a[104:107], v[168:171], v[96:99], 0
v_exp_f32 v125, v125
v_mfma_f32_16x16x32_bf16 a[108:111], v[168:171], v[104:107], 0
v_cvt_pk_bf16_f32 v121, v122, v123
v_mfma_f32_16x16x32_bf16 a[112:115], v[172:175], v[80:83], 0
buffer_load_dwordx4 v3, s[8:11], s93 offen offset:3072 sc1 lds
v_mfma_f32_16x16x32_bf16 a[116:119], v[172:175], v[88:91], 0
s_add_u32 s93, s93, 8192
v_mfma_f32_16x16x32_bf16 a[120:123], v[172:175], v[96:99], 0
v_exp_f32 v126, v126
v_mfma_f32_16x16x32_bf16 a[124:127], v[172:175], v[104:107], 0
v_exp_f32 v127, v127
v_cvt_pk_bf16_f32 v122, v124, v125
v_mfma_f32_16x16x32_bf16 v[64:67], v[44:47], v[80:83], 0
v_exp_f32 v128, v128
v_mfma_f32_16x16x32_bf16 v[68:71], v[44:47], v[88:91], 0
v_exp_f32 v129, v129
v_mfma_f32_16x16x32_bf16 v[72:75], v[44:47], v[96:99], 0
v_cvt_pk_bf16_f32 v123, v126, v127
v_mfma_f32_16x16x32_bf16 v[76:79], v[44:47], v[104:107], 0
v_exp_f32 v130, v130
s_waitcnt lgkmcnt(0)
v_mfma_f32_16x16x32_bf16 v[80:83], v[176:179], a[192:195], v[48:51]
ds_read_b128 v[144:147], v19 offset:16384
v_mfma_f32_16x16x32_bf16 v[88:91], v[176:179], a[208:211], v[52:55]
v_exp_f32 v131, v131
ds_read_b128 v[148:151], v19 offset:17408
v_mfma_f32_16x16x32_bf16 v[96:99], v[176:179], a[224:227], v[56:59]
v_cvt_pk_bf16_f32 v128, v128, v129
ds_read_b128 v[152:155], v19 offset:18432
v_mfma_f32_16x16x32_bf16 v[104:107], v[176:179], a[240:243], v[60:63]
ds_read_b128 v[156:159], v19 offset:19456
v_mfma_f32_16x16x32_bf16 v[84:87], v[180:183], a[192:195], v[48:51]
v_exp_f32 v132, v132
ds_read_b128 v[160:163], v19 offset:20480
v_mfma_f32_16x16x32_bf16 v[92:95], v[180:183], a[208:211], v[52:55]
ds_read_b128 v[164:167], v19 offset:21504
v_mfma_f32_16x16x32_bf16 v[100:103], v[180:183], a[224:227], v[56:59]
v_exp_f32 v133, v133
ds_read_b128 v[168:171], v19 offset:22528
v_mfma_f32_16x16x32_bf16 v[108:111], v[180:183], a[240:243], v[60:63]
v_cvt_pk_bf16_f32 v129, v130, v131
ds_read_b128 v[172:175], v19 offset:23552
v_mfma_f32_16x16x32_bf16 v[80:83], v[184:187], a[196:199], v[80:83]
v_xor_b32 v19, 32768, v19
v_mfma_f32_16x16x32_bf16 v[88:91], v[184:187], a[212:215], v[88:91]
v_exp_f32 v134, v134
v_mfma_f32_16x16x32_bf16 v[96:99], v[184:187], a[228:231], v[96:99]
v_exp_f32 v135, v135
v_cvt_pk_bf16_f32 v130, v132, v133
v_mfma_f32_16x16x32_bf16 v[104:107], v[184:187], a[244:247], v[104:107]
s_add_u32 m0, s94, 16384
v_mfma_f32_16x16x32_bf16 v[84:87], v[188:191], a[196:199], v[84:87]
buffer_load_dwordx4 v3, s[8:11], s92 offen offset:0 sc1 lds
v_mfma_f32_16x16x32_bf16 v[92:95], v[188:191], a[212:215], v[92:95]
v_exp_f32 v136, v136
v_mfma_f32_16x16x32_bf16 v[100:103], v[188:191], a[228:231], v[100:103]
v_exp_f32 v137, v137
v_mfma_f32_16x16x32_bf16 v[108:111], v[188:191], a[244:247], v[108:111]
v_cvt_pk_bf16_f32 v131, v134, v135
v_mfma_f32_16x16x32_bf16 v[80:83], v[192:195], a[200:203], v[80:83]
v_exp_f32 v138, v138
v_mfma_f32_16x16x32_bf16 v[88:91], v[192:195], a[216:219], v[88:91]
buffer_load_dwordx4 v3, s[8:11], s92 offen offset:1024 sc1 lds
v_mfma_f32_16x16x32_bf16 v[96:99], v[192:195], a[232:235], v[96:99]
v_exp_f32 v139, v139
v_mfma_f32_16x16x32_bf16 v[104:107], v[192:195], a[248:251], v[104:107]
v_cvt_pk_bf16_f32 v136, v136, v137
v_mfma_f32_16x16x32_bf16 v[84:87], v[196:199], a[200:203], v[84:87]
v_exp_f32 v140, v140
v_mfma_f32_16x16x32_bf16 v[92:95], v[196:199], a[216:219], v[92:95]
v_exp_f32 v141, v141
v_cvt_pk_bf16_f32 v137, v138, v139
v_mfma_f32_16x16x32_bf16 v[100:103], v[196:199], a[232:235], v[100:103]
buffer_load_dwordx4 v3, s[8:11], s92 offen offset:2048 sc1 lds
v_mfma_f32_16x16x32_bf16 v[108:111], v[196:199], a[248:251], v[108:111]
v_mfma_f32_16x16x32_bf16 v[80:83], v[200:203], a[204:207], v[80:83]
v_exp_f32 v142, v142
v_mfma_f32_16x16x32_bf16 v[88:91], v[200:203], a[220:223], v[88:91]
v_exp_f32 v143, v143
v_cvt_pk_bf16_f32 v138, v140, v141
v_mfma_f32_16x16x32_bf16 v[96:99], v[200:203], a[236:239], v[96:99]
v_cvt_pk_bf16_f32 v139, v142, v143
v_mfma_f32_16x16x32_bf16 v[104:107], v[200:203], a[252:255], v[104:107]
buffer_load_dwordx4 v3, s[8:11], s92 offen offset:3072 sc1 lds
v_mfma_f32_16x16x32_bf16 v[84:87], v[204:207], a[204:207], v[84:87]
s_add_u32 s92, s92, 24576
s_cmp_eq_u32 s92, s52
v_mfma_f32_16x16x32_bf16 v[92:95], v[204:207], a[220:223], v[92:95]
s_cselect_b32 s92, s82, s92
s_cselect_b32 s52, -1, s52
s_xor_b32 s94, s94, 32768
v_mfma_f32_16x16x32_bf16 v[100:103], v[204:207], a[236:239], v[100:103]
v_mfma_f32_16x16x32_bf16 v[108:111], v[204:207], a[252:255], v[108:111]
s_waitcnt vmcnt(25) lgkmcnt(0)
s_barrier
v_mfma_f32_16x16x32_bf16 a[0:3], v[144:147], v[112:115], a[0:3]
ds_read_b128 v[176:179], v16 offset:16384
v_mfma_f32_16x16x32_bf16 a[4:7], v[144:147], v[120:123], a[4:7]
ds_read_b128 v[180:183], v16 offset:17408
v_mfma_f32_16x16x32_bf16 a[8:11], v[144:147], v[128:131], a[8:11]
ds_read_b128 v[184:187], v16 offset:18432
v_mfma_f32_16x16x32_bf16 a[12:15], v[144:147], v[136:139], a[12:15]
ds_read_b128 v[188:191], v16 offset:19456
v_mfma_f32_16x16x32_bf16 a[16:19], v[148:151], v[112:115], a[16:19]
v_exp_f32 v80, v80
ds_read_b128 v[192:195], v16 offset:20480
v_mfma_f32_16x16x32_bf16 a[20:23], v[148:151], v[120:123], a[20:23]
ds_read_b128 v[196:199], v16 offset:21504
v_mfma_f32_16x16x32_bf16 a[24:27], v[148:151], v[128:131], a[24:27]
v_exp_f32 v81, v81
ds_read_b128 v[200:203], v16 offset:22528
v_mfma_f32_16x16x32_bf16 a[28:31], v[148:151], v[136:139], a[28:31]
ds_read_b128 v[204:207], v16 offset:23552
v_mfma_f32_16x16x32_bf16 a[32:35], v[152:155], v[112:115], a[32:35]
v_exp_f32 v82, v82
v_xor_b32 v16, 32768, v16
v_mfma_f32_16x16x32_bf16 a[36:39], v[152:155], v[120:123], a[36:39]
v_exp_f32 v83, v83
s_add_u32 m0, s95, 16384
v_mfma_f32_16x16x32_bf16 a[40:43], v[152:155], v[128:131], a[40:43]
buffer_load_dwordx4 v3, s[8:11], s93 offen offset:0 sc1 lds
v_mfma_f32_16x16x32_bf16 a[44:47], v[152:155], v[136:139], a[44:47]
v_cvt_pk_bf16_f32 v80, v80, v81
v_mfma_f32_16x16x32_bf16 a[48:51], v[156:159], v[112:115], a[48:51]
v_exp_f32 v84, v84
v_mfma_f32_16x16x32_bf16 a[52:55], v[156:159], v[120:123], a[52:55]
v_exp_f32 v85, v85
v_mfma_f32_16x16x32_bf16 a[56:59], v[156:159], v[128:131], a[56:59]
v_cvt_pk_bf16_f32 v81, v82, v83
v_mfma_f32_16x16x32_bf16 a[60:63], v[156:159], v[136:139], a[60:63]
v_exp_f32 v86, v86
v_mfma_f32_16x16x32_bf16 a[64:67], v[160:163], v[112:115], a[64:67]
buffer_load_dwordx4 v3, s[8:11], s93 offen offset:1024 sc1 lds
v_mfma_f32_16x16x32_bf16 a[68:71], v[160:163], v[120:123], a[68:71]
v_exp_f32 v87, v87
v_mfma_f32_16x16x32_bf16 a[72:75], v[160:163], v[128:131], a[72:75]
v_cvt_pk_bf16_f32 v82, v84, v85
v_mfma_f32_16x16x32_bf16 a[76:79], v[160:163], v[136:139], a[76:79]
v_exp_f32 v88, v88
v_mfma_f32_16x16x32_bf16 a[80:83], v[164:167], v[112:115], a[80:83]
v_exp_f32 v89, v89
v_cvt_pk_bf16_f32 v83, v86, v87
v_mfma_f32_16x16x32_bf16 a[84:87], v[164:167], v[120:123], a[84:87]
v_exp_f32 v90, v90
v_mfma_f32_16x16x32_bf16 a[88:91], v[164:167], v[128:131], a[88:91]
buffer_load_dwordx4 v3, s[8:11], s93 offen offset:2048 sc1 lds
v_mfma_f32_16x16x32_bf16 a[92:95], v[164:167], v[136:139], a[92:95]
v_exp_f32 v91, v91
v_mfma_f32_16x16x32_bf16 a[96:99], v[168:171], v[112:115], a[96:99]
v_cvt_pk_bf16_f32 v88, v88, v89
v_mfma_f32_16x16x32_bf16 a[100:103], v[168:171], v[120:123], a[100:103]
v_exp_f32 v92, v92
v_mfma_f32_16x16x32_bf16 a[104:107], v[168:171], v[128:131], a[104:107]
v_exp_f32 v93, v93
v_cvt_pk_bf16_f32 v89, v90, v91
v_mfma_f32_16x16x32_bf16 a[108:111], v[168:171], v[136:139], a[108:111]
v_exp_f32 v94, v94
v_mfma_f32_16x16x32_bf16 a[112:115], v[172:175], v[112:115], a[112:115]
buffer_load_dwordx4 v3, s[8:11], s93 offen offset:3072 sc1 lds
v_mfma_f32_16x16x32_bf16 a[116:119], v[172:175], v[120:123], a[116:119]
s_add_u32 s93, s93, 24576
s_cmp_eq_u32 s93, s53
v_mfma_f32_16x16x32_bf16 a[120:123], v[172:175], v[128:131], a[120:123]
s_cselect_b32 s93, s14, s93
s_cselect_b32 s53, -1, s53
v_mfma_f32_16x16x32_bf16 a[124:127], v[172:175], v[136:139], a[124:127]
s_xor_b32 s95, s95, 32768
v_mfma_f32_16x16x32_bf16 v[64:67], v[44:47], v[112:115], v[64:67]
v_exp_f32 v95, v95
v_mfma_f32_16x16x32_bf16 v[68:71], v[44:47], v[120:123], v[68:71]
buffer_load_dword v245, v244, s[40:43], 0 offen
v_mfma_f32_16x16x32_bf16 v[72:75], v[44:47], v[128:131], v[72:75]
v_cvt_pk_bf16_f32 v90, v92, v93
v_mfma_f32_16x16x32_bf16 v[76:79], v[44:47], v[136:139], v[76:79]
v_exp_f32 v96, v96
s_waitcnt lgkmcnt(0)
v_mfma_f32_16x16x32_bf16 v[112:115], v[176:179], a[192:195], v[48:51]
ds_read_b128 v[144:147], v19 offset:0
v_mfma_f32_16x16x32_bf16 v[120:123], v[176:179], a[208:211], v[52:55]
v_exp_f32 v97, v97
ds_read_b128 v[148:151], v19 offset:1024
v_mfma_f32_16x16x32_bf16 v[128:131], v[176:179], a[224:227], v[56:59]
v_cvt_pk_bf16_f32 v91, v94, v95
ds_read_b128 v[152:155], v19 offset:2048
v_mfma_f32_16x16x32_bf16 v[136:139], v[176:179], a[240:243], v[60:63]
ds_read_b128 v[156:159], v19 offset:3072
v_mfma_f32_16x16x32_bf16 v[116:119], v[180:183], a[192:195], v[48:51]
v_exp_f32 v98, v98
)ASM"
R"ASM(
ds_read_b128 v[160:163], v19 offset:4096
v_mfma_f32_16x16x32_bf16 v[124:127], v[180:183], a[208:211], v[52:55]
ds_read_b128 v[164:167], v19 offset:5120
v_mfma_f32_16x16x32_bf16 v[132:135], v[180:183], a[224:227], v[56:59]
v_exp_f32 v99, v99
ds_read_b128 v[168:171], v19 offset:6144
v_mfma_f32_16x16x32_bf16 v[140:143], v[180:183], a[240:243], v[60:63]
v_cvt_pk_bf16_f32 v96, v96, v97
ds_read_b128 v[172:175], v19 offset:7168
v_mfma_f32_16x16x32_bf16 v[112:115], v[184:187], a[196:199], v[112:115]
v_exp_f32 v100, v100
v_mfma_f32_16x16x32_bf16 v[120:123], v[184:187], a[212:215], v[120:123]
v_exp_f32 v101, v101
v_mfma_f32_16x16x32_bf16 v[128:131], v[184:187], a[228:231], v[128:131]
buffer_load_dword v246, v244, s[44:47], 0 offen
v_mfma_f32_16x16x32_bf16 v[136:139], v[184:187], a[244:247], v[136:139]
v_cvt_pk_bf16_f32 v97, v98, v99
s_mov_b32 m0, s94
v_mfma_f32_16x16x32_bf16 v[116:119], v[188:191], a[196:199], v[116:119]
buffer_load_dwordx4 v3, s[8:11], s92 offen offset:0 sc1 lds
v_mfma_f32_16x16x32_bf16 v[124:127], v[188:191], a[212:215], v[124:127]
v_mfma_f32_16x16x32_bf16 v[132:135], v[188:191], a[228:231], v[132:135]
v_exp_f32 v102, v102
v_mfma_f32_16x16x32_bf16 v[140:143], v[188:191], a[244:247], v[140:143]
v_exp_f32 v103, v103
v_cvt_pk_bf16_f32 v98, v100, v101
v_mfma_f32_16x16x32_bf16 v[112:115], v[192:195], a[200:203], v[112:115]
v_exp_f32 v104, v104
v_mfma_f32_16x16x32_bf16 v[120:123], v[192:195], a[216:219], v[120:123]
buffer_load_dwordx4 v3, s[8:11], s92 offen offset:1024 sc1 lds
v_mfma_f32_16x16x32_bf16 v[128:131], v[192:195], a[232:235], v[128:131]
v_mfma_f32_16x16x32_bf16 v[136:139], v[192:195], a[248:251], v[136:139]
v_exp_f32 v105, v105
v_cvt_pk_bf16_f32 v99, v102, v103
v_mfma_f32_16x16x32_bf16 v[116:119], v[196:199], a[200:203], v[116:119]
v_exp_f32 v106, v106
v_mfma_f32_16x16x32_bf16 v[124:127], v[196:199], a[216:219], v[124:127]
v_exp_f32 v107, v107
v_mfma_f32_16x16x32_bf16 v[132:135], v[196:199], a[232:235], v[132:135]
buffer_load_dwordx4 v3, s[8:11], s92 offen offset:2048 sc1 lds
v_mfma_f32_16x16x32_bf16 v[140:143], v[196:199], a[248:251], v[140:143]
v_cvt_pk_bf16_f32 v104, v104, v105
v_mfma_f32_16x16x32_bf16 v[112:115], v[200:203], a[204:207], v[112:115]
v_exp_f32 v108, v108
v_mfma_f32_16x16x32_bf16 v[120:123], v[200:203], a[220:223], v[120:123]
v_exp_f32 v109, v109
v_cvt_pk_bf16_f32 v105, v106, v107
v_mfma_f32_16x16x32_bf16 v[128:131], v[200:203], a[236:239], v[128:131]
v_exp_f32 v110, v110
v_mfma_f32_16x16x32_bf16 v[136:139], v[200:203], a[252:255], v[136:139]
buffer_load_dwordx4 v3, s[8:11], s92 offen offset:3072 sc1 lds
v_mfma_f32_16x16x32_bf16 v[116:119], v[204:207], a[204:207], v[116:119]
s_add_u32 s92, s92, 8192
v_mfma_f32_16x16x32_bf16 v[124:127], v[204:207], a[220:223], v[124:127]
v_exp_f32 v111, v111
v_mfma_f32_16x16x32_bf16 v[132:135], v[204:207], a[236:239], v[132:135]
v_cvt_pk_bf16_f32 v106, v108, v109
v_cvt_pk_bf16_f32 v107, v110, v111
v_mfma_f32_16x16x32_bf16 v[140:143], v[204:207], a[252:255], v[140:143]
s_waitcnt vmcnt(26) lgkmcnt(0)
s_barrier
s_sub_u32 s96, s55, 2
.LlB_%=:
v_mfma_f32_16x16x32_bf16 a[0:3], v[144:147], v[80:83], a[0:3]
ds_read_b128 v[176:179], v16 offset:0
v_mfma_f32_16x16x32_bf16 a[4:7], v[144:147], v[88:91], a[4:7]
ds_read_b128 v[180:183], v16 offset:1024
v_mfma_f32_16x16x32_bf16 a[8:11], v[144:147], v[96:99], a[8:11]
ds_read_b128 v[184:187], v16 offset:2048
v_mfma_f32_16x16x32_bf16 a[12:15], v[144:147], v[104:107], a[12:15]
ds_read_b128 v[188:191], v16 offset:3072
v_mfma_f32_16x16x32_bf16 a[16:19], v[148:151], v[80:83], a[16:19]
v_exp_f32 v112, v112
ds_read_b128 v[192:195], v16 offset:4096
v_mfma_f32_16x16x32_bf16 a[20:23], v[148:151], v[88:91], a[20:23]
ds_read_b128 v[196:199], v16 offset:5120
v_mfma_f32_16x16x32_bf16 a[24:27], v[148:151], v[96:99], a[24:27]
v_exp_f32 v113, v113
ds_read_b128 v[200:203], v16 offset:6144
v_mfma_f32_16x16x32_bf16 a[28:31], v[148:151], v[104:107], a[28:31]
ds_read_b128 v[204:207], v16 offset:7168
v_mfma_f32_16x16x32_bf16 a[32:35], v[152:155], v[80:83], a[32:35]
v_exp_f32 v114, v114
v_mfma_f32_16x16x32_bf16 a[36:39], v[152:155], v[88:91], a[36:39]
v_exp_f32 v115, v115
s_mov_b32 m0, s95
v_mfma_f32_16x16x32_bf16 a[40:43], v[152:155], v[96:99], a[40:43]
buffer_load_dwordx4 v3, s[8:11], s93 offen offset:0 sc1 lds
v_mfma_f32_16x16x32_bf16 a[44:47], v[152:155], v[104:107], a[44:47]
v_cvt_pk_bf16_f32 v112, v112, v113
v_mfma_f32_16x16x32_bf16 a[48:51], v[156:159], v[80:83], a[48:51]
v_exp_f32 v116, v116
v_mfma_f32_16x16x32_bf16 a[52:55], v[156:159], v[88:91], a[52:55]
v_exp_f32 v117, v117
v_mfma_f32_16x16x32_bf16 a[56:59], v[156:159], v[96:99], a[56:59]
v_cvt_pk_bf16_f32 v113, v114, v115
v_mfma_f32_16x16x32_bf16 a[60:63], v[156:159], v[104:107], a[60:63]
v_exp_f32 v118, v118
v_mfma_f32_16x16x32_bf16 a[64:67], v[160:163], v[80:83], a[64:67]
buffer_load_dwordx4 v3, s[8:11], s93 offen offset:1024 sc1 lds
v_mfma_f32_16x16x32_bf16 a[68:71], v[160:163], v[88:91], a[68:71]
v_exp_f32 v119, v119
v_mfma_f32_16x16x32_bf16 a[72:75], v[160:163], v[96:99], a[72:75]
v_cvt_pk_bf16_f32 v114, v116, v117
v_mfma_f32_16x16x32_bf16 a[76:79], v[160:163], v[104:107], a[76:79]
v_exp_f32 v120, v120
v_mfma_f32_16x16x32_bf16 a[80:83], v[164:167], v[80:83], a[80:83]
v_exp_f32 v121, v121
v_cvt_pk_bf16_f32 v115, v118, v119
v_mfma_f32_16x16x32_bf16 a[84:87], v[164:167], v[88:91], a[84:87]
v_exp_f32 v122, v122
v_mfma_f32_16x16x32_bf16 a[88:91], v[164:167], v[96:99], a[88:91]
buffer_load_dwordx4 v3, s[8:11], s93 offen offset:2048 sc1 lds
v_mfma_f32_16x16x32_bf16 a[92:95], v[164:167], v[104:107], a[92:95]
v_mfma_f32_16x16x32_bf16 a[96:99], v[168:171], v[80:83], a[96:99]
v_exp_f32 v123, v123
v_cvt_pk_bf16_f32 v120, v120, v121
v_mfma_f32_16x16x32_bf16 a[100:103], v[168:171], v[88:91], a[100:103]
v_exp_f32 v124, v124
v_mfma_f32_16x16x32_bf16 a[104:107], v[168:171], v[96:99], a[104:107]
v_exp_f32 v125, v125
v_mfma_f32_16x16x32_bf16 a[108:111], v[168:171], v[104:107], a[108:111]
v_cvt_pk_bf16_f32 v121, v122, v123
v_mfma_f32_16x16x32_bf16 a[112:115], v[172:175], v[80:83], a[112:115]
buffer_load_dwordx4 v3, s[8:11], s93 offen offset:3072 sc1 lds
v_mfma_f32_16x16x32_bf16 a[116:119], v[172:175], v[88:91], a[116:119]
s_add_u32 s93, s93, 8192
v_mfma_f32_16x16x32_bf16 a[120:123], v[172:175], v[96:99], a[120:123]
v_exp_f32 v126, v126
v_mfma_f32_16x16x32_bf16 a[124:127], v[172:175], v[104:107], a[124:127]
v_exp_f32 v127, v127
v_cvt_pk_bf16_f32 v122, v124, v125
v_mfma_f32_16x16x32_bf16 v[64:67], v[44:47], v[80:83], v[64:67]
v_exp_f32 v128, v128
v_mfma_f32_16x16x32_bf16 v[68:71], v[44:47], v[88:91], v[68:71]
v_exp_f32 v129, v129
v_mfma_f32_16x16x32_bf16 v[72:75], v[44:47], v[96:99], v[72:75]
v_cvt_pk_bf16_f32 v123, v126, v127
v_mfma_f32_16x16x32_bf16 v[76:79], v[44:47], v[104:107], v[76:79]
v_exp_f32 v130, v130
s_waitcnt lgkmcnt(0)
v_mfma_f32_16x16x32_bf16 v[80:83], v[176:179], a[192:195], v[48:51]
ds_read_b128 v[144:147], v19 offset:16384
v_mfma_f32_16x16x32_bf16 v[88:91], v[176:179], a[208:211], v[52:55]
v_exp_f32 v131, v131
ds_read_b128 v[148:151], v19 offset:17408
v_mfma_f32_16x16x32_bf16 v[96:99], v[176:179], a[224:227], v[56:59]
v_cvt_pk_bf16_f32 v128, v128, v129
ds_read_b128 v[152:155], v19 offset:18432
v_mfma_f32_16x16x32_bf16 v[104:107], v[176:179], a[240:243], v[60:63]
ds_read_b128 v[156:159], v19 offset:19456
v_mfma_f32_16x16x32_bf16 v[84:87], v[180:183], a[192:195], v[48:51]
v_exp_f32 v132, v132
ds_read_b128 v[160:163], v19 offset:20480
v_mfma_f32_16x16x32_bf16 v[92:95], v[180:183], a[208:211], v[52:55]
ds_read_b128 v[164:167], v19 offset:21504
v_mfma_f32_16x16x32_bf16 v[100:103], v[180:183], a[224:227], v[56:59]
v_exp_f32 v133, v133
ds_read_b128 v[168:171], v19 offset:22528
v_mfma_f32_16x16x32_bf16 v[108:111], v[180:183], a[240:243], v[60:63]
v_cvt_pk_bf16_f32 v129, v130, v131
ds_read_b128 v[172:175], v19 offset:23552
v_mfma_f32_16x16x32_bf16 v[80:83], v[184:187], a[196:199], v[80:83]
v_xor_b32 v19, 32768, v19
v_mfma_f32_16x16x32_bf16 v[88:91], v[184:187], a[212:215], v[88:91]
v_exp_f32 v134, v134
v_mfma_f32_16x16x32_bf16 v[96:99], v[184:187], a[228:231], v[96:99]
v_exp_f32 v135, v135
v_cvt_pk_bf16_f32 v130, v132, v133
v_mfma_f32_16x16x32_bf16 v[104:107], v[184:187], a[244:247], v[104:107]
s_add_u32 m0, s94, 16384
v_mfma_f32_16x16x32_bf16 v[84:87], v[188:191], a[196:199], v[84:87]
buffer_load_dwordx4 v3, s[8:11], s92 offen offset:0 sc1 lds
v_mfma_f32_16x16x32_bf16 v[92:95], v[188:191], a[212:215], v[92:95]
v_exp_f32 v136, v136
v_mfma_f32_16x16x32_bf16 v[100:103], v[188:191], a[228:231], v[100:103]
v_exp_f32 v137, v137
v_mfma_f32_16x16x32_bf16 v[108:111], v[188:191], a[244:247], v[108:111]
v_cvt_pk_bf16_f32 v131, v134, v135
v_mfma_f32_16x16x32_bf16 v[80:83], v[192:195], a[200:203], v[80:83]
v_exp_f32 v138, v138
v_mfma_f32_16x16x32_bf16 v[88:91], v[192:195], a[216:219], v[88:91]
buffer_load_dwordx4 v3, s[8:11], s92 offen offset:1024 sc1 lds
v_mfma_f32_16x16x32_bf16 v[96:99], v[192:195], a[232:235], v[96:99]
v_exp_f32 v139, v139
v_mfma_f32_16x16x32_bf16 v[104:107], v[192:195], a[248:251], v[104:107]
v_cvt_pk_bf16_f32 v136, v136, v137
v_mfma_f32_16x16x32_bf16 v[84:87], v[196:199], a[200:203], v[84:87]
v_exp_f32 v140, v140
v_mfma_f32_16x16x32_bf16 v[92:95], v[196:199], a[216:219], v[92:95]
v_exp_f32 v141, v141
v_cvt_pk_bf16_f32 v137, v138, v139
v_mfma_f32_16x16x32_bf16 v[100:103], v[196:199], a[232:235], v[100:103]
buffer_load_dwordx4 v3, s[8:11], s92 offen offset:2048 sc1 lds
v_mfma_f32_16x16x32_bf16 v[108:111], v[196:199], a[248:251], v[108:111]
v_mfma_f32_16x16x32_bf16 v[80:83], v[200:203], a[204:207], v[80:83]
v_exp_f32 v142, v142
v_mfma_f32_16x16x32_bf16 v[88:91], v[200:203], a[220:223], v[88:91]
v_exp_f32 v143, v143
v_cvt_pk_bf16_f32 v138, v140, v141
)ASM"
R"ASM(
v_mfma_f32_16x16x32_bf16 v[96:99], v[200:203], a[236:239], v[96:99]
v_cvt_pk_bf16_f32 v139, v142, v143
v_mfma_f32_16x16x32_bf16 v[104:107], v[200:203], a[252:255], v[104:107]
buffer_load_dwordx4 v3, s[8:11], s92 offen offset:3072 sc1 lds
v_mfma_f32_16x16x32_bf16 v[84:87], v[204:207], a[204:207], v[84:87]
s_add_u32 s92, s92, 24576
s_cmp_eq_u32 s92, s52
v_mfma_f32_16x16x32_bf16 v[92:95], v[204:207], a[220:223], v[92:95]
s_cselect_b32 s92, s82, s92
s_cselect_b32 s52, -1, s52
s_xor_b32 s94, s94, 32768
v_mfma_f32_16x16x32_bf16 v[100:103], v[204:207], a[236:239], v[100:103]
v_mfma_f32_16x16x32_bf16 v[108:111], v[204:207], a[252:255], v[108:111]
s_waitcnt vmcnt(16) lgkmcnt(0)
s_barrier
v_mfma_f32_16x16x32_bf16 a[0:3], v[144:147], v[112:115], a[0:3]
ds_read_b128 v[176:179], v16 offset:16384
v_mfma_f32_16x16x32_bf16 a[4:7], v[144:147], v[120:123], a[4:7]
ds_read_b128 v[180:183], v16 offset:17408
v_mfma_f32_16x16x32_bf16 a[8:11], v[144:147], v[128:131], a[8:11]
ds_read_b128 v[184:187], v16 offset:18432
v_mfma_f32_16x16x32_bf16 a[12:15], v[144:147], v[136:139], a[12:15]
ds_read_b128 v[188:191], v16 offset:19456
v_mfma_f32_16x16x32_bf16 a[16:19], v[148:151], v[112:115], a[16:19]
v_exp_f32 v80, v80
ds_read_b128 v[192:195], v16 offset:20480
v_mfma_f32_16x16x32_bf16 a[20:23], v[148:151], v[120:123], a[20:23]
ds_read_b128 v[196:199], v16 offset:21504
v_mfma_f32_16x16x32_bf16 a[24:27], v[148:151], v[128:131], a[24:27]
v_exp_f32 v81, v81
ds_read_b128 v[200:203], v16 offset:22528
v_mfma_f32_16x16x32_bf16 a[28:31], v[148:151], v[136:139], a[28:31]
ds_read_b128 v[204:207], v16 offset:23552
v_mfma_f32_16x16x32_bf16 a[32:35], v[152:155], v[112:115], a[32:35]
v_exp_f32 v82, v82
v_xor_b32 v16, 32768, v16
v_mfma_f32_16x16x32_bf16 a[36:39], v[152:155], v[120:123], a[36:39]
s_add_u32 m0, s95, 16384
v_mfma_f32_16x16x32_bf16 a[40:43], v[152:155], v[128:131], a[40:43]
buffer_load_dwordx4 v3, s[8:11], s93 offen offset:0 sc1 lds
v_mfma_f32_16x16x32_bf16 a[44:47], v[152:155], v[136:139], a[44:47]
v_exp_f32 v83, v83
v_mfma_f32_16x16x32_bf16 a[48:51], v[156:159], v[112:115], a[48:51]
v_cvt_pk_bf16_f32 v80, v80, v81
v_mfma_f32_16x16x32_bf16 a[52:55], v[156:159], v[120:123], a[52:55]
v_exp_f32 v84, v84
v_mfma_f32_16x16x32_bf16 a[56:59], v[156:159], v[128:131], a[56:59]
v_exp_f32 v85, v85
v_cvt_pk_bf16_f32 v81, v82, v83
v_mfma_f32_16x16x32_bf16 a[60:63], v[156:159], v[136:139], a[60:63]
v_exp_f32 v86, v86
v_mfma_f32_16x16x32_bf16 a[64:67], v[160:163], v[112:115], a[64:67]
buffer_load_dwordx4 v3, s[8:11], s93 offen offset:1024 sc1 lds
v_mfma_f32_16x16x32_bf16 a[68:71], v[160:163], v[120:123], a[68:71]
v_mfma_f32_16x16x32_bf16 a[72:75], v[160:163], v[128:131], a[72:75]
v_exp_f32 v87, v87
v_cvt_pk_bf16_f32 v82, v84, v85
v_mfma_f32_16x16x32_bf16 a[76:79], v[160:163], v[136:139], a[76:79]
v_exp_f32 v88, v88
v_mfma_f32_16x16x32_bf16 a[80:83], v[164:167], v[112:115], a[80:83]
v_exp_f32 v89, v89
v_mfma_f32_16x16x32_bf16 a[84:87], v[164:167], v[120:123], a[84:87]
v_cvt_pk_bf16_f32 v83, v86, v87
v_mfma_f32_16x16x32_bf16 a[88:91], v[164:167], v[128:131], a[88:91]
buffer_load_dwordx4 v3, s[8:11], s93 offen offset:2048 sc1 lds
v_mfma_f32_16x16x32_bf16 a[92:95], v[164:167], v[136:139], a[92:95]
v_exp_f32 v90, v90
v_mfma_f32_16x16x32_bf16 a[96:99], v[168:171], v[112:115], a[96:99]
v_exp_f32 v91, v91
v_mfma_f32_16x16x32_bf16 a[100:103], v[168:171], v[120:123], a[100:103]
v_cvt_pk_bf16_f32 v88, v88, v89
v_mfma_f32_16x16x32_bf16 a[104:107], v[168:171], v[128:131], a[104:107]
v_exp_f32 v92, v92
v_mfma_f32_16x16x32_bf16 a[108:111], v[168:171], v[136:139], a[108:111]
v_exp_f32 v93, v93
v_cvt_pk_bf16_f32 v89, v90, v91
v_mfma_f32_16x16x32_bf16 a[112:115], v[172:175], v[112:115], a[112:115]
buffer_load_dwordx4 v3, s[8:11], s93 offen offset:3072 sc1 lds
v_mfma_f32_16x16x32_bf16 a[116:119], v[172:175], v[120:123], a[116:119]
s_add_u32 s93, s93, 24576
s_cmp_eq_u32 s93, s53
v_mfma_f32_16x16x32_bf16 a[120:123], v[172:175], v[128:131], a[120:123]
s_cselect_b32 s93, s14, s93
s_cselect_b32 s53, -1, s53
v_mfma_f32_16x16x32_bf16 a[124:127], v[172:175], v[136:139], a[124:127]
s_xor_b32 s95, s95, 32768
v_mfma_f32_16x16x32_bf16 v[64:67], v[44:47], v[112:115], v[64:67]
v_exp_f32 v94, v94
v_mfma_f32_16x16x32_bf16 v[68:71], v[44:47], v[120:123], v[68:71]
v_exp_f32 v95, v95
v_mfma_f32_16x16x32_bf16 v[72:75], v[44:47], v[128:131], v[72:75]
v_cvt_pk_bf16_f32 v90, v92, v93
v_exp_f32 v96, v96
v_mfma_f32_16x16x32_bf16 v[76:79], v[44:47], v[136:139], v[76:79]
s_waitcnt lgkmcnt(0)
v_mfma_f32_16x16x32_bf16 v[112:115], v[176:179], a[192:195], v[48:51]
v_exp_f32 v97, v97
ds_read_b128 v[144:147], v19 offset:0
v_mfma_f32_16x16x32_bf16 v[120:123], v[176:179], a[208:211], v[52:55]
v_cvt_pk_bf16_f32 v91, v94, v95
ds_read_b128 v[148:151], v19 offset:1024
v_mfma_f32_16x16x32_bf16 v[128:131], v[176:179], a[224:227], v[56:59]
ds_read_b128 v[152:155], v19 offset:2048
v_mfma_f32_16x16x32_bf16 v[136:139], v[176:179], a[240:243], v[60:63]
v_exp_f32 v98, v98
ds_read_b128 v[156:159], v19 offset:3072
v_mfma_f32_16x16x32_bf16 v[116:119], v[180:183], a[192:195], v[48:51]
ds_read_b128 v[160:163], v19 offset:4096
v_mfma_f32_16x16x32_bf16 v[124:127], v[180:183], a[208:211], v[52:55]
v_exp_f32 v99, v99
ds_read_b128 v[164:167], v19 offset:5120
v_mfma_f32_16x16x32_bf16 v[132:135], v[180:183], a[224:227], v[56:59]
v_cvt_pk_bf16_f32 v96, v96, v97
ds_read_b128 v[168:171], v19 offset:6144
v_mfma_f32_16x16x32_bf16 v[140:143], v[180:183], a[240:243], v[60:63]
ds_read_b128 v[172:175], v19 offset:7168
v_mfma_f32_16x16x32_bf16 v[112:115], v[184:187], a[196:199], v[112:115]
v_exp_f32 v100, v100
v_mfma_f32_16x16x32_bf16 v[120:123], v[184:187], a[212:215], v[120:123]
v_exp_f32 v101, v101
v_cvt_pk_bf16_f32 v97, v98, v99
v_mfma_f32_16x16x32_bf16 v[128:131], v[184:187], a[228:231], v[128:131]
v_exp_f32 v102, v102
v_mfma_f32_16x16x32_bf16 v[136:139], v[184:187], a[244:247], v[136:139]
s_mov_b32 m0, s94
v_mfma_f32_16x16x32_bf16 v[116:119], v[188:191], a[196:199], v[116:119]
buffer_load_dwordx4 v3, s[8:11], s92 offen offset:0 sc1 lds
v_mfma_f32_16x16x32_bf16 v[124:127], v[188:191], a[212:215], v[124:127]
v_exp_f32 v103, v103
v_mfma_f32_16x16x32_bf16 v[132:135], v[188:191], a[228:231], v[132:135]
v_cvt_pk_bf16_f32 v98, v100, v101
v_mfma_f32_16x16x32_bf16 v[140:143], v[188:191], a[244:247], v[140:143]
v_exp_f32 v104, v104
v_mfma_f32_16x16x32_bf16 v[112:115], v[192:195], a[200:203], v[112:115]
v_exp_f32 v105, v105
v_cvt_pk_bf16_f32 v99, v102, v103
v_mfma_f32_16x16x32_bf16 v[120:123], v[192:195], a[216:219], v[120:123]
buffer_load_dwordx4 v3, s[8:11], s92 offen offset:1024 sc1 lds
v_mfma_f32_16x16x32_bf16 v[128:131], v[192:195], a[232:235], v[128:131]
v_mfma_f32_16x16x32_bf16 v[136:139], v[192:195], a[248:251], v[136:139]
v_exp_f32 v106, v106
v_mfma_f32_16x16x32_bf16 v[116:119], v[196:199], a[200:203], v[116:119]
v_exp_f32 v107, v107
v_cvt_pk_bf16_f32 v104, v104, v105
v_mfma_f32_16x16x32_bf16 v[124:127], v[196:199], a[216:219], v[124:127]
v_exp_f32 v108, v108
v_mfma_f32_16x16x32_bf16 v[132:135], v[196:199], a[232:235], v[132:135]
buffer_load_dwordx4 v3, s[8:11], s92 offen offset:2048 sc1 lds
v_mfma_f32_16x16x32_bf16 v[140:143], v[196:199], a[248:251], v[140:143]
v_mfma_f32_16x16x32_bf16 v[112:115], v[200:203], a[204:207], v[112:115]
v_exp_f32 v109, v109
v_cvt_pk_bf16_f32 v105, v106, v107
v_mfma_f32_16x16x32_bf16 v[120:123], v[200:203], a[220:223], v[120:123]
v_exp_f32 v110, v110
v_mfma_f32_16x16x32_bf16 v[128:131], v[200:203], a[236:239], v[128:131]
v_exp_f32 v111, v111
v_mfma_f32_16x16x32_bf16 v[136:139], v[200:203], a[252:255], v[136:139]
buffer_load_dwordx4 v3, s[8:11], s92 offen offset:3072 sc1 lds
v_mfma_f32_16x16x32_bf16 v[116:119], v[204:207], a[204:207], v[116:119]
s_add_u32 s92, s92, 8192
v_mfma_f32_16x16x32_bf16 v[124:127], v[204:207], a[220:223], v[124:127]
v_cvt_pk_bf16_f32 v106, v108, v109
v_cvt_pk_bf16_f32 v107, v110, v111
v_mfma_f32_16x16x32_bf16 v[132:135], v[204:207], a[236:239], v[132:135]
s_sub_u32 s96, s96, 1
s_cmp_lg_u32 s96, 0
v_mfma_f32_16x16x32_bf16 v[140:143], v[204:207], a[252:255], v[140:143]
s_waitcnt vmcnt(16) lgkmcnt(0)
s_barrier
s_cbranch_scc1 .LlB_%=
v_mfma_f32_16x16x32_bf16 a[0:3], v[144:147], v[80:83], a[0:3]
ds_read_b128 v[176:179], v16 offset:0
v_mfma_f32_16x16x32_bf16 a[4:7], v[144:147], v[88:91], a[4:7]
ds_read_b128 v[180:183], v16 offset:1024
v_mfma_f32_16x16x32_bf16 a[8:11], v[144:147], v[96:99], a[8:11]
ds_read_b128 v[184:187], v16 offset:2048
v_mfma_f32_16x16x32_bf16 a[12:15], v[144:147], v[104:107], a[12:15]
ds_read_b128 v[188:191], v16 offset:3072
v_mfma_f32_16x16x32_bf16 a[16:19], v[148:151], v[80:83], a[16:19]
v_exp_f32 v112, v112
ds_read_b128 v[192:195], v16 offset:4096
v_mfma_f32_16x16x32_bf16 a[20:23], v[148:151], v[88:91], a[20:23]
ds_read_b128 v[196:199], v16 offset:5120
v_mfma_f32_16x16x32_bf16 a[24:27], v[148:151], v[96:99], a[24:27]
v_exp_f32 v113, v113
ds_read_b128 v[200:203], v16 offset:6144
v_mfma_f32_16x16x32_bf16 a[28:31], v[148:151], v[104:107], a[28:31]
ds_read_b128 v[204:207], v16 offset:7168
v_mfma_f32_16x16x32_bf16 a[32:35], v[152:155], v[80:83], a[32:35]
v_exp_f32 v114, v114
v_mfma_f32_16x16x32_bf16 a[36:39], v[152:155], v[88:91], a[36:39]
v_exp_f32 v115, v115
v_cvt_pk_bf16_f32 v112, v112, v113
v_mfma_f32_16x16x32_bf16 a[40:43], v[152:155], v[96:99], a[40:43]
v_exp_f32 v116, v116
v_mfma_f32_16x16x32_bf16 a[44:47], v[152:155], v[104:107], a[44:47]
v_exp_f32 v117, v117
v_mfma_f32_16x16x32_bf16 a[48:51], v[156:159], v[80:83], a[48:51]
v_cvt_pk_bf16_f32 v113, v114, v115
v_mfma_f32_16x16x32_bf16 a[52:55], v[156:159], v[88:91], a[52:55]
)ASM"
R"ASM(
v_exp_f32 v118, v118
v_mfma_f32_16x16x32_bf16 a[56:59], v[156:159], v[96:99], a[56:59]
v_exp_f32 v119, v119
v_cvt_pk_bf16_f32 v114, v116, v117
v_mfma_f32_16x16x32_bf16 a[60:63], v[156:159], v[104:107], a[60:63]
v_exp_f32 v120, v120
v_mfma_f32_16x16x32_bf16 a[64:67], v[160:163], v[80:83], a[64:67]
v_exp_f32 v121, v121
v_mfma_f32_16x16x32_bf16 a[68:71], v[160:163], v[88:91], a[68:71]
v_cvt_pk_bf16_f32 v115, v118, v119
v_mfma_f32_16x16x32_bf16 a[72:75], v[160:163], v[96:99], a[72:75]
v_exp_f32 v122, v122
v_mfma_f32_16x16x32_bf16 a[76:79], v[160:163], v[104:107], a[76:79]
v_exp_f32 v123, v123
v_cvt_pk_bf16_f32 v120, v120, v121
v_mfma_f32_16x16x32_bf16 a[80:83], v[164:167], v[80:83], a[80:83]
v_and_b32 v32, 496, v2
v_lshlrev_b32 v32, 4, v32
buffer_load_dword v33, v32, s[8:11], s88 offen sc1
v_mfma_f32_16x16x32_bf16 a[84:87], v[164:167], v[88:91], a[84:87]
v_mfma_f32_16x16x32_bf16 a[88:91], v[164:167], v[96:99], a[88:91]
v_mfma_f32_16x16x32_bf16 a[92:95], v[164:167], v[104:107], a[92:95]
v_exp_f32 v124, v124
v_mfma_f32_16x16x32_bf16 a[96:99], v[168:171], v[80:83], a[96:99]
v_exp_f32 v125, v125
v_cvt_pk_bf16_f32 v121, v122, v123
v_mfma_f32_16x16x32_bf16 a[100:103], v[168:171], v[88:91], a[100:103]
v_exp_f32 v126, v126
v_mfma_f32_16x16x32_bf16 a[104:107], v[168:171], v[96:99], a[104:107]
v_exp_f32 v127, v127
v_mfma_f32_16x16x32_bf16 a[108:111], v[168:171], v[104:107], a[108:111]
v_cvt_pk_bf16_f32 v122, v124, v125
v_mfma_f32_16x16x32_bf16 a[112:115], v[172:175], v[80:83], a[112:115]
v_exp_f32 v128, v128
v_mfma_f32_16x16x32_bf16 a[116:119], v[172:175], v[88:91], a[116:119]
v_exp_f32 v129, v129
v_cvt_pk_bf16_f32 v123, v126, v127
v_mfma_f32_16x16x32_bf16 a[120:123], v[172:175], v[96:99], a[120:123]
v_exp_f32 v130, v130
v_mfma_f32_16x16x32_bf16 a[124:127], v[172:175], v[104:107], a[124:127]
v_exp_f32 v131, v131
v_mfma_f32_16x16x32_bf16 v[64:67], v[44:47], v[80:83], v[64:67]
v_cvt_pk_bf16_f32 v128, v128, v129
v_mfma_f32_16x16x32_bf16 v[68:71], v[44:47], v[88:91], v[68:71]
v_exp_f32 v132, v132
v_mfma_f32_16x16x32_bf16 v[72:75], v[44:47], v[96:99], v[72:75]
v_exp_f32 v133, v133
v_cvt_pk_bf16_f32 v129, v130, v131
v_mfma_f32_16x16x32_bf16 v[76:79], v[44:47], v[104:107], v[76:79]
s_waitcnt lgkmcnt(0)
v_mfma_f32_16x16x32_bf16 v[80:83], v[176:179], a[192:195], v[48:51]
v_exp_f32 v134, v134
ds_read_b128 v[144:147], v19 offset:16384
v_mfma_f32_16x16x32_bf16 v[88:91], v[176:179], a[208:211], v[52:55]
ds_read_b128 v[148:151], v19 offset:17408
v_mfma_f32_16x16x32_bf16 v[96:99], v[176:179], a[224:227], v[56:59]
v_exp_f32 v135, v135
ds_read_b128 v[152:155], v19 offset:18432
v_mfma_f32_16x16x32_bf16 v[104:107], v[176:179], a[240:243], v[60:63]
v_cvt_pk_bf16_f32 v130, v132, v133
ds_read_b128 v[156:159], v19 offset:19456
v_mfma_f32_16x16x32_bf16 v[84:87], v[180:183], a[192:195], v[48:51]
ds_read_b128 v[160:163], v19 offset:20480
v_mfma_f32_16x16x32_bf16 v[92:95], v[180:183], a[208:211], v[52:55]
v_exp_f32 v136, v136
ds_read_b128 v[164:167], v19 offset:21504
v_mfma_f32_16x16x32_bf16 v[100:103], v[180:183], a[224:227], v[56:59]
ds_read_b128 v[168:171], v19 offset:22528
v_mfma_f32_16x16x32_bf16 v[108:111], v[180:183], a[240:243], v[60:63]
v_exp_f32 v137, v137
ds_read_b128 v[172:175], v19 offset:23552
v_mfma_f32_16x16x32_bf16 v[80:83], v[184:187], a[196:199], v[80:83]
v_cvt_pk_bf16_f32 v131, v134, v135
v_xor_b32 v19, 32768, v19
v_mfma_f32_16x16x32_bf16 v[88:91], v[184:187], a[212:215], v[88:91]
v_exp_f32 v138, v138
v_mfma_f32_16x16x32_bf16 v[96:99], v[184:187], a[228:231], v[96:99]
v_exp_f32 v139, v139
v_mfma_f32_16x16x32_bf16 v[104:107], v[184:187], a[244:247], v[104:107]
v_cvt_pk_bf16_f32 v136, v136, v137
v_mfma_f32_16x16x32_bf16 v[84:87], v[188:191], a[196:199], v[84:87]
v_exp_f32 v140, v140
v_mfma_f32_16x16x32_bf16 v[92:95], v[188:191], a[212:215], v[92:95]
v_exp_f32 v141, v141
v_cvt_pk_bf16_f32 v137, v138, v139
v_mfma_f32_16x16x32_bf16 v[100:103], v[188:191], a[228:231], v[100:103]
v_exp_f32 v142, v142
v_mfma_f32_16x16x32_bf16 v[108:111], v[188:191], a[244:247], v[108:111]
v_exp_f32 v143, v143
v_mfma_f32_16x16x32_bf16 v[80:83], v[192:195], a[200:203], v[80:83]
v_cvt_pk_bf16_f32 v138, v140, v141
v_cvt_pk_bf16_f32 v139, v142, v143
v_mfma_f32_16x16x32_bf16 v[88:91], v[192:195], a[216:219], v[88:91]
v_mfma_f32_16x16x32_bf16 v[96:99], v[192:195], a[232:235], v[96:99]
v_mfma_f32_16x16x32_bf16 v[104:107], v[192:195], a[248:251], v[104:107]
v_mfma_f32_16x16x32_bf16 v[84:87], v[196:199], a[200:203], v[84:87]
v_mfma_f32_16x16x32_bf16 v[92:95], v[196:199], a[216:219], v[92:95]
v_mfma_f32_16x16x32_bf16 v[100:103], v[196:199], a[232:235], v[100:103]
v_mfma_f32_16x16x32_bf16 v[108:111], v[196:199], a[248:251], v[108:111]
v_mfma_f32_16x16x32_bf16 v[80:83], v[200:203], a[204:207], v[80:83]
v_mfma_f32_16x16x32_bf16 v[88:91], v[200:203], a[220:223], v[88:91]
v_mfma_f32_16x16x32_bf16 v[96:99], v[200:203], a[236:239], v[96:99]
v_mfma_f32_16x16x32_bf16 v[104:107], v[200:203], a[252:255], v[104:107]
v_mfma_f32_16x16x32_bf16 v[84:87], v[204:207], a[204:207], v[84:87]
v_mfma_f32_16x16x32_bf16 v[92:95], v[204:207], a[220:223], v[92:95]
v_mfma_f32_16x16x32_bf16 v[100:103], v[204:207], a[236:239], v[100:103]
v_mfma_f32_16x16x32_bf16 v[108:111], v[204:207], a[252:255], v[108:111]
s_waitcnt vmcnt(0) lgkmcnt(0)
s_barrier
s_add_u32 s98, s100, 2
v_subrev_u32 v33, s98, v33
v_cmp_lt_u32 vcc, 2, v33
s_cmp_lg_u64 vcc, 0
s_cbranch_scc1 .Lsp3_%=
.Lsr3_%=:
v_mfma_f32_16x16x32_bf16 a[0:3], v[144:147], v[112:115], a[0:3]
ds_read_b128 v[176:179], v16 offset:16384
v_mfma_f32_16x16x32_bf16 a[4:7], v[144:147], v[120:123], a[4:7]
ds_read_b128 v[180:183], v16 offset:17408
v_mfma_f32_16x16x32_bf16 a[8:11], v[144:147], v[128:131], a[8:11]
ds_read_b128 v[184:187], v16 offset:18432
v_mfma_f32_16x16x32_bf16 a[12:15], v[144:147], v[136:139], a[12:15]
v_fma_f32 v80, v40, s64, v80
ds_read_b128 v[188:191], v16 offset:19456
v_mfma_f32_16x16x32_bf16 a[16:19], v[148:151], v[112:115], a[16:19]
v_fma_f32 v81, v41, s64, v81
ds_read_b128 v[192:195], v16 offset:20480
v_mfma_f32_16x16x32_bf16 a[20:23], v[148:151], v[120:123], a[20:23]
ds_read_b128 v[196:199], v16 offset:21504
v_mfma_f32_16x16x32_bf16 a[24:27], v[148:151], v[128:131], a[24:27]
v_exp_f32 v80, v80
ds_read_b128 v[200:203], v16 offset:22528
v_mfma_f32_16x16x32_bf16 a[28:31], v[148:151], v[136:139], a[28:31]
v_fma_f32 v82, v42, s64, v82
ds_read_b128 v[204:207], v16 offset:23552
v_mfma_f32_16x16x32_bf16 a[32:35], v[152:155], v[112:115], a[32:35]
v_xor_b32 v16, 32768, v16
v_mfma_f32_16x16x32_bf16 a[36:39], v[152:155], v[120:123], a[36:39]
v_exp_f32 v81, v81
v_fma_f32 v83, v43, s64, v83
v_mfma_f32_16x16x32_bf16 a[40:43], v[152:155], v[128:131], a[40:43]
v_exp_f32 v82, v82
v_mfma_f32_16x16x32_bf16 a[44:47], v[152:155], v[136:139], a[44:47]
v_fma_f32 v84, v40, s65, v84
v_mfma_f32_16x16x32_bf16 a[48:51], v[156:159], v[112:115], a[48:51]
v_exp_f32 v83, v83
v_fma_f32 v85, v41, s65, v85
v_mfma_f32_16x16x32_bf16 a[52:55], v[156:159], v[120:123], a[52:55]
v_cvt_pk_bf16_f32 v80, v80, v81
v_mfma_f32_16x16x32_bf16 a[56:59], v[156:159], v[128:131], a[56:59]
v_exp_f32 v84, v84
v_fma_f32 v86, v42, s65, v86
v_mfma_f32_16x16x32_bf16 a[60:63], v[156:159], v[136:139], a[60:63]
v_exp_f32 v85, v85
v_mfma_f32_16x16x32_bf16 a[64:67], v[160:163], v[112:115], a[64:67]
v_fma_f32 v87, v43, s65, v87
v_cvt_pk_bf16_f32 v81, v82, v83
v_mfma_f32_16x16x32_bf16 a[68:71], v[160:163], v[120:123], a[68:71]
v_exp_f32 v86, v86
v_mfma_f32_16x16x32_bf16 a[72:75], v[160:163], v[128:131], a[72:75]
v_fma_f32 v88, v40, s66, v88
v_mfma_f32_16x16x32_bf16 a[76:79], v[160:163], v[136:139], a[76:79]
v_exp_f32 v87, v87
v_fma_f32 v89, v41, s66, v89
v_mfma_f32_16x16x32_bf16 a[80:83], v[164:167], v[112:115], a[80:83]
v_cvt_pk_bf16_f32 v82, v84, v85
v_mfma_f32_16x16x32_bf16 a[84:87], v[164:167], v[120:123], a[84:87]
v_exp_f32 v88, v88
v_fma_f32 v90, v42, s66, v90
v_mfma_f32_16x16x32_bf16 a[88:91], v[164:167], v[128:131], a[88:91]
v_exp_f32 v89, v89
v_mfma_f32_16x16x32_bf16 a[92:95], v[164:167], v[136:139], a[92:95]
v_fma_f32 v91, v43, s66, v91
v_cvt_pk_bf16_f32 v83, v86, v87
v_mfma_f32_16x16x32_bf16 a[96:99], v[168:171], v[112:115], a[96:99]
v_exp_f32 v90, v90
v_mfma_f32_16x16x32_bf16 a[100:103], v[168:171], v[120:123], a[100:103]
v_fma_f32 v92, v40, s67, v92
v_mfma_f32_16x16x32_bf16 a[104:107], v[168:171], v[128:131], a[104:107]
v_exp_f32 v91, v91
v_fma_f32 v93, v41, s67, v93
v_mfma_f32_16x16x32_bf16 a[108:111], v[168:171], v[136:139], a[108:111]
v_cvt_pk_bf16_f32 v88, v88, v89
v_mfma_f32_16x16x32_bf16 a[112:115], v[172:175], v[112:115], a[112:115]
v_exp_f32 v92, v92
v_fma_f32 v94, v42, s67, v94
v_mfma_f32_16x16x32_bf16 a[116:119], v[172:175], v[120:123], a[116:119]
v_exp_f32 v93, v93
v_mfma_f32_16x16x32_bf16 a[120:123], v[172:175], v[128:131], a[120:123]
v_fma_f32 v95, v43, s67, v95
v_cvt_pk_bf16_f32 v89, v90, v91
v_mfma_f32_16x16x32_bf16 a[124:127], v[172:175], v[136:139], a[124:127]
v_exp_f32 v94, v94
v_mfma_f32_16x16x32_bf16 v[64:67], v[44:47], v[112:115], v[64:67]
v_fma_f32 v96, v40, s68, v96
v_mfma_f32_16x16x32_bf16 v[68:71], v[44:47], v[120:123], v[68:71]
v_exp_f32 v95, v95
v_fma_f32 v97, v41, s68, v97
v_mfma_f32_16x16x32_bf16 v[72:75], v[44:47], v[128:131], v[72:75]
v_cvt_pk_bf16_f32 v90, v92, v93
)ASM"
R"ASM(
v_mfma_f32_16x16x32_bf16 v[76:79], v[44:47], v[136:139], v[76:79]
v_exp_f32 v96, v96
s_waitcnt lgkmcnt(0)
v_mfma_f32_16x16x32_bf16 v[112:115], v[176:179], a[192:195], v[48:51]
v_fma_f32 v98, v42, s68, v98
ds_read_b128 v[144:147], v19 offset:0
v_mfma_f32_16x16x32_bf16 v[120:123], v[176:179], a[208:211], v[52:55]
ds_read_b128 v[148:151], v19 offset:1024
v_mfma_f32_16x16x32_bf16 v[128:131], v[176:179], a[224:227], v[56:59]
v_exp_f32 v97, v97
ds_read_b128 v[152:155], v19 offset:2048
v_mfma_f32_16x16x32_bf16 v[136:139], v[176:179], a[240:243], v[60:63]
v_fma_f32 v99, v43, s68, v99
ds_read_b128 v[156:159], v19 offset:3072
v_mfma_f32_16x16x32_bf16 v[116:119], v[180:183], a[192:195], v[48:51]
v_cvt_pk_bf16_f32 v91, v94, v95
ds_read_b128 v[160:163], v19 offset:4096
v_mfma_f32_16x16x32_bf16 v[124:127], v[180:183], a[208:211], v[52:55]
ds_read_b128 v[164:167], v19 offset:5120
v_mfma_f32_16x16x32_bf16 v[132:135], v[180:183], a[224:227], v[56:59]
v_exp_f32 v98, v98
ds_read_b128 v[168:171], v19 offset:6144
v_mfma_f32_16x16x32_bf16 v[140:143], v[180:183], a[240:243], v[60:63]
v_fma_f32 v100, v40, s69, v100
ds_read_b128 v[172:175], v19 offset:7168
v_mfma_f32_16x16x32_bf16 v[112:115], v[184:187], a[196:199], v[112:115]
v_exp_f32 v99, v99
v_mfma_f32_16x16x32_bf16 v[120:123], v[184:187], a[212:215], v[120:123]
v_fma_f32 v101, v41, s69, v101
v_cvt_pk_bf16_f32 v96, v96, v97
v_mfma_f32_16x16x32_bf16 v[128:131], v[184:187], a[228:231], v[128:131]
v_exp_f32 v100, v100
v_mfma_f32_16x16x32_bf16 v[136:139], v[184:187], a[244:247], v[136:139]
v_fma_f32 v102, v42, s69, v102
v_mfma_f32_16x16x32_bf16 v[116:119], v[188:191], a[196:199], v[116:119]
v_exp_f32 v101, v101
v_fma_f32 v103, v43, s69, v103
v_mfma_f32_16x16x32_bf16 v[124:127], v[188:191], a[212:215], v[124:127]
v_cvt_pk_bf16_f32 v97, v98, v99
v_mfma_f32_16x16x32_bf16 v[132:135], v[188:191], a[228:231], v[132:135]
v_exp_f32 v102, v102
v_fma_f32 v104, v40, s70, v104
v_mfma_f32_16x16x32_bf16 v[140:143], v[188:191], a[244:247], v[140:143]
v_exp_f32 v103, v103
v_mfma_f32_16x16x32_bf16 v[112:115], v[192:195], a[200:203], v[112:115]
v_fma_f32 v105, v41, s70, v105
v_cvt_pk_bf16_f32 v98, v100, v101
v_mfma_f32_16x16x32_bf16 v[120:123], v[192:195], a[216:219], v[120:123]
v_exp_f32 v104, v104
v_mfma_f32_16x16x32_bf16 v[128:131], v[192:195], a[232:235], v[128:131]
v_fma_f32 v106, v42, s70, v106
v_mfma_f32_16x16x32_bf16 v[136:139], v[192:195], a[248:251], v[136:139]
v_exp_f32 v105, v105
v_fma_f32 v107, v43, s70, v107
v_mfma_f32_16x16x32_bf16 v[116:119], v[196:199], a[200:203], v[116:119]
v_cvt_pk_bf16_f32 v99, v102, v103
v_mfma_f32_16x16x32_bf16 v[124:127], v[196:199], a[216:219], v[124:127]
v_exp_f32 v106, v106
v_fma_f32 v108, v40, s71, v108
v_mfma_f32_16x16x32_bf16 v[132:135], v[196:199], a[232:235], v[132:135]
v_exp_f32 v107, v107
v_mfma_f32_16x16x32_bf16 v[140:143], v[196:199], a[248:251], v[140:143]
v_fma_f32 v109, v41, s71, v109
v_cvt_pk_bf16_f32 v104, v104, v105
v_mfma_f32_16x16x32_bf16 v[112:115], v[200:203], a[204:207], v[112:115]
v_exp_f32 v108, v108
v_mfma_f32_16x16x32_bf16 v[120:123], v[200:203], a[220:223], v[120:123]
v_fma_f32 v110, v42, s71, v110
v_mfma_f32_16x16x32_bf16 v[128:131], v[200:203], a[236:239], v[128:131]
v_exp_f32 v109, v109
v_fma_f32 v111, v43, s71, v111
v_mfma_f32_16x16x32_bf16 v[136:139], v[200:203], a[252:255], v[136:139]
v_cvt_pk_bf16_f32 v105, v106, v107
v_mfma_f32_16x16x32_bf16 v[116:119], v[204:207], a[204:207], v[116:119]
v_exp_f32 v110, v110
v_mfma_f32_16x16x32_bf16 v[124:127], v[204:207], a[220:223], v[124:127]
v_exp_f32 v111, v111
v_cvt_pk_bf16_f32 v106, v108, v109
v_mfma_f32_16x16x32_bf16 v[132:135], v[204:207], a[236:239], v[132:135]
v_cvt_pk_bf16_f32 v107, v110, v111
v_mfma_f32_16x16x32_bf16 v[140:143], v[204:207], a[252:255], v[140:143]
s_waitcnt vmcnt(5) lgkmcnt(0)
s_barrier
v_mfma_f32_16x16x32_bf16 a[0:3], v[144:147], v[80:83], a[0:3]
ds_read_b128 v[176:179], v19 offset:16384
s_lshl_b32 s94, s51, 1
v_mfma_f32_16x16x32_bf16 a[4:7], v[144:147], v[88:91], a[4:7]
ds_read_b128 v[180:183], v19 offset:17408
v_mfma_f32_16x16x32_bf16 a[8:11], v[144:147], v[96:99], a[8:11]
ds_read_b128 v[184:187], v19 offset:18432
s_add_u32 m0, s50, 8192
s_add_u32 s93, s91, 0
v_mfma_f32_16x16x32_bf16 a[12:15], v[144:147], v[104:107], a[12:15]
v_fma_f32 v112, v40, s72, v112
ds_read_b128 v[188:191], v19 offset:19456
buffer_load_dwordx4 v242, s[44:47], s93 offen lds
v_mfma_f32_16x16x32_bf16 a[16:19], v[148:151], v[80:83], a[16:19]
v_fma_f32 v113, v41, s72, v113
v_exp_f32 v112, v112
v_fma_f32 v114, v42, s72, v114
ds_read_b128 v[192:195], v19 offset:20480
v_mfma_f32_16x16x32_bf16 a[20:23], v[148:151], v[88:91], a[20:23]
v_exp_f32 v113, v113
ds_read_b128 v[196:199], v19 offset:21504
buffer_load_dwordx4 v243, s[44:47], s93 offen offset:2048 lds
v_mfma_f32_16x16x32_bf16 a[24:27], v[148:151], v[96:99], a[24:27]
v_fma_f32 v115, v43, s72, v115
v_exp_f32 v114, v114
ds_read_b128 v[200:203], v19 offset:22528
s_add_u32 m0, s94, 0
s_add_u32 s92, s90, 0
v_mfma_f32_16x16x32_bf16 a[28:31], v[148:151], v[104:107], a[28:31]
v_fma_f32 v116, v40, s73, v116
ds_read_b128 v[204:207], v19 offset:23552
buffer_load_dwordx4 v240, s[8:11], s92 offen sc1 lds
v_mfma_f32_16x16x32_bf16 a[32:35], v[152:155], v[80:83], a[32:35]
v_exp_f32 v115, v115
v_fma_f32 v117, v41, s73, v117
v_cvt_pk_bf16_f32 v112, v112, v113
v_exp_f32 v116, v116
v_mfma_f32_16x16x32_bf16 a[36:39], v[152:155], v[88:91], a[36:39]
v_fma_f32 v118, v42, s73, v118
buffer_load_dwordx4 v241, s[8:11], s92 offen offset:1024 sc1 lds
v_mfma_f32_16x16x32_bf16 a[40:43], v[152:155], v[96:99], a[40:43]
v_exp_f32 v117, v117
v_fma_f32 v119, v43, s73, v119
v_cvt_pk_bf16_f32 v113, v114, v115
s_add_u32 m0, s50, 24576
s_add_u32 s93, s91, 32768
v_mfma_f32_16x16x32_bf16 a[44:47], v[152:155], v[104:107], a[44:47]
v_exp_f32 v118, v118
v_fma_f32 v120, v40, s74, v120
buffer_load_dwordx4 v242, s[44:47], s93 offen lds
v_mfma_f32_16x16x32_bf16 a[48:51], v[156:159], v[80:83], a[48:51]
v_exp_f32 v119, v119
v_fma_f32 v121, v41, s74, v121
v_cvt_pk_bf16_f32 v114, v116, v117
v_exp_f32 v120, v120
v_mfma_f32_16x16x32_bf16 a[52:55], v[156:159], v[88:91], a[52:55]
v_fma_f32 v122, v42, s74, v122
buffer_load_dwordx4 v243, s[44:47], s93 offen offset:2048 lds
v_mfma_f32_16x16x32_bf16 a[56:59], v[156:159], v[96:99], a[56:59]
v_exp_f32 v121, v121
v_fma_f32 v123, v43, s74, v123
v_cvt_pk_bf16_f32 v115, v118, v119
s_add_u32 m0, s94, 16384
s_add_u32 s92, s90, 1024
v_mfma_f32_16x16x32_bf16 a[60:63], v[156:159], v[104:107], a[60:63]
v_exp_f32 v122, v122
v_fma_f32 v124, v40, s75, v124
buffer_load_dwordx4 v240, s[8:11], s92 offen sc1 lds
v_mfma_f32_16x16x32_bf16 a[64:67], v[160:163], v[80:83], a[64:67]
v_exp_f32 v123, v123
v_fma_f32 v125, v41, s75, v125
v_cvt_pk_bf16_f32 v120, v120, v121
v_mfma_f32_16x16x32_bf16 a[68:71], v[160:163], v[88:91], a[68:71]
v_exp_f32 v124, v124
v_fma_f32 v126, v42, s75, v126
buffer_load_dwordx4 v241, s[8:11], s92 offen offset:1024 sc1 lds
v_mfma_f32_16x16x32_bf16 a[72:75], v[160:163], v[96:99], a[72:75]
v_exp_f32 v125, v125
v_fma_f32 v127, v43, s75, v127
v_cvt_pk_bf16_f32 v121, v122, v123
s_add_u32 m0, s50, 40960
s_add_u32 s93, s91, 65536
v_mfma_f32_16x16x32_bf16 a[76:79], v[160:163], v[104:107], a[76:79]
v_exp_f32 v126, v126
v_fma_f32 v128, v40, s76, v128
buffer_load_dwordx4 v242, s[44:47], s93 offen lds
v_mfma_f32_16x16x32_bf16 a[80:83], v[164:167], v[80:83], a[80:83]
v_exp_f32 v127, v127
v_fma_f32 v129, v41, s76, v129
v_cvt_pk_bf16_f32 v122, v124, v125
v_mfma_f32_16x16x32_bf16 a[84:87], v[164:167], v[88:91], a[84:87]
v_exp_f32 v128, v128
v_fma_f32 v130, v42, s76, v130
buffer_load_dwordx4 v243, s[44:47], s93 offen offset:2048 lds
v_mfma_f32_16x16x32_bf16 a[88:91], v[164:167], v[96:99], a[88:91]
v_exp_f32 v129, v129
v_fma_f32 v131, v43, s76, v131
v_cvt_pk_bf16_f32 v123, v126, v127
s_add_u32 m0, s94, 32768
s_add_u32 s92, s90, 2048
v_mfma_f32_16x16x32_bf16 a[92:95], v[164:167], v[104:107], a[92:95]
v_exp_f32 v130, v130
v_fma_f32 v132, v40, s77, v132
buffer_load_dwordx4 v240, s[8:11], s92 offen sc1 lds
v_mfma_f32_16x16x32_bf16 a[96:99], v[168:171], v[80:83], a[96:99]
v_exp_f32 v131, v131
v_fma_f32 v133, v41, s77, v133
v_cvt_pk_bf16_f32 v128, v128, v129
v_mfma_f32_16x16x32_bf16 a[100:103], v[168:171], v[88:91], a[100:103]
v_exp_f32 v132, v132
v_fma_f32 v134, v42, s77, v134
buffer_load_dwordx4 v241, s[8:11], s92 offen offset:1024 sc1 lds
v_mfma_f32_16x16x32_bf16 a[104:107], v[168:171], v[96:99], a[104:107]
v_exp_f32 v133, v133
v_fma_f32 v135, v43, s77, v135
v_cvt_pk_bf16_f32 v129, v130, v131
s_add_u32 m0, s50, 57344
)ASM"
R"ASM(
s_add_u32 s93, s91, 98304
v_mfma_f32_16x16x32_bf16 a[108:111], v[168:171], v[104:107], a[108:111]
v_exp_f32 v134, v134
buffer_load_dwordx4 v242, s[44:47], s93 offen lds
v_mfma_f32_16x16x32_bf16 a[112:115], v[172:175], v[80:83], a[112:115]
v_fma_f32 v136, v40, s78, v136
v_exp_f32 v135, v135
v_fma_f32 v137, v41, s78, v137
v_cvt_pk_bf16_f32 v130, v132, v133
v_mfma_f32_16x16x32_bf16 a[116:119], v[172:175], v[88:91], a[116:119]
v_exp_f32 v136, v136
v_fma_f32 v138, v42, s78, v138
buffer_load_dwordx4 v243, s[44:47], s93 offen offset:2048 lds
v_mfma_f32_16x16x32_bf16 a[120:123], v[172:175], v[96:99], a[120:123]
v_exp_f32 v137, v137
v_fma_f32 v139, v43, s78, v139
v_cvt_pk_bf16_f32 v131, v134, v135
s_add_u32 m0, s94, 49152
s_add_u32 s92, s90, 3072
v_mfma_f32_16x16x32_bf16 a[124:127], v[172:175], v[104:107], a[124:127]
v_exp_f32 v138, v138
buffer_load_dwordx4 v240, s[8:11], s92 offen sc1 lds
v_mfma_f32_16x16x32_bf16 v[64:67], v[44:47], v[80:83], v[64:67]
v_fma_f32 v140, v40, s79, v140
v_exp_f32 v139, v139
v_fma_f32 v141, v41, s79, v141
v_cvt_pk_bf16_f32 v136, v136, v137
v_mfma_f32_16x16x32_bf16 v[68:71], v[44:47], v[88:91], v[68:71]
v_exp_f32 v140, v140
v_fma_f32 v142, v42, s79, v142
buffer_load_dwordx4 v241, s[8:11], s92 offen offset:1024 sc1 lds
v_mfma_f32_16x16x32_bf16 v[72:75], v[44:47], v[96:99], v[72:75]
v_exp_f32 v141, v141
v_fma_f32 v143, v43, s79, v143
v_cvt_pk_bf16_f32 v137, v138, v139
v_exp_f32 v142, v142
v_mfma_f32_16x16x32_bf16 v[76:79], v[44:47], v[104:107], v[76:79]
v_exp_f32 v143, v143
v_cvt_pk_bf16_f32 v138, v140, v141
v_cvt_pk_bf16_f32 v139, v142, v143
s_waitcnt lgkmcnt(0)
s_nop 1
v_mfma_f32_16x16x32_bf16 v[64:67], v[44:47], v[112:115], v[64:67]
v_mfma_f32_16x16x32_bf16 v[68:71], v[44:47], v[120:123], v[68:71]
v_mfma_f32_16x16x32_bf16 v[72:75], v[44:47], v[128:131], v[72:75]
v_mfma_f32_16x16x32_bf16 v[76:79], v[44:47], v[136:139], v[76:79]
v_mfma_f32_16x16x32_bf16 a[8:11], v[176:179], v[128:131], a[8:11]
v_mfma_f32_16x16x32_bf16 a[12:15], v[176:179], v[136:139], a[12:15]
v_mfma_f32_16x16x32_bf16 a[24:27], v[180:183], v[128:131], a[24:27]
v_mfma_f32_16x16x32_bf16 a[28:31], v[180:183], v[136:139], a[28:31]
v_mfma_f32_16x16x32_bf16 a[40:43], v[184:187], v[128:131], a[40:43]
v_mfma_f32_16x16x32_bf16 a[44:47], v[184:187], v[136:139], a[44:47]
v_mfma_f32_16x16x32_bf16 a[56:59], v[188:191], v[128:131], a[56:59]
v_mfma_f32_16x16x32_bf16 a[60:63], v[188:191], v[136:139], a[60:63]
v_mfma_f32_16x16x32_bf16 a[72:75], v[192:195], v[128:131], a[72:75]
v_mfma_f32_16x16x32_bf16 a[76:79], v[192:195], v[136:139], a[76:79]
v_mfma_f32_16x16x32_bf16 a[88:91], v[196:199], v[128:131], a[88:91]
v_mfma_f32_16x16x32_bf16 a[92:95], v[196:199], v[136:139], a[92:95]
v_mfma_f32_16x16x32_bf16 a[104:107], v[200:203], v[128:131], a[104:107]
v_mfma_f32_16x16x32_bf16 a[108:111], v[200:203], v[136:139], a[108:111]
v_mfma_f32_16x16x32_bf16 a[120:123], v[204:207], v[128:131], a[120:123]
v_mfma_f32_16x16x32_bf16 a[124:127], v[204:207], v[136:139], a[124:127]
v_mfma_f32_16x16x32_bf16 a[0:3], v[176:179], v[112:115], a[0:3]
v_mfma_f32_16x16x32_bf16 a[4:7], v[176:179], v[120:123], a[4:7]
v_mfma_f32_16x16x32_bf16 a[16:19], v[180:183], v[112:115], a[16:19]
v_mfma_f32_16x16x32_bf16 a[20:23], v[180:183], v[120:123], a[20:23]
v_mfma_f32_16x16x32_bf16 a[32:35], v[184:187], v[112:115], a[32:35]
v_mfma_f32_16x16x32_bf16 a[36:39], v[184:187], v[120:123], a[36:39]
v_mfma_f32_16x16x32_bf16 a[48:51], v[188:191], v[112:115], a[48:51]
v_mfma_f32_16x16x32_bf16 a[52:55], v[188:191], v[120:123], a[52:55]
v_mfma_f32_16x16x32_bf16 a[64:67], v[192:195], v[112:115], a[64:67]
v_mfma_f32_16x16x32_bf16 a[68:71], v[192:195], v[120:123], a[68:71]
v_mfma_f32_16x16x32_bf16 a[80:83], v[196:199], v[112:115], a[80:83]
v_mfma_f32_16x16x32_bf16 a[84:87], v[196:199], v[120:123], a[84:87]
v_mfma_f32_16x16x32_bf16 a[96:99], v[200:203], v[112:115], a[96:99]
v_mfma_f32_16x16x32_bf16 a[100:103], v[200:203], v[120:123], a[100:103]
v_mfma_f32_16x16x32_bf16 a[112:115], v[204:207], v[112:115], a[112:115]
v_mfma_f32_16x16x32_bf16 a[116:119], v[204:207], v[120:123], a[116:119]
s_nop 4
v_mov_b32 v28, v22
v_mov_b32 v29, v23
v_mov_b32 v30, v72
v_mov_b32 v31, v76
ds_write_b128 v17, v[28:31]
s_waitcnt lgkmcnt(0)
s_barrier
ds_read_b128 v[32:35], v18
v_accvgpr_read_b32 v176, a0
v_accvgpr_read_b32 v177, a1
v_accvgpr_read_b32 v178, a2
v_accvgpr_read_b32 v179, a3
v_accvgpr_read_b32 v180, a4
v_accvgpr_read_b32 v181, a5
v_accvgpr_read_b32 v182, a6
v_accvgpr_read_b32 v183, a7
v_accvgpr_read_b32 v184, a16
v_accvgpr_read_b32 v185, a17
v_accvgpr_read_b32 v186, a18
v_accvgpr_read_b32 v187, a19
v_accvgpr_read_b32 v188, a20
v_accvgpr_read_b32 v189, a21
v_accvgpr_read_b32 v190, a22
v_accvgpr_read_b32 v191, a23
s_waitcnt lgkmcnt(0)
s_barrier
s_add_u32 s99, s57, 175104
ds_write_b128 v17, a[8:11] offset:0
v_max_f32 v28, v20, v32
v_max_f32 v29, v21, v33
v_sub_f32 v36, v20, v28
v_sub_f32 v38, v32, v28
v_sub_f32 v37, v21, v29
v_sub_f32 v39, v33, v29
v_exp_f32 v36, v36
ds_write_b128 v17, a[12:15] offset:1024
v_exp_f32 v37, v37
v_exp_f32 v38, v38
v_exp_f32 v39, v39
v_add_f32 v30, v64, v34
v_add_f32 v31, v68, v35
v_mul_f32 v28, v64, v36
v_mul_f32 v29, v68, v37
v_fma_f32 v28, v34, v38, v28
ds_write_b128 v17, a[24:27] offset:2048
v_fma_f32 v29, v35, v39, v29
v_add_f32 v30, v30, v31
s_mov_b32 s98, 0x4e800000
v_rcp_f32 v28, v28
v_rcp_f32 v29, v29
v_cmp_nge_f32 vcc, s98, v30
v_mul_f32 v36, v36, v28
ds_write_b128 v17, a[28:31] offset:3072
v_mul_f32 v38, v38, v28
v_mul_f32 v37, v37, v29
v_mul_f32 v39, v39, v29
s_cmp_lg_u64 vcc, 0
s_cselect_b32 s98, 2, 0
s_or_b32 s97, s97, s98
v_accvgpr_read_b32 v192, a32
v_accvgpr_read_b32 v193, a33
ds_write_b128 v17, a[40:43] offset:4096
v_accvgpr_read_b32 v194, a34
v_accvgpr_read_b32 v195, a35
v_accvgpr_read_b32 v196, a36
v_accvgpr_read_b32 v197, a37
v_accvgpr_read_b32 v198, a38
v_accvgpr_read_b32 v199, a39
v_accvgpr_read_b32 v200, a48
ds_write_b128 v17, a[44:47] offset:5120
v_accvgpr_read_b32 v201, a49
v_accvgpr_read_b32 v202, a50
v_accvgpr_read_b32 v203, a51
v_accvgpr_read_b32 v204, a52
v_accvgpr_read_b32 v205, a53
v_accvgpr_read_b32 v206, a54
v_accvgpr_read_b32 v207, a55
v_pk_mul_f32 v[176:177], v[176:177], v[36:37] op_sel_hi:[1,0]
ds_write_b128 v17, a[56:59] offset:6144
v_pk_mul_f32 v[178:179], v[178:179], v[36:37] op_sel_hi:[1,0]
v_pk_mul_f32 v[180:181], v[180:181], v[36:37] op_sel:[0,1] op_sel_hi:[1,1]
v_pk_mul_f32 v[182:183], v[182:183], v[36:37] op_sel:[0,1] op_sel_hi:[1,1]
v_pk_mul_f32 v[184:185], v[184:185], v[36:37] op_sel_hi:[1,0]
v_pk_mul_f32 v[186:187], v[186:187], v[36:37] op_sel_hi:[1,0]
v_pk_mul_f32 v[188:189], v[188:189], v[36:37] op_sel:[0,1] op_sel_hi:[1,1]
v_pk_mul_f32 v[190:191], v[190:191], v[36:37] op_sel:[0,1] op_sel_hi:[1,1]
ds_write_b128 v17, a[60:63] offset:7168
v_pk_mul_f32 v[192:193], v[192:193], v[36:37] op_sel_hi:[1,0]
v_pk_mul_f32 v[194:195], v[194:195], v[36:37] op_sel_hi:[1,0]
v_pk_mul_f32 v[196:197], v[196:197], v[36:37] op_sel:[0,1] op_sel_hi:[1,1]
v_pk_mul_f32 v[198:199], v[198:199], v[36:37] op_sel:[0,1] op_sel_hi:[1,1]
v_pk_mul_f32 v[200:201], v[200:201], v[36:37] op_sel_hi:[1,0]
v_pk_mul_f32 v[202:203], v[202:203], v[36:37] op_sel_hi:[1,0]
v_pk_mul_f32 v[204:205], v[204:205], v[36:37] op_sel:[0,1] op_sel_hi:[1,1]
v_pk_mul_f32 v[206:207], v[206:207], v[36:37] op_sel:[0,1] op_sel_hi:[1,1]
s_waitcnt lgkmcnt(0)
s_barrier
ds_read_b128 v[208:211], v18 offset:0
ds_read_b128 v[212:215], v18 offset:1024
ds_read_b128 v[216:219], v18 offset:2048
ds_read_b128 v[220:223], v18 offset:3072
ds_read_b128 v[224:227], v18 offset:4096
ds_read_b128 v[228:231], v18 offset:5120
ds_read_b128 v[232:235], v18 offset:6144
ds_read_b128 v[236:239], v18 offset:7168
s_waitcnt lgkmcnt(0)
s_barrier
ds_write_b128 v17, a[72:75] offset:0
v_pk_fma_f32 v[176:177], v[208:209], v[38:39], v[176:177] op_sel_hi:[1,0,1]
v_pk_fma_f32 v[178:179], v[210:211], v[38:39], v[178:179] op_sel_hi:[1,0,1]
v_pk_fma_f32 v[180:181], v[212:213], v[38:39], v[180:181] op_sel:[0,1,0] op_sel_hi:[1,1,1]
v_pk_fma_f32 v[182:183], v[214:215], v[38:39], v[182:183] op_sel:[0,1,0] op_sel_hi:[1,1,1]
ds_write_b128 v17, a[76:79] offset:1024
v_pk_fma_f32 v[184:185], v[216:217], v[38:39], v[184:185] op_sel_hi:[1,0,1]
v_pk_fma_f32 v[186:187], v[218:219], v[38:39], v[186:187] op_sel_hi:[1,0,1]
v_pk_fma_f32 v[188:189], v[220:221], v[38:39], v[188:189] op_sel:[0,1,0] op_sel_hi:[1,1,1]
v_pk_fma_f32 v[190:191], v[222:223], v[38:39], v[190:191] op_sel:[0,1,0] op_sel_hi:[1,1,1]
v_pk_fma_f32 v[192:193], v[224:225], v[38:39], v[192:193] op_sel_hi:[1,0,1]
ds_write_b128 v17, a[88:91] offset:2048
v_pk_fma_f32 v[194:195], v[226:227], v[38:39], v[194:195] op_sel_hi:[1,0,1]
v_pk_fma_f32 v[196:197], v[228:229], v[38:39], v[196:197] op_sel:[0,1,0] op_sel_hi:[1,1,1]
)ASM"
R"ASM(
v_pk_fma_f32 v[198:199], v[230:231], v[38:39], v[198:199] op_sel:[0,1,0] op_sel_hi:[1,1,1]
v_pk_fma_f32 v[200:201], v[232:233], v[38:39], v[200:201] op_sel_hi:[1,0,1]
ds_write_b128 v17, a[92:95] offset:3072
v_pk_fma_f32 v[202:203], v[234:235], v[38:39], v[202:203] op_sel_hi:[1,0,1]
v_pk_fma_f32 v[204:205], v[236:237], v[38:39], v[204:205] op_sel:[0,1,0] op_sel_hi:[1,1,1]
v_pk_fma_f32 v[206:207], v[238:239], v[38:39], v[206:207] op_sel:[0,1,0] op_sel_hi:[1,1,1]
v_cvt_pk_bf16_f32 v176, v176, v177
v_cvt_pk_bf16_f32 v177, v178, v179
ds_write_b128 v17, a[104:107] offset:4096
v_cvt_pk_bf16_f32 v178, v184, v185
v_cvt_pk_bf16_f32 v179, v186, v187
buffer_store_dwordx4 v[176:179], v2, s[8:11], s57 offen offset:0 sc1
v_cvt_pk_bf16_f32 v180, v180, v181
ds_write_b128 v17, a[108:111] offset:5120
v_cvt_pk_bf16_f32 v181, v182, v183
v_cvt_pk_bf16_f32 v182, v188, v189
v_cvt_pk_bf16_f32 v183, v190, v191
buffer_store_dwordx4 v[180:183], v2, s[8:11], s99 offen offset:0 sc1
v_cvt_pk_bf16_f32 v192, v192, v193
ds_write_b128 v17, a[120:123] offset:6144
v_cvt_pk_bf16_f32 v193, v194, v195
v_cvt_pk_bf16_f32 v194, v200, v201
v_cvt_pk_bf16_f32 v195, v202, v203
buffer_store_dwordx4 v[192:195], v2, s[8:11], s57 offen offset:1024 sc1
ds_write_b128 v17, a[124:127] offset:7168
v_cvt_pk_bf16_f32 v196, v196, v197
v_cvt_pk_bf16_f32 v197, v198, v199
v_cvt_pk_bf16_f32 v198, v204, v205
v_cvt_pk_bf16_f32 v199, v206, v207
buffer_store_dwordx4 v[196:199], v2, s[8:11], s99 offen offset:1024 sc1
s_nop 1
v_accvgpr_read_b32 v176, a64
v_accvgpr_read_b32 v177, a65
v_accvgpr_read_b32 v178, a66
v_accvgpr_read_b32 v179, a67
v_accvgpr_read_b32 v180, a68
v_accvgpr_read_b32 v181, a69
v_accvgpr_read_b32 v182, a70
v_accvgpr_read_b32 v183, a71
v_accvgpr_read_b32 v184, a80
v_accvgpr_read_b32 v185, a81
v_accvgpr_read_b32 v186, a82
v_accvgpr_read_b32 v187, a83
v_accvgpr_read_b32 v188, a84
v_accvgpr_read_b32 v189, a85
v_accvgpr_read_b32 v190, a86
v_accvgpr_read_b32 v191, a87
v_accvgpr_read_b32 v192, a96
v_accvgpr_read_b32 v193, a97
v_accvgpr_read_b32 v194, a98
v_accvgpr_read_b32 v195, a99
v_accvgpr_read_b32 v196, a100
v_accvgpr_read_b32 v197, a101
v_accvgpr_read_b32 v198, a102
v_accvgpr_read_b32 v199, a103
v_accvgpr_read_b32 v200, a112
v_accvgpr_read_b32 v201, a113
v_accvgpr_read_b32 v202, a114
v_accvgpr_read_b32 v203, a115
v_accvgpr_read_b32 v204, a116
v_accvgpr_read_b32 v205, a117
v_accvgpr_read_b32 v206, a118
v_accvgpr_read_b32 v207, a119
v_pk_mul_f32 v[176:177], v[176:177], v[36:37] op_sel_hi:[1,0]
v_pk_mul_f32 v[178:179], v[178:179], v[36:37] op_sel_hi:[1,0]
v_pk_mul_f32 v[180:181], v[180:181], v[36:37] op_sel:[0,1] op_sel_hi:[1,1]
v_pk_mul_f32 v[182:183], v[182:183], v[36:37] op_sel:[0,1] op_sel_hi:[1,1]
v_pk_mul_f32 v[184:185], v[184:185], v[36:37] op_sel_hi:[1,0]
v_pk_mul_f32 v[186:187], v[186:187], v[36:37] op_sel_hi:[1,0]
v_pk_mul_f32 v[188:189], v[188:189], v[36:37] op_sel:[0,1] op_sel_hi:[1,1]
v_pk_mul_f32 v[190:191], v[190:191], v[36:37] op_sel:[0,1] op_sel_hi:[1,1]
v_pk_mul_f32 v[192:193], v[192:193], v[36:37] op_sel_hi:[1,0]
v_pk_mul_f32 v[194:195], v[194:195], v[36:37] op_sel_hi:[1,0]
v_pk_mul_f32 v[196:197], v[196:197], v[36:37] op_sel:[0,1] op_sel_hi:[1,1]
v_pk_mul_f32 v[198:199], v[198:199], v[36:37] op_sel:[0,1] op_sel_hi:[1,1]
v_pk_mul_f32 v[200:201], v[200:201], v[36:37] op_sel_hi:[1,0]
v_pk_mul_f32 v[202:203], v[202:203], v[36:37] op_sel_hi:[1,0]
v_pk_mul_f32 v[204:205], v[204:205], v[36:37] op_sel:[0,1] op_sel_hi:[1,1]
v_pk_mul_f32 v[206:207], v[206:207], v[36:37] op_sel:[0,1] op_sel_hi:[1,1]
s_waitcnt lgkmcnt(0)
s_barrier
ds_read_b128 v[208:211], v18 offset:0
ds_read_b128 v[212:215], v18 offset:1024
ds_read_b128 v[216:219], v18 offset:2048
ds_read_b128 v[220:223], v18 offset:3072
ds_read_b128 v[224:227], v18 offset:4096
ds_read_b128 v[228:231], v18 offset:5120
ds_read_b128 v[232:235], v18 offset:6144
ds_read_b128 v[236:239], v18 offset:7168
s_waitcnt lgkmcnt(0)
v_pk_fma_f32 v[176:177], v[208:209], v[38:39], v[176:177] op_sel_hi:[1,0,1]
v_pk_fma_f32 v[178:179], v[210:211], v[38:39], v[178:179] op_sel_hi:[1,0,1]
v_pk_fma_f32 v[180:181], v[212:213], v[38:39], v[180:181] op_sel:[0,1,0] op_sel_hi:[1,1,1]
v_pk_fma_f32 v[182:183], v[214:215], v[38:39], v[182:183] op_sel:[0,1,0] op_sel_hi:[1,1,1]
v_pk_fma_f32 v[184:185], v[216:217], v[38:39], v[184:185] op_sel_hi:[1,0,1]
v_pk_fma_f32 v[186:187], v[218:219], v[38:39], v[186:187] op_sel_hi:[1,0,1]
v_pk_fma_f32 v[188:189], v[220:221], v[38:39], v[188:189] op_sel:[0,1,0] op_sel_hi:[1,1,1]
v_pk_fma_f32 v[190:191], v[222:223], v[38:39], v[190:191] op_sel:[0,1,0] op_sel_hi:[1,1,1]
v_pk_fma_f32 v[192:193], v[224:225], v[38:39], v[192:193] op_sel_hi:[1,0,1]
v_pk_fma_f32 v[194:195], v[226:227], v[38:39], v[194:195] op_sel_hi:[1,0,1]
v_pk_fma_f32 v[196:197], v[228:229], v[38:39], v[196:197] op_sel:[0,1,0] op_sel_hi:[1,1,1]
v_pk_fma_f32 v[198:199], v[230:231], v[38:39], v[198:199] op_sel:[0,1,0] op_sel_hi:[1,1,1]
v_pk_fma_f32 v[200:201], v[232:233], v[38:39], v[200:201] op_sel_hi:[1,0,1]
v_pk_fma_f32 v[202:203], v[234:235], v[38:39], v[202:203] op_sel_hi:[1,0,1]
v_pk_fma_f32 v[204:205], v[236:237], v[38:39], v[204:205] op_sel:[0,1,0] op_sel_hi:[1,1,1]
v_pk_fma_f32 v[206:207], v[238:239], v[38:39], v[206:207] op_sel:[0,1,0] op_sel_hi:[1,1,1]
v_cvt_pk_bf16_f32 v176, v176, v177
v_cvt_pk_bf16_f32 v177, v178, v179
v_cvt_pk_bf16_f32 v178, v184, v185
v_cvt_pk_bf16_f32 v179, v186, v187
buffer_store_dwordx4 v[176:179], v2, s[8:11], s57 offen offset:2048 sc1
v_cvt_pk_bf16_f32 v180, v180, v181
v_cvt_pk_bf16_f32 v181, v182, v183
v_cvt_pk_bf16_f32 v182, v188, v189
v_cvt_pk_bf16_f32 v183, v190, v191
buffer_store_dwordx4 v[180:183], v2, s[8:11], s99 offen offset:2048 sc1
v_cvt_pk_bf16_f32 v192, v192, v193
v_cvt_pk_bf16_f32 v193, v194, v195
v_cvt_pk_bf16_f32 v194, v200, v201
v_cvt_pk_bf16_f32 v195, v202, v203
buffer_store_dwordx4 v[192:195], v2, s[8:11], s57 offen offset:3072 sc1
v_cvt_pk_bf16_f32 v196, v196, v197
v_cvt_pk_bf16_f32 v197, v198, v199
v_cvt_pk_bf16_f32 v198, v204, v205
v_cvt_pk_bf16_f32 v199, v206, v207
buffer_store_dwordx4 v[196:199], v2, s[8:11], s99 offen offset:3072 sc1
s_branch .Lend_%=
.Lsp1_%=:
s_mov_b32 s15, 0x100000
.Lsq1_%=:
s_sleep 1
buffer_load_dword v73, v72, s[8:11], s88 offen sc1
s_waitcnt vmcnt(0)
v_subrev_u32 v73, s98, v73
v_cmp_lt_u32 vcc, 3, v73
s_sub_u32 s15, s15, 1
s_cmp_eq_u32 s15, 0
s_cbranch_scc1 .Lsr1_%=
s_cmp_lg_u64 vcc, 0
s_cbranch_scc1 .Lsq1_%=
s_branch .Lsr1_%=
.Lsp2_%=:
s_mov_b32 s15, 0x100000
.Lsq2_%=:
s_sleep 1
buffer_load_dword v39, v38, s[8:11], s89 offen sc1
s_waitcnt vmcnt(0)
v_subrev_u32 v39, s98, v39
v_cmp_lt_u32 vcc, 0, v39
s_sub_u32 s15, s15, 1
s_cmp_eq_u32 s15, 0
s_cbranch_scc1 .Lsr2_%=
s_cmp_lg_u64 vcc, 0
s_cbranch_scc1 .Lsq2_%=
s_branch .Lsr2_%=
.Lsp3_%=:
s_mov_b32 s15, 0x100000
.Lsq3_%=:
s_sleep 1
buffer_load_dword v33, v32, s[8:11], s88 offen sc1
s_waitcnt vmcnt(0)
v_subrev_u32 v33, s98, v33
v_cmp_lt_u32 vcc, 2, v33
s_sub_u32 s15, s15, 1
s_cmp_eq_u32 s15, 0
s_cbranch_scc1 .Lsr3_%=
s_cmp_lg_u64 vcc, 0
s_cbranch_scc1 .Lsq3_%=
s_branch .Lsr3_%=
.Lend_%=:
s_nop 1
)ASM"
        : "+{s[4:7]}"(s0), "+{s[8:11]}"(t_ws), "+{s[12:15]}"(t_c), "+{s[16:23]}"(t_kv), "+{s[24:27]}"(t_x), "+{s[28:31]}"(t_wq), "+{s[32:39]}"(t_w1), "+{s[40:47]}"(sc),
          "+{s[48:55]}"(t_d0), "+{s[56:63]}"(t_d1), "+{s[80:83]}"(sf0), "+{s[84:87]}"(sf1), "+{s[88:91]}"(sf2), "+{s[92:95]}"(t_k4), "+{v[2:17]}"(vv), "+{v[40:55]}"(vk), "+{v[76:79]}"(vo), "={s97}"(redo), "={s[100:101]}"(tg)
        :
        : "memory", "scc", "vcc", "s64", "s65", "s66", "s67", "s68", "s69", "s70", "s71", "s72", "s73", "s74", "s75", "s76", "s77", "s78", "s79", "s96", "s98", "s99", "v18", "v19", "v20", "v21", "v22", "v23", "v24", "v25", "v26", "v27", "v28", "v29", "v30", "v31", "v32", "v33", "v34", "v35", "v36", "v37", "v38", "v39", "v56", "v57", "v58", "v59", "v60", "v61", "v62", "v63", "v64", "v65", "v66", "v67", "v68", "v69", "v70", "v71", "v72", "v73", "v74", "v75", "v80", "v81", "v82", "v83", "v84", "v85", "v86", "v87", "v88", "v89", "v90", "v91", "v92", "v93", "v94", "v95", "v96", "v97", "v98", "v99", "v100", "v101", "v102", "v103", "v104", "v105", "v106", "v107", "v108", "v109", "v110", "v111", "v112", "v113", "v114", "v115", "v116", "v117", "v118", "v119", "v120", "v121", "v122", "v123", "v124", "v125", "v126", "v127", "v128", "v129", "v130", "v131", "v132", "v133", "v134", "v135", "v136", "v137", "v138", "v139", "v140", "v141", "v142", "v143", "v144", "v145", "v146", "v147", "v148", "v149", "v150", "v151", "v152", "v153", "v154", "v155", "v156", "v157", "v158", "v159", "v160", "v161", "v162", "v163", "v164", "v165", "v166", "v167", "v168", "v169", "v170", "v171", "v172", "v173", "v174", "v175", "v176", "v177", "v178", "v179", "v180", "v181", "v182", "v183", "v184", "v185", "v186", "v187", "v188", "v189", "v190", "v191", "v192", "v193", "v194", "v195", "v196", "v197", "v198", "v199", "v200", "v201", "v202", "v203", "v204", "v205", "v206", "v207", "v208", "v209", "v210", "v211", "v212", "v213", "v214", "v215", "v216", "v217", "v218", "v219", "v220", "v221", "v222", "v223", "v224", "v225", "v226", "v227", "v228", "v229", "v230", "v231", "v232", "v233", "v234", "v235", "v236", "v237", "v238", "v239", "v240", "v241", "v242", "v243", "v244", "v245", "v246", "v247", "v248", "v249", "v250", "v251", "v252", "v253", "v254", "v255", "a0", "a1", "a2", "a3", "a4", "a5", "a6", "a7", "a8", "a9", "a10", "a11", "a12", "a13", "a14", "a15", "a16", "a17", "a18", "a19", "a20", "a21", "a22", "a23", "a24", "a25", "a26", "a27", "a28", "a29", "a30", "a31", "a32", "a33", "a34", "a35", "a36", "a37", "a38", "a39", "a40", "a41", "a42", "a43", "a44", "a45", "a46", "a47", "a48", "a49", "a50", "a51", "a52", "a53", "a54", "a55", "a56", "a57", "a58", "a59", "a60", "a61", "a62", "a63", "a64", "a65", "a66", "a67", "a68", "a69", "a70", "a71", "a72", "a73", "a74", "a75", "a76", "a77", "a78", "a79", "a80", "a81", "a82", "a83", "a84", "a85", "a86", "a87", "a88", "a89", "a90", "a91", "a92", "a93", "a94", "a95", "a96", "a97", "a98", "a99", "a100", "a101", "a102", "a103", "a104", "a105", "a106", "a107", "a108", "a109", "a110", "a111", "a112", "a113", "a114", "a115", "a116", "a117", "a118", "a119", "a120", "a121", "a122", "a123", "a124", "a125", "a126", "a127", "a128", "a129", "a130", "a131", "a132", "a133", "a134", "a135", "a136", "a137", "a138", "a139", "a140", "a141", "a142", "a143", "a144", "a145", "a146", "a147", "a148", "a149", "a150", "a151", "a152", "a153", "a154", "a155", "a156", "a157", "a158", "a159", "a160", "a161", "a162", "a163", "a164", "a165", "a166", "a167", "a168", "a169", "a170", "a171", "a172", "a173", "a174", "a175", "a176", "a177", "a178", "a179", "a180", "a181", "a182", "a183", "a184", "a185", "a186", "a187", "a188", "a189", "a190", "a191", "a192", "a193", "a194", "a195", "a196", "a197", "a198", "a199", "a200", "a201", "a202", "a203", "a204", "a205", "a206", "a207", "a208", "a209", "a210", "a211", "a212", "a213", "a214", "a215", "a216", "a217", "a218", "a219", "a220", "a221", "a222", "a223", "a224", "a225", "a226", "a227", "a228", "a229", "a230", "a231", "a232", "a233", "a234", "a235", "a236", "a237", "a238", "a239", "a240", "a241", "a242", "a243", "a244", "a245", "a246", "a247", "a248", "a249", "a250", "a251", "a252", "a253", "a254", "a255");
#define RFL(x) ((unsigned)__builtin_amdgcn_readfirstlane((int)(x)))
    const int h = (512u * 8192u - RFL(t_wq[2])) >> 8, w = RFL(t_d0[3]) >> 10, blk2 = RFL(t_d0[7]), jp = 15 - blk2, L = vv[0] >> 4;
    if (RFL(redo)) {
        GAS char* ws2 = (GAS char*)(((unsigned long long)RFL(t_ws[1]) << 32) | RFL(t_ws[0]));
        const GAS u16* wq2 = (const GAS u16*)((((unsigned long long)RFL(t_wq[1]) << 32) | RFL(t_wq[0])) - h * 256);
        asm volatile("s_waitcnt vmcnt(0)" ::: "memory");
#pragma nounroll
        for (int b = 0; b < 4; ++b) if ((RFL(redo) >> (b >> 1)) & 1) attn_safe(wq2, ws2, h, 2 * (4 * ((b >> 1) ? blk2 : jp) + w) + (b & 1), L);
    }
    u32x8 u_bo, u_d1; u32x16 uv;
    {
        const int hh = L >> 5, kq = h >> 3, t = h & 7, rblk = (t >> 2) ? blk2 : jp, nt = t & 3, wq_ = w >> 1, wn = w & 1, i16 = L & 15, g = L >> 4, q = i16 >> 2, p = L & 3;
        u_bo[0] = RFL(s0[0]); u_bo[1] = RFL(s0[1]) & 0xffffu; u_bo[2] = 1024u; u_bo[3] = 0x20000u; u_bo[4] = RFL(s0[2]); u_bo[5] = RFL(s0[3]) & 0xffffu; u_bo[6] = 2097152u; u_bo[7] = 0x20000u;
        const unsigned cfb = (unsigned)WS_CF + jp * 8192u;
        u_d1[0] = (unsigned)WS_AF + (8u * rblk * KS3 + 128 + 11 * kq + (kq == 3 ? 9 : 10)) * 1024u; u_d1[1] = (unsigned)WS_AF + (8u * rblk * KS3 + 32u * kq) * 1024u; u_d1[2] = 32u * kq * 32768u;
        u_d1[3] = (unsigned)WS_PX + (jp * 8 + t) * 131072u + kq * 32768u + w * 8192u; u_d1[4] = cfb + kq * 2048u; u_d1[5] = cfb + t * 256u; u_d1[6] = 0; u_d1[7] = 0;
        uv[0] = L * 16; uv[1] = L * 16 + 2 * w * KS3 * 1024; uv[2] = uv[1] + KS3 * 1024 - 1024;
        uv[3] = (8 * wq_ + ((L >> 2) & 7)) * 1024 + (128 * nt + 64 * wn + 32 * hh) * 2 + 16 * (p ^ (2 * (g & 1))); uv[4] = uv[3] + 16 * 1024 - 2048;
        uv[5] = 8192 + 1024 * wn + 4096 * (g >> 1) + 256 * (g & 1) + 64 * q + 8 * p + 32 * (g & 1); uv[6] = uv[5] ^ 32;
#pragma unroll
        for (int i = 0; i < 4; ++i) uv[7 + i] = (4 * wq_ + (i ^ kq)) * 1024 + L * 16;
        uv[11] = (128 * rblk + 64 * wq_ + 16 * kq + i16) * 1024 + (128 * nt + 64 * wn + 4 * g) * 2; uv[12] = (128 * nt + 64 * wn + 4 * g) * 2; uv[13] = L; uv[14] = L; uv[15] = L;
    }
    asm volatile(
R"ASM(
v_add_u32 v18, 65536, v7
v_add_u32 v19, 65536, v8
v_add_u32 v20, 65536, v9
v_add_u32 v21, 65536, v10
v_add_u32 v22, 65536, v11
v_add_u32 v23, 65536, v12
s_waitcnt vmcnt(0)
s_barrier
s_add_u32 s98, s100, 3
v_mov_b32 v178, s98
v_mov_b32 v179, 0
buffer_store_dword v178, v179, s[8:11], s84 offen sc1
ds_read_b64_tr_b16 v[64:65], v7 offset:0
ds_read_b64_tr_b16 v[66:67], v7 offset:2048
ds_read_b128 v[32:35], v9 offset:0
ds_read_b64_tr_b16 v[68:69], v8 offset:0
ds_read_b64_tr_b16 v[70:71], v8 offset:2048
ds_read_b128 v[36:39], v10 offset:0
ds_read_b64_tr_b16 v[72:73], v7 offset:512
ds_read_b64_tr_b16 v[74:75], v7 offset:2560
ds_read_b128 v[40:43], v11 offset:0
ds_read_b64_tr_b16 v[76:77], v8 offset:512
ds_read_b64_tr_b16 v[78:79], v8 offset:2560
ds_read_b128 v[44:47], v12 offset:0
s_add_u32 m0, s50, 73728
s_add_u32 s93, s91, 131072
buffer_load_dwordx4 v5, s[44:47], s93 offen lds
buffer_load_dwordx4 v6, s[44:47], s93 offen offset:2048 lds
s_add_u32 m0, s94, 65536
s_add_u32 s92, s90, 4096
buffer_load_dwordx4 v3, s[8:11], s92 offen sc1 lds
buffer_load_dwordx4 v4, s[8:11], s92 offen offset:1024 sc1 lds
s_add_u32 m0, s50, 90112
s_add_u32 s93, s91, 163840
buffer_load_dwordx4 v5, s[44:47], s93 offen lds
buffer_load_dwordx4 v6, s[44:47], s93 offen offset:2048 lds
s_add_u32 m0, s94, 81920
s_add_u32 s92, s90, 5120
buffer_load_dwordx4 v3, s[8:11], s92 offen sc1 lds
buffer_load_dwordx4 v4, s[8:11], s92 offen offset:1024 sc1 lds
s_add_u32 m0, s50, 106496
s_add_u32 s93, s91, 196608
buffer_load_dwordx4 v5, s[44:47], s93 offen lds
buffer_load_dwordx4 v6, s[44:47], s93 offen offset:2048 lds
s_add_u32 m0, s94, 98304
s_add_u32 s92, s90, 6144
buffer_load_dwordx4 v3, s[8:11], s92 offen sc1 lds
buffer_load_dwordx4 v4, s[8:11], s92 offen offset:1024 sc1 lds
s_waitcnt vmcnt(63) lgkmcnt(0)
s_barrier
v_mfma_f32_16x16x32_bf16 v[96:99], v[64:67], v[32:35], 0
ds_read_b64_tr_b16 v[80:81], v7 offset:16384
ds_read_b64_tr_b16 v[82:83], v7 offset:18432
v_mfma_f32_16x16x32_bf16 v[112:115], v[64:67], v[36:39], 0
ds_read_b128 v[48:51], v9 offset:16384
v_mfma_f32_16x16x32_bf16 v[128:131], v[64:67], v[40:43], 0
ds_read_b64_tr_b16 v[84:85], v8 offset:16384
ds_read_b64_tr_b16 v[86:87], v8 offset:18432
v_mfma_f32_16x16x32_bf16 v[144:147], v[64:67], v[44:47], 0
ds_read_b128 v[52:55], v10 offset:16384
v_mfma_f32_16x16x32_bf16 v[100:103], v[68:71], v[32:35], 0
ds_read_b64_tr_b16 v[88:89], v7 offset:16896
ds_read_b64_tr_b16 v[90:91], v7 offset:18944
v_mfma_f32_16x16x32_bf16 v[116:119], v[68:71], v[36:39], 0
ds_read_b128 v[56:59], v11 offset:16384
v_mfma_f32_16x16x32_bf16 v[132:135], v[68:71], v[40:43], 0
ds_read_b64_tr_b16 v[92:93], v8 offset:16896
ds_read_b64_tr_b16 v[94:95], v8 offset:18944
v_mfma_f32_16x16x32_bf16 v[148:151], v[68:71], v[44:47], 0
ds_read_b128 v[60:63], v12 offset:16384
v_mfma_f32_16x16x32_bf16 v[104:107], v[72:75], v[32:35], 0
v_mfma_f32_16x16x32_bf16 v[120:123], v[72:75], v[36:39], 0
s_add_u32 m0, s50, 122880
s_add_u32 s93, s91, 229376
v_mfma_f32_16x16x32_bf16 v[136:139], v[72:75], v[40:43], 0
buffer_load_dwordx4 v5, s[44:47], s93 offen lds
v_mfma_f32_16x16x32_bf16 v[152:155], v[72:75], v[44:47], 0
buffer_load_dwordx4 v6, s[44:47], s93 offen offset:2048 lds
s_add_u32 m0, s94, 114688
s_add_u32 s92, s90, 7168
v_mfma_f32_16x16x32_bf16 v[108:111], v[76:79], v[32:35], 0
buffer_load_dwordx4 v3, s[8:11], s92 offen sc1 lds
v_mfma_f32_16x16x32_bf16 v[124:127], v[76:79], v[36:39], 0
buffer_load_dwordx4 v4, s[8:11], s92 offen offset:1024 sc1 lds
v_mfma_f32_16x16x32_bf16 v[140:143], v[76:79], v[40:43], 0
v_mfma_f32_16x16x32_bf16 v[156:159], v[76:79], v[44:47], 0
s_waitcnt vmcnt(63) lgkmcnt(0)
s_barrier
v_mfma_f32_16x16x32_bf16 v[96:99], v[80:83], v[48:51], v[96:99]
ds_read_b64_tr_b16 v[64:65], v7 offset:32768
ds_read_b64_tr_b16 v[66:67], v7 offset:34816
v_mfma_f32_16x16x32_bf16 v[112:115], v[80:83], v[52:55], v[112:115]
ds_read_b128 v[32:35], v9 offset:32768
v_mfma_f32_16x16x32_bf16 v[128:131], v[80:83], v[56:59], v[128:131]
ds_read_b64_tr_b16 v[68:69], v8 offset:32768
ds_read_b64_tr_b16 v[70:71], v8 offset:34816
v_mfma_f32_16x16x32_bf16 v[144:147], v[80:83], v[60:63], v[144:147]
ds_read_b128 v[36:39], v10 offset:32768
v_mfma_f32_16x16x32_bf16 v[100:103], v[84:87], v[48:51], v[100:103]
ds_read_b64_tr_b16 v[72:73], v7 offset:33280
ds_read_b64_tr_b16 v[74:75], v7 offset:35328
v_mfma_f32_16x16x32_bf16 v[116:119], v[84:87], v[52:55], v[116:119]
ds_read_b128 v[40:43], v11 offset:32768
v_mfma_f32_16x16x32_bf16 v[132:135], v[84:87], v[56:59], v[132:135]
ds_read_b64_tr_b16 v[76:77], v8 offset:33280
ds_read_b64_tr_b16 v[78:79], v8 offset:35328
v_mfma_f32_16x16x32_bf16 v[148:151], v[84:87], v[60:63], v[148:151]
ds_read_b128 v[44:47], v12 offset:32768
v_mfma_f32_16x16x32_bf16 v[104:107], v[88:91], v[48:51], v[104:107]
v_mfma_f32_16x16x32_bf16 v[120:123], v[88:91], v[52:55], v[120:123]
s_add_u32 m0, s50, 8192
s_add_u32 s93, s91, 262144
v_mfma_f32_16x16x32_bf16 v[136:139], v[88:91], v[56:59], v[136:139]
buffer_load_dwordx4 v5, s[44:47], s93 offen lds
v_mfma_f32_16x16x32_bf16 v[152:155], v[88:91], v[60:63], v[152:155]
buffer_load_dwordx4 v6, s[44:47], s93 offen offset:2048 lds
s_add_u32 m0, s94, 0
s_add_u32 s92, s90, 8192
v_mfma_f32_16x16x32_bf16 v[108:111], v[92:95], v[48:51], v[108:111]
buffer_load_dwordx4 v3, s[8:11], s92 offen sc1 lds
v_mfma_f32_16x16x32_bf16 v[124:127], v[92:95], v[52:55], v[124:127]
buffer_load_dwordx4 v4, s[8:11], s92 offen offset:1024 sc1 lds
v_mfma_f32_16x16x32_bf16 v[140:143], v[92:95], v[56:59], v[140:143]
v_mfma_f32_16x16x32_bf16 v[156:159], v[92:95], v[60:63], v[156:159]
s_waitcnt vmcnt(63) lgkmcnt(0)
s_barrier
v_mfma_f32_16x16x32_bf16 v[96:99], v[64:67], v[32:35], v[96:99]
ds_read_b64_tr_b16 v[80:81], v7 offset:49152
ds_read_b64_tr_b16 v[82:83], v7 offset:51200
v_mfma_f32_16x16x32_bf16 v[112:115], v[64:67], v[36:39], v[112:115]
ds_read_b128 v[48:51], v9 offset:49152
v_mfma_f32_16x16x32_bf16 v[128:131], v[64:67], v[40:43], v[128:131]
ds_read_b64_tr_b16 v[84:85], v8 offset:49152
ds_read_b64_tr_b16 v[86:87], v8 offset:51200
v_mfma_f32_16x16x32_bf16 v[144:147], v[64:67], v[44:47], v[144:147]
ds_read_b128 v[52:55], v10 offset:49152
v_mfma_f32_16x16x32_bf16 v[100:103], v[68:71], v[32:35], v[100:103]
ds_read_b64_tr_b16 v[88:89], v7 offset:49664
ds_read_b64_tr_b16 v[90:91], v7 offset:51712
v_mfma_f32_16x16x32_bf16 v[116:119], v[68:71], v[36:39], v[116:119]
ds_read_b128 v[56:59], v11 offset:49152
v_mfma_f32_16x16x32_bf16 v[132:135], v[68:71], v[40:43], v[132:135]
ds_read_b64_tr_b16 v[92:93], v8 offset:49664
ds_read_b64_tr_b16 v[94:95], v8 offset:51712
v_mfma_f32_16x16x32_bf16 v[148:151], v[68:71], v[44:47], v[148:151]
ds_read_b128 v[60:63], v12 offset:49152
v_mfma_f32_16x16x32_bf16 v[104:107], v[72:75], v[32:35], v[104:107]
v_mfma_f32_16x16x32_bf16 v[120:123], v[72:75], v[36:39], v[120:123]
s_add_u32 m0, s50, 24576
s_add_u32 s93, s91, 294912
v_mfma_f32_16x16x32_bf16 v[136:139], v[72:75], v[40:43], v[136:139]
buffer_load_dwordx4 v5, s[44:47], s93 offen lds
v_mfma_f32_16x16x32_bf16 v[152:155], v[72:75], v[44:47], v[152:155]
buffer_load_dwordx4 v6, s[44:47], s93 offen offset:2048 lds
s_add_u32 m0, s94, 16384
s_add_u32 s92, s90, 9216
v_mfma_f32_16x16x32_bf16 v[108:111], v[76:79], v[32:35], v[108:111]
buffer_load_dwordx4 v3, s[8:11], s92 offen sc1 lds
v_mfma_f32_16x16x32_bf16 v[124:127], v[76:79], v[36:39], v[124:127]
buffer_load_dwordx4 v4, s[8:11], s92 offen offset:1024 sc1 lds
v_mfma_f32_16x16x32_bf16 v[140:143], v[76:79], v[40:43], v[140:143]
v_mfma_f32_16x16x32_bf16 v[156:159], v[76:79], v[44:47], v[156:159]
s_waitcnt vmcnt(20) lgkmcnt(0)
s_barrier
v_mfma_f32_16x16x32_bf16 v[96:99], v[80:83], v[48:51], v[96:99]
ds_read_b64_tr_b16 v[64:65], v18 offset:0
ds_read_b64_tr_b16 v[66:67], v18 offset:2048
v_mfma_f32_16x16x32_bf16 v[112:115], v[80:83], v[52:55], v[112:115]
ds_read_b128 v[32:35], v20 offset:0
v_mfma_f32_16x16x32_bf16 v[128:131], v[80:83], v[56:59], v[128:131]
ds_read_b64_tr_b16 v[68:69], v19 offset:0
ds_read_b64_tr_b16 v[70:71], v19 offset:2048
v_mfma_f32_16x16x32_bf16 v[144:147], v[80:83], v[60:63], v[144:147]
ds_read_b128 v[36:39], v21 offset:0
v_mfma_f32_16x16x32_bf16 v[100:103], v[84:87], v[48:51], v[100:103]
ds_read_b64_tr_b16 v[72:73], v18 offset:512
ds_read_b64_tr_b16 v[74:75], v18 offset:2560
v_mfma_f32_16x16x32_bf16 v[116:119], v[84:87], v[52:55], v[116:119]
ds_read_b128 v[40:43], v22 offset:0
v_mfma_f32_16x16x32_bf16 v[132:135], v[84:87], v[56:59], v[132:135]
ds_read_b64_tr_b16 v[76:77], v19 offset:512
ds_read_b64_tr_b16 v[78:79], v19 offset:2560
v_mfma_f32_16x16x32_bf16 v[148:151], v[84:87], v[60:63], v[148:151]
ds_read_b128 v[44:47], v23 offset:0
v_mfma_f32_16x16x32_bf16 v[104:107], v[88:91], v[48:51], v[104:107]
v_and_b32 v178, 112, v2
v_lshlrev_b32 v178, 4, v178
buffer_load_dword v179, v178, s[8:11], s60 offen sc1
v_mfma_f32_16x16x32_bf16 v[120:123], v[88:91], v[52:55], v[120:123]
s_add_u32 m0, s50, 40960
s_add_u32 s93, s91, 327680
v_mfma_f32_16x16x32_bf16 v[136:139], v[88:91], v[56:59], v[136:139]
buffer_load_dwordx4 v5, s[44:47], s93 offen lds
v_mfma_f32_16x16x32_bf16 v[152:155], v[88:91], v[60:63], v[152:155]
buffer_load_dwordx4 v6, s[44:47], s93 offen offset:2048 lds
s_add_u32 m0, s94, 32768
s_mov_b32 s92, s56
v_mfma_f32_16x16x32_bf16 v[108:111], v[92:95], v[48:51], v[108:111]
buffer_load_dwordx4 v3, s[8:11], s92 offen sc1 lds
v_mfma_f32_16x16x32_bf16 v[124:127], v[92:95], v[52:55], v[124:127]
)ASM"
R"ASM(
buffer_load_dwordx4 v4, s[8:11], s92 offen offset:1024 sc1 lds
v_mfma_f32_16x16x32_bf16 v[140:143], v[92:95], v[56:59], v[140:143]
v_mfma_f32_16x16x32_bf16 v[156:159], v[92:95], v[60:63], v[156:159]
s_waitcnt vmcnt(4) lgkmcnt(0)
s_barrier
s_add_u32 s98, s100, 3
v_subrev_u32 v179, s98, v179
v_cmp_lt_u32 vcc, 1, v179
s_cmp_lg_u64 vcc, 0
s_cbranch_scc1 .Lsp4_%=
.Lsr4_%=:
v_mfma_f32_16x16x32_bf16 v[96:99], v[64:67], v[32:35], v[96:99]
ds_read_b64_tr_b16 v[80:81], v18 offset:16384
ds_read_b64_tr_b16 v[82:83], v18 offset:18432
v_mfma_f32_16x16x32_bf16 v[112:115], v[64:67], v[36:39], v[112:115]
ds_read_b128 v[48:51], v20 offset:16384
v_mfma_f32_16x16x32_bf16 v[128:131], v[64:67], v[40:43], v[128:131]
ds_read_b64_tr_b16 v[84:85], v19 offset:16384
ds_read_b64_tr_b16 v[86:87], v19 offset:18432
v_mfma_f32_16x16x32_bf16 v[144:147], v[64:67], v[44:47], v[144:147]
ds_read_b128 v[52:55], v21 offset:16384
v_mfma_f32_16x16x32_bf16 v[100:103], v[68:71], v[32:35], v[100:103]
ds_read_b64_tr_b16 v[88:89], v18 offset:16896
ds_read_b64_tr_b16 v[90:91], v18 offset:18944
v_mfma_f32_16x16x32_bf16 v[116:119], v[68:71], v[36:39], v[116:119]
ds_read_b128 v[56:59], v22 offset:16384
v_mfma_f32_16x16x32_bf16 v[132:135], v[68:71], v[40:43], v[132:135]
ds_read_b64_tr_b16 v[92:93], v19 offset:16896
ds_read_b64_tr_b16 v[94:95], v19 offset:18944
v_mfma_f32_16x16x32_bf16 v[148:151], v[68:71], v[44:47], v[148:151]
ds_read_b128 v[60:63], v23 offset:16384
v_mfma_f32_16x16x32_bf16 v[104:107], v[72:75], v[32:35], v[104:107]
v_mfma_f32_16x16x32_bf16 v[120:123], v[72:75], v[36:39], v[120:123]
s_add_u32 m0, s50, 57344
s_add_u32 s93, s58, 0
v_mfma_f32_16x16x32_bf16 v[136:139], v[72:75], v[40:43], v[136:139]
buffer_load_dwordx4 v5, s[40:43], s93 offen lds
v_mfma_f32_16x16x32_bf16 v[152:155], v[72:75], v[44:47], v[152:155]
buffer_load_dwordx4 v6, s[40:43], s93 offen offset:2048 lds
s_add_u32 m0, s94, 49152
s_add_u32 s92, s57, 0
v_mfma_f32_16x16x32_bf16 v[108:111], v[76:79], v[32:35], v[108:111]
buffer_load_dwordx4 v3, s[8:11], s92 offen sc1 lds
v_mfma_f32_16x16x32_bf16 v[124:127], v[76:79], v[36:39], v[124:127]
buffer_load_dwordx4 v4, s[8:11], s92 offen offset:1024 sc1 lds
v_mfma_f32_16x16x32_bf16 v[140:143], v[76:79], v[40:43], v[140:143]
v_mfma_f32_16x16x32_bf16 v[156:159], v[76:79], v[44:47], v[156:159]
s_waitcnt vmcnt(21) lgkmcnt(0)
s_barrier
v_mfma_f32_16x16x32_bf16 v[96:99], v[80:83], v[48:51], v[96:99]
ds_read_b64_tr_b16 v[64:65], v18 offset:32768
ds_read_b64_tr_b16 v[66:67], v18 offset:34816
v_mfma_f32_16x16x32_bf16 v[112:115], v[80:83], v[52:55], v[112:115]
ds_read_b128 v[32:35], v20 offset:32768
v_mfma_f32_16x16x32_bf16 v[128:131], v[80:83], v[56:59], v[128:131]
ds_read_b64_tr_b16 v[68:69], v19 offset:32768
ds_read_b64_tr_b16 v[70:71], v19 offset:34816
v_mfma_f32_16x16x32_bf16 v[144:147], v[80:83], v[60:63], v[144:147]
ds_read_b128 v[36:39], v21 offset:32768
v_mfma_f32_16x16x32_bf16 v[100:103], v[84:87], v[48:51], v[100:103]
ds_read_b64_tr_b16 v[72:73], v18 offset:33280
ds_read_b64_tr_b16 v[74:75], v18 offset:35328
v_mfma_f32_16x16x32_bf16 v[116:119], v[84:87], v[52:55], v[116:119]
ds_read_b128 v[40:43], v22 offset:32768
v_mfma_f32_16x16x32_bf16 v[132:135], v[84:87], v[56:59], v[132:135]
ds_read_b64_tr_b16 v[76:77], v19 offset:33280
ds_read_b64_tr_b16 v[78:79], v19 offset:35328
v_mfma_f32_16x16x32_bf16 v[148:151], v[84:87], v[60:63], v[148:151]
ds_read_b128 v[44:47], v23 offset:32768
v_mfma_f32_16x16x32_bf16 v[104:107], v[88:91], v[48:51], v[104:107]
v_mfma_f32_16x16x32_bf16 v[120:123], v[88:91], v[52:55], v[120:123]
s_add_u32 m0, s50, 73728
s_add_u32 s93, s58, 32768
v_mfma_f32_16x16x32_bf16 v[136:139], v[88:91], v[56:59], v[136:139]
buffer_load_dwordx4 v5, s[40:43], s93 offen lds
v_mfma_f32_16x16x32_bf16 v[152:155], v[88:91], v[60:63], v[152:155]
buffer_load_dwordx4 v6, s[40:43], s93 offen offset:2048 lds
s_add_u32 m0, s94, 65536
s_add_u32 s92, s57, 1024
v_mfma_f32_16x16x32_bf16 v[108:111], v[92:95], v[48:51], v[108:111]
buffer_load_dwordx4 v3, s[8:11], s92 offen sc1 lds
v_mfma_f32_16x16x32_bf16 v[124:127], v[92:95], v[52:55], v[124:127]
buffer_load_dwordx4 v4, s[8:11], s92 offen offset:1024 sc1 lds
v_mfma_f32_16x16x32_bf16 v[140:143], v[92:95], v[56:59], v[140:143]
v_mfma_f32_16x16x32_bf16 v[156:159], v[92:95], v[60:63], v[156:159]
s_waitcnt vmcnt(21) lgkmcnt(0)
s_barrier
v_mfma_f32_16x16x32_bf16 v[96:99], v[64:67], v[32:35], v[96:99]
ds_read_b64_tr_b16 v[80:81], v18 offset:49152
ds_read_b64_tr_b16 v[82:83], v18 offset:51200
v_mfma_f32_16x16x32_bf16 v[112:115], v[64:67], v[36:39], v[112:115]
ds_read_b128 v[48:51], v20 offset:49152
v_mfma_f32_16x16x32_bf16 v[128:131], v[64:67], v[40:43], v[128:131]
ds_read_b64_tr_b16 v[84:85], v19 offset:49152
ds_read_b64_tr_b16 v[86:87], v19 offset:51200
v_mfma_f32_16x16x32_bf16 v[144:147], v[64:67], v[44:47], v[144:147]
ds_read_b128 v[52:55], v21 offset:49152
v_mfma_f32_16x16x32_bf16 v[100:103], v[68:71], v[32:35], v[100:103]
ds_read_b64_tr_b16 v[88:89], v18 offset:49664
ds_read_b64_tr_b16 v[90:91], v18 offset:51712
v_mfma_f32_16x16x32_bf16 v[116:119], v[68:71], v[36:39], v[116:119]
ds_read_b128 v[56:59], v22 offset:49152
v_mfma_f32_16x16x32_bf16 v[132:135], v[68:71], v[40:43], v[132:135]
ds_read_b64_tr_b16 v[92:93], v19 offset:49664
ds_read_b64_tr_b16 v[94:95], v19 offset:51712
v_mfma_f32_16x16x32_bf16 v[148:151], v[68:71], v[44:47], v[148:151]
ds_read_b128 v[60:63], v23 offset:49152
v_mfma_f32_16x16x32_bf16 v[104:107], v[72:75], v[32:35], v[104:107]
v_mfma_f32_16x16x32_bf16 v[120:123], v[72:75], v[36:39], v[120:123]
s_add_u32 m0, s50, 90112
s_add_u32 s93, s58, 65536
v_mfma_f32_16x16x32_bf16 v[136:139], v[72:75], v[40:43], v[136:139]
buffer_load_dwordx4 v5, s[40:43], s93 offen lds
v_mfma_f32_16x16x32_bf16 v[152:155], v[72:75], v[44:47], v[152:155]
buffer_load_dwordx4 v6, s[40:43], s93 offen offset:2048 lds
s_add_u32 m0, s94, 81920
s_add_u32 s92, s57, 2048
v_mfma_f32_16x16x32_bf16 v[108:111], v[76:79], v[32:35], v[108:111]
buffer_load_dwordx4 v3, s[8:11], s92 offen sc1 lds
v_mfma_f32_16x16x32_bf16 v[124:127], v[76:79], v[36:39], v[124:127]
buffer_load_dwordx4 v4, s[8:11], s92 offen offset:1024 sc1 lds
v_mfma_f32_16x16x32_bf16 v[140:143], v[76:79], v[40:43], v[140:143]
v_mfma_f32_16x16x32_bf16 v[156:159], v[76:79], v[44:47], v[156:159]
s_waitcnt vmcnt(21) lgkmcnt(0)
s_barrier
v_mfma_f32_16x16x32_bf16 v[96:99], v[80:83], v[48:51], v[96:99]
ds_read_b64_tr_b16 v[64:65], v7 offset:0
ds_read_b64_tr_b16 v[66:67], v7 offset:2048
v_mfma_f32_16x16x32_bf16 v[112:115], v[80:83], v[52:55], v[112:115]
ds_read_b128 v[32:35], v9 offset:0
v_mfma_f32_16x16x32_bf16 v[128:131], v[80:83], v[56:59], v[128:131]
ds_read_b64_tr_b16 v[68:69], v8 offset:0
ds_read_b64_tr_b16 v[70:71], v8 offset:2048
v_mfma_f32_16x16x32_bf16 v[144:147], v[80:83], v[60:63], v[144:147]
ds_read_b128 v[36:39], v10 offset:0
v_mfma_f32_16x16x32_bf16 v[100:103], v[84:87], v[48:51], v[100:103]
ds_read_b64_tr_b16 v[72:73], v7 offset:512
ds_read_b64_tr_b16 v[74:75], v7 offset:2560
v_mfma_f32_16x16x32_bf16 v[116:119], v[84:87], v[52:55], v[116:119]
ds_read_b128 v[40:43], v11 offset:0
v_mfma_f32_16x16x32_bf16 v[132:135], v[84:87], v[56:59], v[132:135]
ds_read_b64_tr_b16 v[76:77], v8 offset:512
ds_read_b64_tr_b16 v[78:79], v8 offset:2560
v_mfma_f32_16x16x32_bf16 v[148:151], v[84:87], v[60:63], v[148:151]
ds_read_b128 v[44:47], v12 offset:0
v_mfma_f32_16x16x32_bf16 v[104:107], v[88:91], v[48:51], v[104:107]
v_mfma_f32_16x16x32_bf16 v[120:123], v[88:91], v[52:55], v[120:123]
s_add_u32 m0, s50, 106496
s_add_u32 s93, s58, 98304
v_mfma_f32_16x16x32_bf16 v[136:139], v[88:91], v[56:59], v[136:139]
buffer_load_dwordx4 v5, s[40:43], s93 offen lds
v_mfma_f32_16x16x32_bf16 v[152:155], v[88:91], v[60:63], v[152:155]
buffer_load_dwordx4 v6, s[40:43], s93 offen offset:2048 lds
s_add_u32 m0, s94, 98304
s_add_u32 s92, s57, 3072
v_mfma_f32_16x16x32_bf16 v[108:111], v[92:95], v[48:51], v[108:111]
buffer_load_dwordx4 v3, s[8:11], s92 offen sc1 lds
v_mfma_f32_16x16x32_bf16 v[124:127], v[92:95], v[52:55], v[124:127]
buffer_load_dwordx4 v4, s[8:11], s92 offen offset:1024 sc1 lds
v_mfma_f32_16x16x32_bf16 v[140:143], v[92:95], v[56:59], v[140:143]
v_mfma_f32_16x16x32_bf16 v[156:159], v[92:95], v[60:63], v[156:159]
s_waitcnt vmcnt(21) lgkmcnt(0)
s_barrier
v_mfma_f32_16x16x32_bf16 v[96:99], v[64:67], v[32:35], v[96:99]
ds_read_b64_tr_b16 v[80:81], v7 offset:16384
ds_read_b64_tr_b16 v[82:83], v7 offset:18432
v_mfma_f32_16x16x32_bf16 v[112:115], v[64:67], v[36:39], v[112:115]
ds_read_b128 v[48:51], v9 offset:16384
v_mfma_f32_16x16x32_bf16 v[128:131], v[64:67], v[40:43], v[128:131]
ds_read_b64_tr_b16 v[84:85], v8 offset:16384
ds_read_b64_tr_b16 v[86:87], v8 offset:18432
v_mfma_f32_16x16x32_bf16 v[144:147], v[64:67], v[44:47], v[144:147]
ds_read_b128 v[52:55], v10 offset:16384
v_mfma_f32_16x16x32_bf16 v[100:103], v[68:71], v[32:35], v[100:103]
ds_read_b64_tr_b16 v[88:89], v7 offset:16896
ds_read_b64_tr_b16 v[90:91], v7 offset:18944
v_mfma_f32_16x16x32_bf16 v[116:119], v[68:71], v[36:39], v[116:119]
ds_read_b128 v[56:59], v11 offset:16384
v_mfma_f32_16x16x32_bf16 v[132:135], v[68:71], v[40:43], v[132:135]
ds_read_b64_tr_b16 v[92:93], v8 offset:16896
ds_read_b64_tr_b16 v[94:95], v8 offset:18944
v_mfma_f32_16x16x32_bf16 v[148:151], v[68:71], v[44:47], v[148:151]
ds_read_b128 v[60:63], v12 offset:16384
v_mfma_f32_16x16x32_bf16 v[104:107], v[72:75], v[32:35], v[104:107]
v_mfma_f32_16x16x32_bf16 v[120:123], v[72:75], v[36:39], v[120:123]
s_add_u32 m0, s50, 122880
s_add_u32 s93, s58, 131072
v_mfma_f32_16x16x32_bf16 v[136:139], v[72:75], v[40:43], v[136:139]
buffer_load_dwordx4 v5, s[40:43], s93 offen lds
v_mfma_f32_16x16x32_bf16 v[152:155], v[72:75], v[44:47], v[152:155]
buffer_load_dwordx4 v6, s[40:43], s93 offen offset:2048 lds
s_add_u32 m0, s94, 114688
s_add_u32 s92, s57, 4096
v_mfma_f32_16x16x32_bf16 v[108:111], v[76:79], v[32:35], v[108:111]
buffer_load_dwordx4 v3, s[8:11], s92 offen sc1 lds
v_mfma_f32_16x16x32_bf16 v[124:127], v[76:79], v[36:39], v[124:127]
buffer_load_dwordx4 v4, s[8:11], s92 offen offset:1024 sc1 lds
v_mfma_f32_16x16x32_bf16 v[140:143], v[76:79], v[40:43], v[140:143]
v_mfma_f32_16x16x32_bf16 v[156:159], v[76:79], v[44:47], v[156:159]
s_waitcnt vmcnt(20) lgkmcnt(0)
)ASM"
R"ASM(
s_barrier
v_mfma_f32_16x16x32_bf16 v[96:99], v[80:83], v[48:51], v[96:99]
ds_read_b64_tr_b16 v[64:65], v7 offset:32768
ds_read_b64_tr_b16 v[66:67], v7 offset:34816
v_mfma_f32_16x16x32_bf16 v[112:115], v[80:83], v[52:55], v[112:115]
ds_read_b128 v[32:35], v9 offset:32768
v_mfma_f32_16x16x32_bf16 v[128:131], v[80:83], v[56:59], v[128:131]
ds_read_b64_tr_b16 v[68:69], v8 offset:32768
ds_read_b64_tr_b16 v[70:71], v8 offset:34816
v_mfma_f32_16x16x32_bf16 v[144:147], v[80:83], v[60:63], v[144:147]
ds_read_b128 v[36:39], v10 offset:32768
v_mfma_f32_16x16x32_bf16 v[100:103], v[84:87], v[48:51], v[100:103]
ds_read_b64_tr_b16 v[72:73], v7 offset:33280
ds_read_b64_tr_b16 v[74:75], v7 offset:35328
v_mfma_f32_16x16x32_bf16 v[116:119], v[84:87], v[52:55], v[116:119]
ds_read_b128 v[40:43], v11 offset:32768
v_mfma_f32_16x16x32_bf16 v[132:135], v[84:87], v[56:59], v[132:135]
ds_read_b64_tr_b16 v[76:77], v8 offset:33280
ds_read_b64_tr_b16 v[78:79], v8 offset:35328
v_mfma_f32_16x16x32_bf16 v[148:151], v[84:87], v[60:63], v[148:151]
ds_read_b128 v[44:47], v12 offset:32768
v_mfma_f32_16x16x32_bf16 v[104:107], v[88:91], v[48:51], v[104:107]
v_mfma_f32_16x16x32_bf16 v[120:123], v[88:91], v[52:55], v[120:123]
s_add_u32 m0, s50, 8192
s_add_u32 s93, s58, 163840
v_mfma_f32_16x16x32_bf16 v[136:139], v[88:91], v[56:59], v[136:139]
buffer_load_dwordx4 v5, s[40:43], s93 offen lds
v_mfma_f32_16x16x32_bf16 v[152:155], v[88:91], v[60:63], v[152:155]
buffer_load_dwordx4 v6, s[40:43], s93 offen offset:2048 lds
s_add_u32 m0, s94, 0
s_add_u32 s92, s57, 5120
v_mfma_f32_16x16x32_bf16 v[108:111], v[92:95], v[48:51], v[108:111]
buffer_load_dwordx4 v3, s[8:11], s92 offen sc1 lds
v_mfma_f32_16x16x32_bf16 v[124:127], v[92:95], v[52:55], v[124:127]
buffer_load_dwordx4 v4, s[8:11], s92 offen offset:1024 sc1 lds
v_mfma_f32_16x16x32_bf16 v[140:143], v[92:95], v[56:59], v[140:143]
v_mfma_f32_16x16x32_bf16 v[156:159], v[92:95], v[60:63], v[156:159]
s_waitcnt vmcnt(20) lgkmcnt(0)
s_barrier
v_mfma_f32_16x16x32_bf16 v[96:99], v[64:67], v[32:35], v[96:99]
ds_read_b64_tr_b16 v[80:81], v7 offset:49152
ds_read_b64_tr_b16 v[82:83], v7 offset:51200
v_mfma_f32_16x16x32_bf16 v[112:115], v[64:67], v[36:39], v[112:115]
ds_read_b128 v[48:51], v9 offset:49152
v_mfma_f32_16x16x32_bf16 v[128:131], v[64:67], v[40:43], v[128:131]
ds_read_b64_tr_b16 v[84:85], v8 offset:49152
ds_read_b64_tr_b16 v[86:87], v8 offset:51200
v_mfma_f32_16x16x32_bf16 v[144:147], v[64:67], v[44:47], v[144:147]
ds_read_b128 v[52:55], v10 offset:49152
v_mfma_f32_16x16x32_bf16 v[100:103], v[68:71], v[32:35], v[100:103]
ds_read_b64_tr_b16 v[88:89], v7 offset:49664
ds_read_b64_tr_b16 v[90:91], v7 offset:51712
v_mfma_f32_16x16x32_bf16 v[116:119], v[68:71], v[36:39], v[116:119]
ds_read_b128 v[56:59], v11 offset:49152
v_mfma_f32_16x16x32_bf16 v[132:135], v[68:71], v[40:43], v[132:135]
ds_read_b64_tr_b16 v[92:93], v8 offset:49664
ds_read_b64_tr_b16 v[94:95], v8 offset:51712
v_mfma_f32_16x16x32_bf16 v[148:151], v[68:71], v[44:47], v[148:151]
ds_read_b128 v[60:63], v12 offset:49152
v_mfma_f32_16x16x32_bf16 v[104:107], v[72:75], v[32:35], v[104:107]
v_mfma_f32_16x16x32_bf16 v[120:123], v[72:75], v[36:39], v[120:123]
s_add_u32 m0, s50, 24576
s_add_u32 s93, s58, 196608
v_mfma_f32_16x16x32_bf16 v[136:139], v[72:75], v[40:43], v[136:139]
buffer_load_dwordx4 v5, s[40:43], s93 offen lds
v_mfma_f32_16x16x32_bf16 v[152:155], v[72:75], v[44:47], v[152:155]
buffer_load_dwordx4 v6, s[40:43], s93 offen offset:2048 lds
s_add_u32 m0, s94, 16384
s_add_u32 s92, s57, 6144
v_mfma_f32_16x16x32_bf16 v[108:111], v[76:79], v[32:35], v[108:111]
buffer_load_dwordx4 v3, s[8:11], s92 offen sc1 lds
v_mfma_f32_16x16x32_bf16 v[124:127], v[76:79], v[36:39], v[124:127]
buffer_load_dwordx4 v4, s[8:11], s92 offen offset:1024 sc1 lds
v_mfma_f32_16x16x32_bf16 v[140:143], v[76:79], v[40:43], v[140:143]
v_mfma_f32_16x16x32_bf16 v[156:159], v[76:79], v[44:47], v[156:159]
s_waitcnt vmcnt(20) lgkmcnt(0)
s_barrier
v_mfma_f32_16x16x32_bf16 v[96:99], v[80:83], v[48:51], v[96:99]
ds_read_b64_tr_b16 v[64:65], v18 offset:0
ds_read_b64_tr_b16 v[66:67], v18 offset:2048
v_mfma_f32_16x16x32_bf16 v[112:115], v[80:83], v[52:55], v[112:115]
ds_read_b128 v[32:35], v20 offset:0
v_mfma_f32_16x16x32_bf16 v[128:131], v[80:83], v[56:59], v[128:131]
ds_read_b64_tr_b16 v[68:69], v19 offset:0
ds_read_b64_tr_b16 v[70:71], v19 offset:2048
v_mfma_f32_16x16x32_bf16 v[144:147], v[80:83], v[60:63], v[144:147]
ds_read_b128 v[36:39], v21 offset:0
v_mfma_f32_16x16x32_bf16 v[100:103], v[84:87], v[48:51], v[100:103]
ds_read_b64_tr_b16 v[72:73], v18 offset:512
ds_read_b64_tr_b16 v[74:75], v18 offset:2560
v_mfma_f32_16x16x32_bf16 v[116:119], v[84:87], v[52:55], v[116:119]
ds_read_b128 v[40:43], v22 offset:0
v_mfma_f32_16x16x32_bf16 v[132:135], v[84:87], v[56:59], v[132:135]
ds_read_b64_tr_b16 v[76:77], v19 offset:512
ds_read_b64_tr_b16 v[78:79], v19 offset:2560
v_mfma_f32_16x16x32_bf16 v[148:151], v[84:87], v[60:63], v[148:151]
ds_read_b128 v[44:47], v23 offset:0
v_mfma_f32_16x16x32_bf16 v[104:107], v[88:91], v[48:51], v[104:107]
v_mfma_f32_16x16x32_bf16 v[120:123], v[88:91], v[52:55], v[120:123]
s_add_u32 m0, s50, 40960
s_add_u32 s93, s58, 229376
v_mfma_f32_16x16x32_bf16 v[136:139], v[88:91], v[56:59], v[136:139]
buffer_load_dwordx4 v5, s[40:43], s93 offen lds
v_mfma_f32_16x16x32_bf16 v[152:155], v[88:91], v[60:63], v[152:155]
buffer_load_dwordx4 v6, s[40:43], s93 offen offset:2048 lds
s_add_u32 m0, s94, 32768
s_add_u32 s92, s57, 7168
v_mfma_f32_16x16x32_bf16 v[108:111], v[92:95], v[48:51], v[108:111]
buffer_load_dwordx4 v3, s[8:11], s92 offen sc1 lds
v_mfma_f32_16x16x32_bf16 v[124:127], v[92:95], v[52:55], v[124:127]
buffer_load_dwordx4 v4, s[8:11], s92 offen offset:1024 sc1 lds
v_mfma_f32_16x16x32_bf16 v[140:143], v[92:95], v[56:59], v[140:143]
v_mfma_f32_16x16x32_bf16 v[156:159], v[92:95], v[60:63], v[156:159]
s_waitcnt vmcnt(20) lgkmcnt(0)
s_barrier
v_mfma_f32_16x16x32_bf16 v[96:99], v[64:67], v[32:35], v[96:99]
ds_read_b64_tr_b16 v[80:81], v18 offset:16384
ds_read_b64_tr_b16 v[82:83], v18 offset:18432
v_mfma_f32_16x16x32_bf16 v[112:115], v[64:67], v[36:39], v[112:115]
ds_read_b128 v[48:51], v20 offset:16384
v_mfma_f32_16x16x32_bf16 v[128:131], v[64:67], v[40:43], v[128:131]
ds_read_b64_tr_b16 v[84:85], v19 offset:16384
ds_read_b64_tr_b16 v[86:87], v19 offset:18432
v_mfma_f32_16x16x32_bf16 v[144:147], v[64:67], v[44:47], v[144:147]
ds_read_b128 v[52:55], v21 offset:16384
v_mfma_f32_16x16x32_bf16 v[100:103], v[68:71], v[32:35], v[100:103]
ds_read_b64_tr_b16 v[88:89], v18 offset:16896
ds_read_b64_tr_b16 v[90:91], v18 offset:18944
v_mfma_f32_16x16x32_bf16 v[116:119], v[68:71], v[36:39], v[116:119]
ds_read_b128 v[56:59], v22 offset:16384
v_mfma_f32_16x16x32_bf16 v[132:135], v[68:71], v[40:43], v[132:135]
ds_read_b64_tr_b16 v[92:93], v19 offset:16896
ds_read_b64_tr_b16 v[94:95], v19 offset:18944
v_mfma_f32_16x16x32_bf16 v[148:151], v[68:71], v[44:47], v[148:151]
ds_read_b128 v[60:63], v23 offset:16384
v_mfma_f32_16x16x32_bf16 v[104:107], v[72:75], v[32:35], v[104:107]
v_mfma_f32_16x16x32_bf16 v[120:123], v[72:75], v[36:39], v[120:123]
s_add_u32 m0, s50, 57344
s_add_u32 s93, s58, 262144
v_mfma_f32_16x16x32_bf16 v[136:139], v[72:75], v[40:43], v[136:139]
buffer_load_dwordx4 v5, s[40:43], s93 offen lds
v_mfma_f32_16x16x32_bf16 v[152:155], v[72:75], v[44:47], v[152:155]
buffer_load_dwordx4 v6, s[40:43], s93 offen offset:2048 lds
s_add_u32 m0, s94, 49152
s_add_u32 s92, s57, 8192
v_mfma_f32_16x16x32_bf16 v[108:111], v[76:79], v[32:35], v[108:111]
buffer_load_dwordx4 v3, s[8:11], s92 offen sc1 lds
v_mfma_f32_16x16x32_bf16 v[124:127], v[76:79], v[36:39], v[124:127]
buffer_load_dwordx4 v4, s[8:11], s92 offen offset:1024 sc1 lds
v_mfma_f32_16x16x32_bf16 v[140:143], v[76:79], v[40:43], v[140:143]
v_mfma_f32_16x16x32_bf16 v[156:159], v[76:79], v[44:47], v[156:159]
s_waitcnt vmcnt(20) lgkmcnt(0)
s_barrier
v_mfma_f32_16x16x32_bf16 v[96:99], v[80:83], v[48:51], v[96:99]
ds_read_b64_tr_b16 v[64:65], v18 offset:32768
ds_read_b64_tr_b16 v[66:67], v18 offset:34816
v_mfma_f32_16x16x32_bf16 v[112:115], v[80:83], v[52:55], v[112:115]
ds_read_b128 v[32:35], v20 offset:32768
v_mfma_f32_16x16x32_bf16 v[128:131], v[80:83], v[56:59], v[128:131]
ds_read_b64_tr_b16 v[68:69], v19 offset:32768
ds_read_b64_tr_b16 v[70:71], v19 offset:34816
v_mfma_f32_16x16x32_bf16 v[144:147], v[80:83], v[60:63], v[144:147]
ds_read_b128 v[36:39], v21 offset:32768
v_mfma_f32_16x16x32_bf16 v[100:103], v[84:87], v[48:51], v[100:103]
ds_read_b64_tr_b16 v[72:73], v18 offset:33280
ds_read_b64_tr_b16 v[74:75], v18 offset:35328
v_mfma_f32_16x16x32_bf16 v[116:119], v[84:87], v[52:55], v[116:119]
ds_read_b128 v[40:43], v22 offset:32768
v_mfma_f32_16x16x32_bf16 v[132:135], v[84:87], v[56:59], v[132:135]
ds_read_b64_tr_b16 v[76:77], v19 offset:33280
ds_read_b64_tr_b16 v[78:79], v19 offset:35328
v_mfma_f32_16x16x32_bf16 v[148:151], v[84:87], v[60:63], v[148:151]
ds_read_b128 v[44:47], v23 offset:32768
v_mfma_f32_16x16x32_bf16 v[104:107], v[88:91], v[48:51], v[104:107]
v_mfma_f32_16x16x32_bf16 v[120:123], v[88:91], v[52:55], v[120:123]
s_add_u32 m0, s50, 73728
s_add_u32 s93, s58, 294912
v_mfma_f32_16x16x32_bf16 v[136:139], v[88:91], v[56:59], v[136:139]
buffer_load_dwordx4 v5, s[40:43], s93 offen lds
v_mfma_f32_16x16x32_bf16 v[152:155], v[88:91], v[60:63], v[152:155]
buffer_load_dwordx4 v6, s[40:43], s93 offen offset:2048 lds
s_add_u32 m0, s94, 65536
s_add_u32 s92, s57, 9216
v_mfma_f32_16x16x32_bf16 v[108:111], v[92:95], v[48:51], v[108:111]
buffer_load_dwordx4 v3, s[8:11], s92 offen sc1 lds
v_mfma_f32_16x16x32_bf16 v[124:127], v[92:95], v[52:55], v[124:127]
buffer_load_dwordx4 v4, s[8:11], s92 offen offset:1024 sc1 lds
v_mfma_f32_16x16x32_bf16 v[140:143], v[92:95], v[56:59], v[140:143]
v_mfma_f32_16x16x32_bf16 v[156:159], v[92:95], v[60:63], v[156:159]
s_waitcnt vmcnt(20) lgkmcnt(0)
s_barrier
v_mfma_f32_16x16x32_bf16 v[96:99], v[64:67], v[32:35], v[96:99]
ds_read_b64_tr_b16 v[80:81], v18 offset:49152
ds_read_b64_tr_b16 v[82:83], v18 offset:51200
v_mfma_f32_16x16x32_bf16 v[112:115], v[64:67], v[36:39], v[112:115]
ds_read_b128 v[48:51], v20 offset:49152
v_mfma_f32_16x16x32_bf16 v[128:131], v[64:67], v[40:43], v[128:131]
ds_read_b64_tr_b16 v[84:85], v19 offset:49152
ds_read_b64_tr_b16 v[86:87], v19 offset:51200
v_mfma_f32_16x16x32_bf16 v[144:147], v[64:67], v[44:47], v[144:147]
)ASM"
R"ASM(
ds_read_b128 v[52:55], v21 offset:49152
v_mfma_f32_16x16x32_bf16 v[100:103], v[68:71], v[32:35], v[100:103]
ds_read_b64_tr_b16 v[88:89], v18 offset:49664
ds_read_b64_tr_b16 v[90:91], v18 offset:51712
v_mfma_f32_16x16x32_bf16 v[116:119], v[68:71], v[36:39], v[116:119]
ds_read_b128 v[56:59], v22 offset:49152
v_mfma_f32_16x16x32_bf16 v[132:135], v[68:71], v[40:43], v[132:135]
ds_read_b64_tr_b16 v[92:93], v19 offset:49664
ds_read_b64_tr_b16 v[94:95], v19 offset:51712
v_mfma_f32_16x16x32_bf16 v[148:151], v[68:71], v[44:47], v[148:151]
ds_read_b128 v[60:63], v23 offset:49152
v_mfma_f32_16x16x32_bf16 v[104:107], v[72:75], v[32:35], v[104:107]
v_mfma_f32_16x16x32_bf16 v[120:123], v[72:75], v[36:39], v[120:123]
s_add_u32 m0, s50, 90112
s_add_u32 s93, s58, 327680
v_mfma_f32_16x16x32_bf16 v[136:139], v[72:75], v[40:43], v[136:139]
buffer_load_dwordx4 v5, s[40:43], s93 offen lds
v_mfma_f32_16x16x32_bf16 v[152:155], v[72:75], v[44:47], v[152:155]
buffer_load_dwordx4 v6, s[40:43], s93 offen offset:2048 lds
s_add_u32 m0, s94, 81920
s_add_u32 s92, s57, 10240
v_mfma_f32_16x16x32_bf16 v[108:111], v[76:79], v[32:35], v[108:111]
buffer_load_dwordx4 v3, s[8:11], s92 offen sc1 lds
v_mfma_f32_16x16x32_bf16 v[124:127], v[76:79], v[36:39], v[124:127]
buffer_load_dwordx4 v4, s[8:11], s92 offen offset:1024 sc1 lds
v_mfma_f32_16x16x32_bf16 v[140:143], v[76:79], v[40:43], v[140:143]
v_mfma_f32_16x16x32_bf16 v[156:159], v[76:79], v[44:47], v[156:159]
s_waitcnt vmcnt(20) lgkmcnt(0)
s_barrier
v_mfma_f32_16x16x32_bf16 v[96:99], v[80:83], v[48:51], v[96:99]
ds_read_b64_tr_b16 v[64:65], v7 offset:0
ds_read_b64_tr_b16 v[66:67], v7 offset:2048
v_mfma_f32_16x16x32_bf16 v[112:115], v[80:83], v[52:55], v[112:115]
ds_read_b128 v[32:35], v9 offset:0
v_mfma_f32_16x16x32_bf16 v[128:131], v[80:83], v[56:59], v[128:131]
ds_read_b64_tr_b16 v[68:69], v8 offset:0
ds_read_b64_tr_b16 v[70:71], v8 offset:2048
v_mfma_f32_16x16x32_bf16 v[144:147], v[80:83], v[60:63], v[144:147]
ds_read_b128 v[36:39], v10 offset:0
v_mfma_f32_16x16x32_bf16 v[100:103], v[84:87], v[48:51], v[100:103]
ds_read_b64_tr_b16 v[72:73], v7 offset:512
ds_read_b64_tr_b16 v[74:75], v7 offset:2560
v_mfma_f32_16x16x32_bf16 v[116:119], v[84:87], v[52:55], v[116:119]
ds_read_b128 v[40:43], v11 offset:0
v_mfma_f32_16x16x32_bf16 v[132:135], v[84:87], v[56:59], v[132:135]
ds_read_b64_tr_b16 v[76:77], v8 offset:512
ds_read_b64_tr_b16 v[78:79], v8 offset:2560
v_mfma_f32_16x16x32_bf16 v[148:151], v[84:87], v[60:63], v[148:151]
ds_read_b128 v[44:47], v12 offset:0
v_mfma_f32_16x16x32_bf16 v[104:107], v[88:91], v[48:51], v[104:107]
v_mfma_f32_16x16x32_bf16 v[120:123], v[88:91], v[52:55], v[120:123]
s_add_u32 m0, s50, 106496
s_add_u32 s93, s58, 360448
v_mfma_f32_16x16x32_bf16 v[136:139], v[88:91], v[56:59], v[136:139]
buffer_load_dwordx4 v5, s[40:43], s93 offen lds
v_mfma_f32_16x16x32_bf16 v[152:155], v[88:91], v[60:63], v[152:155]
buffer_load_dwordx4 v6, s[40:43], s93 offen offset:2048 lds
s_add_u32 m0, s94, 98304
s_add_u32 s92, s57, 11264
v_mfma_f32_16x16x32_bf16 v[108:111], v[92:95], v[48:51], v[108:111]
buffer_load_dwordx4 v3, s[8:11], s92 offen sc1 lds
v_mfma_f32_16x16x32_bf16 v[124:127], v[92:95], v[52:55], v[124:127]
buffer_load_dwordx4 v4, s[8:11], s92 offen offset:1024 sc1 lds
v_mfma_f32_16x16x32_bf16 v[140:143], v[92:95], v[56:59], v[140:143]
v_mfma_f32_16x16x32_bf16 v[156:159], v[92:95], v[60:63], v[156:159]
s_waitcnt vmcnt(20) lgkmcnt(0)
s_barrier
v_mfma_f32_16x16x32_bf16 v[96:99], v[64:67], v[32:35], v[96:99]
ds_read_b64_tr_b16 v[80:81], v7 offset:16384
ds_read_b64_tr_b16 v[82:83], v7 offset:18432
v_mfma_f32_16x16x32_bf16 v[112:115], v[64:67], v[36:39], v[112:115]
ds_read_b128 v[48:51], v9 offset:16384
v_mfma_f32_16x16x32_bf16 v[128:131], v[64:67], v[40:43], v[128:131]
ds_read_b64_tr_b16 v[84:85], v8 offset:16384
ds_read_b64_tr_b16 v[86:87], v8 offset:18432
v_mfma_f32_16x16x32_bf16 v[144:147], v[64:67], v[44:47], v[144:147]
ds_read_b128 v[52:55], v10 offset:16384
v_mfma_f32_16x16x32_bf16 v[100:103], v[68:71], v[32:35], v[100:103]
ds_read_b64_tr_b16 v[88:89], v7 offset:16896
ds_read_b64_tr_b16 v[90:91], v7 offset:18944
v_mfma_f32_16x16x32_bf16 v[116:119], v[68:71], v[36:39], v[116:119]
ds_read_b128 v[56:59], v11 offset:16384
v_mfma_f32_16x16x32_bf16 v[132:135], v[68:71], v[40:43], v[132:135]
ds_read_b64_tr_b16 v[92:93], v8 offset:16896
ds_read_b64_tr_b16 v[94:95], v8 offset:18944
v_mfma_f32_16x16x32_bf16 v[148:151], v[68:71], v[44:47], v[148:151]
ds_read_b128 v[60:63], v12 offset:16384
v_mfma_f32_16x16x32_bf16 v[104:107], v[72:75], v[32:35], v[104:107]
v_mfma_f32_16x16x32_bf16 v[120:123], v[72:75], v[36:39], v[120:123]
s_add_u32 m0, s50, 122880
s_add_u32 s93, s58, 393216
v_mfma_f32_16x16x32_bf16 v[136:139], v[72:75], v[40:43], v[136:139]
buffer_load_dwordx4 v5, s[40:43], s93 offen lds
v_mfma_f32_16x16x32_bf16 v[152:155], v[72:75], v[44:47], v[152:155]
buffer_load_dwordx4 v6, s[40:43], s93 offen offset:2048 lds
s_add_u32 m0, s94, 114688
s_add_u32 s92, s57, 12288
v_mfma_f32_16x16x32_bf16 v[108:111], v[76:79], v[32:35], v[108:111]
buffer_load_dwordx4 v3, s[8:11], s92 offen sc1 lds
v_mfma_f32_16x16x32_bf16 v[124:127], v[76:79], v[36:39], v[124:127]
buffer_load_dwordx4 v4, s[8:11], s92 offen offset:1024 sc1 lds
v_mfma_f32_16x16x32_bf16 v[140:143], v[76:79], v[40:43], v[140:143]
v_mfma_f32_16x16x32_bf16 v[156:159], v[76:79], v[44:47], v[156:159]
s_waitcnt vmcnt(20) lgkmcnt(0)
s_barrier
v_mfma_f32_16x16x32_bf16 v[96:99], v[80:83], v[48:51], v[96:99]
ds_read_b64_tr_b16 v[64:65], v7 offset:32768
ds_read_b64_tr_b16 v[66:67], v7 offset:34816
v_mfma_f32_16x16x32_bf16 v[112:115], v[80:83], v[52:55], v[112:115]
ds_read_b128 v[32:35], v9 offset:32768
v_mfma_f32_16x16x32_bf16 v[128:131], v[80:83], v[56:59], v[128:131]
ds_read_b64_tr_b16 v[68:69], v8 offset:32768
ds_read_b64_tr_b16 v[70:71], v8 offset:34816
v_mfma_f32_16x16x32_bf16 v[144:147], v[80:83], v[60:63], v[144:147]
ds_read_b128 v[36:39], v10 offset:32768
v_mfma_f32_16x16x32_bf16 v[100:103], v[84:87], v[48:51], v[100:103]
ds_read_b64_tr_b16 v[72:73], v7 offset:33280
ds_read_b64_tr_b16 v[74:75], v7 offset:35328
v_mfma_f32_16x16x32_bf16 v[116:119], v[84:87], v[52:55], v[116:119]
ds_read_b128 v[40:43], v11 offset:32768
v_mfma_f32_16x16x32_bf16 v[132:135], v[84:87], v[56:59], v[132:135]
ds_read_b64_tr_b16 v[76:77], v8 offset:33280
ds_read_b64_tr_b16 v[78:79], v8 offset:35328
v_mfma_f32_16x16x32_bf16 v[148:151], v[84:87], v[60:63], v[148:151]
ds_read_b128 v[44:47], v12 offset:32768
v_mfma_f32_16x16x32_bf16 v[104:107], v[88:91], v[48:51], v[104:107]
v_mfma_f32_16x16x32_bf16 v[120:123], v[88:91], v[52:55], v[120:123]
s_add_u32 m0, s50, 8192
s_add_u32 s93, s58, 425984
v_mfma_f32_16x16x32_bf16 v[136:139], v[88:91], v[56:59], v[136:139]
buffer_load_dwordx4 v5, s[40:43], s93 offen lds
v_mfma_f32_16x16x32_bf16 v[152:155], v[88:91], v[60:63], v[152:155]
buffer_load_dwordx4 v6, s[40:43], s93 offen offset:2048 lds
s_add_u32 m0, s94, 0
s_add_u32 s92, s57, 13312
v_mfma_f32_16x16x32_bf16 v[108:111], v[92:95], v[48:51], v[108:111]
buffer_load_dwordx4 v3, s[8:11], s92 offen sc1 lds
v_mfma_f32_16x16x32_bf16 v[124:127], v[92:95], v[52:55], v[124:127]
buffer_load_dwordx4 v4, s[8:11], s92 offen offset:1024 sc1 lds
v_mfma_f32_16x16x32_bf16 v[140:143], v[92:95], v[56:59], v[140:143]
v_mfma_f32_16x16x32_bf16 v[156:159], v[92:95], v[60:63], v[156:159]
s_waitcnt vmcnt(20) lgkmcnt(0)
s_barrier
v_mfma_f32_16x16x32_bf16 v[96:99], v[64:67], v[32:35], v[96:99]
ds_read_b64_tr_b16 v[80:81], v7 offset:49152
ds_read_b64_tr_b16 v[82:83], v7 offset:51200
v_mfma_f32_16x16x32_bf16 v[112:115], v[64:67], v[36:39], v[112:115]
ds_read_b128 v[48:51], v9 offset:49152
v_mfma_f32_16x16x32_bf16 v[128:131], v[64:67], v[40:43], v[128:131]
ds_read_b64_tr_b16 v[84:85], v8 offset:49152
ds_read_b64_tr_b16 v[86:87], v8 offset:51200
v_mfma_f32_16x16x32_bf16 v[144:147], v[64:67], v[44:47], v[144:147]
ds_read_b128 v[52:55], v10 offset:49152
v_mfma_f32_16x16x32_bf16 v[100:103], v[68:71], v[32:35], v[100:103]
ds_read_b64_tr_b16 v[88:89], v7 offset:49664
ds_read_b64_tr_b16 v[90:91], v7 offset:51712
v_mfma_f32_16x16x32_bf16 v[116:119], v[68:71], v[36:39], v[116:119]
ds_read_b128 v[56:59], v11 offset:49152
v_mfma_f32_16x16x32_bf16 v[132:135], v[68:71], v[40:43], v[132:135]
ds_read_b64_tr_b16 v[92:93], v8 offset:49664
ds_read_b64_tr_b16 v[94:95], v8 offset:51712
v_mfma_f32_16x16x32_bf16 v[148:151], v[68:71], v[44:47], v[148:151]
ds_read_b128 v[60:63], v12 offset:49152
v_mfma_f32_16x16x32_bf16 v[104:107], v[72:75], v[32:35], v[104:107]
v_mfma_f32_16x16x32_bf16 v[120:123], v[72:75], v[36:39], v[120:123]
s_add_u32 m0, s50, 24576
s_add_u32 s93, s58, 458752
v_mfma_f32_16x16x32_bf16 v[136:139], v[72:75], v[40:43], v[136:139]
buffer_load_dwordx4 v5, s[40:43], s93 offen lds
v_mfma_f32_16x16x32_bf16 v[152:155], v[72:75], v[44:47], v[152:155]
buffer_load_dwordx4 v6, s[40:43], s93 offen offset:2048 lds
s_add_u32 m0, s94, 16384
s_add_u32 s92, s57, 14336
v_mfma_f32_16x16x32_bf16 v[108:111], v[76:79], v[32:35], v[108:111]
buffer_load_dwordx4 v3, s[8:11], s92 offen sc1 lds
v_mfma_f32_16x16x32_bf16 v[124:127], v[76:79], v[36:39], v[124:127]
buffer_load_dwordx4 v4, s[8:11], s92 offen offset:1024 sc1 lds
v_mfma_f32_16x16x32_bf16 v[140:143], v[76:79], v[40:43], v[140:143]
v_mfma_f32_16x16x32_bf16 v[156:159], v[76:79], v[44:47], v[156:159]
s_waitcnt vmcnt(20) lgkmcnt(0)
s_barrier
v_mfma_f32_16x16x32_bf16 v[96:99], v[80:83], v[48:51], v[96:99]
ds_read_b64_tr_b16 v[64:65], v18 offset:0
ds_read_b64_tr_b16 v[66:67], v18 offset:2048
v_mfma_f32_16x16x32_bf16 v[112:115], v[80:83], v[52:55], v[112:115]
ds_read_b128 v[32:35], v20 offset:0
v_mfma_f32_16x16x32_bf16 v[128:131], v[80:83], v[56:59], v[128:131]
ds_read_b64_tr_b16 v[68:69], v19 offset:0
ds_read_b64_tr_b16 v[70:71], v19 offset:2048
v_mfma_f32_16x16x32_bf16 v[144:147], v[80:83], v[60:63], v[144:147]
ds_read_b128 v[36:39], v21 offset:0
v_mfma_f32_16x16x32_bf16 v[100:103], v[84:87], v[48:51], v[100:103]
ds_read_b64_tr_b16 v[72:73], v18 offset:512
ds_read_b64_tr_b16 v[74:75], v18 offset:2560
v_mfma_f32_16x16x32_bf16 v[116:119], v[84:87], v[52:55], v[116:119]
ds_read_b128 v[40:43], v22 offset:0
v_mfma_f32_16x16x32_bf16 v[132:135], v[84:87], v[56:59], v[132:135]
ds_read_b64_tr_b16 v[76:77], v19 offset:512
ds_read_b64_tr_b16 v[78:79], v19 offset:2560
v_mfma_f32_16x16x32_bf16 v[148:151], v[84:87], v[60:63], v[148:151]
)ASM"
R"ASM(
ds_read_b128 v[44:47], v23 offset:0
v_mfma_f32_16x16x32_bf16 v[104:107], v[88:91], v[48:51], v[104:107]
v_mfma_f32_16x16x32_bf16 v[120:123], v[88:91], v[52:55], v[120:123]
s_add_u32 m0, s50, 40960
s_add_u32 s93, s58, 491520
v_mfma_f32_16x16x32_bf16 v[136:139], v[88:91], v[56:59], v[136:139]
buffer_load_dwordx4 v5, s[40:43], s93 offen lds
v_mfma_f32_16x16x32_bf16 v[152:155], v[88:91], v[60:63], v[152:155]
buffer_load_dwordx4 v6, s[40:43], s93 offen offset:2048 lds
s_add_u32 m0, s94, 32768
s_add_u32 s92, s57, 15360
v_mfma_f32_16x16x32_bf16 v[108:111], v[92:95], v[48:51], v[108:111]
buffer_load_dwordx4 v3, s[8:11], s92 offen sc1 lds
v_mfma_f32_16x16x32_bf16 v[124:127], v[92:95], v[52:55], v[124:127]
buffer_load_dwordx4 v4, s[8:11], s92 offen offset:1024 sc1 lds
v_mfma_f32_16x16x32_bf16 v[140:143], v[92:95], v[56:59], v[140:143]
v_mfma_f32_16x16x32_bf16 v[156:159], v[92:95], v[60:63], v[156:159]
s_waitcnt vmcnt(20) lgkmcnt(0)
s_barrier
v_mfma_f32_16x16x32_bf16 v[96:99], v[64:67], v[32:35], v[96:99]
ds_read_b64_tr_b16 v[80:81], v18 offset:16384
ds_read_b64_tr_b16 v[82:83], v18 offset:18432
v_mfma_f32_16x16x32_bf16 v[112:115], v[64:67], v[36:39], v[112:115]
ds_read_b128 v[48:51], v20 offset:16384
v_mfma_f32_16x16x32_bf16 v[128:131], v[64:67], v[40:43], v[128:131]
ds_read_b64_tr_b16 v[84:85], v19 offset:16384
ds_read_b64_tr_b16 v[86:87], v19 offset:18432
v_mfma_f32_16x16x32_bf16 v[144:147], v[64:67], v[44:47], v[144:147]
ds_read_b128 v[52:55], v21 offset:16384
v_mfma_f32_16x16x32_bf16 v[100:103], v[68:71], v[32:35], v[100:103]
ds_read_b64_tr_b16 v[88:89], v18 offset:16896
ds_read_b64_tr_b16 v[90:91], v18 offset:18944
v_mfma_f32_16x16x32_bf16 v[116:119], v[68:71], v[36:39], v[116:119]
ds_read_b128 v[56:59], v22 offset:16384
v_mfma_f32_16x16x32_bf16 v[132:135], v[68:71], v[40:43], v[132:135]
ds_read_b64_tr_b16 v[92:93], v19 offset:16896
ds_read_b64_tr_b16 v[94:95], v19 offset:18944
v_mfma_f32_16x16x32_bf16 v[148:151], v[68:71], v[44:47], v[148:151]
ds_read_b128 v[60:63], v23 offset:16384
v_mfma_f32_16x16x32_bf16 v[104:107], v[72:75], v[32:35], v[104:107]
v_mfma_f32_16x16x32_bf16 v[120:123], v[72:75], v[36:39], v[120:123]
s_add_u32 m0, s50, 57344
s_add_u32 s93, s58, 524288
v_mfma_f32_16x16x32_bf16 v[136:139], v[72:75], v[40:43], v[136:139]
buffer_load_dwordx4 v5, s[40:43], s93 offen lds
v_mfma_f32_16x16x32_bf16 v[152:155], v[72:75], v[44:47], v[152:155]
buffer_load_dwordx4 v6, s[40:43], s93 offen offset:2048 lds
s_add_u32 m0, s94, 49152
s_add_u32 s92, s57, 16384
v_mfma_f32_16x16x32_bf16 v[108:111], v[76:79], v[32:35], v[108:111]
buffer_load_dwordx4 v3, s[8:11], s92 offen sc1 lds
v_mfma_f32_16x16x32_bf16 v[124:127], v[76:79], v[36:39], v[124:127]
buffer_load_dwordx4 v4, s[8:11], s92 offen offset:1024 sc1 lds
v_mfma_f32_16x16x32_bf16 v[140:143], v[76:79], v[40:43], v[140:143]
v_mfma_f32_16x16x32_bf16 v[156:159], v[76:79], v[44:47], v[156:159]
s_waitcnt vmcnt(20) lgkmcnt(0)
s_barrier
v_mfma_f32_16x16x32_bf16 v[96:99], v[80:83], v[48:51], v[96:99]
ds_read_b64_tr_b16 v[64:65], v18 offset:32768
ds_read_b64_tr_b16 v[66:67], v18 offset:34816
v_mfma_f32_16x16x32_bf16 v[112:115], v[80:83], v[52:55], v[112:115]
ds_read_b128 v[32:35], v20 offset:32768
v_mfma_f32_16x16x32_bf16 v[128:131], v[80:83], v[56:59], v[128:131]
ds_read_b64_tr_b16 v[68:69], v19 offset:32768
ds_read_b64_tr_b16 v[70:71], v19 offset:34816
v_mfma_f32_16x16x32_bf16 v[144:147], v[80:83], v[60:63], v[144:147]
ds_read_b128 v[36:39], v21 offset:32768
v_mfma_f32_16x16x32_bf16 v[100:103], v[84:87], v[48:51], v[100:103]
ds_read_b64_tr_b16 v[72:73], v18 offset:33280
ds_read_b64_tr_b16 v[74:75], v18 offset:35328
v_mfma_f32_16x16x32_bf16 v[116:119], v[84:87], v[52:55], v[116:119]
ds_read_b128 v[40:43], v22 offset:32768
v_mfma_f32_16x16x32_bf16 v[132:135], v[84:87], v[56:59], v[132:135]
ds_read_b64_tr_b16 v[76:77], v19 offset:33280
ds_read_b64_tr_b16 v[78:79], v19 offset:35328
v_mfma_f32_16x16x32_bf16 v[148:151], v[84:87], v[60:63], v[148:151]
ds_read_b128 v[44:47], v23 offset:32768
v_mfma_f32_16x16x32_bf16 v[104:107], v[88:91], v[48:51], v[104:107]
v_mfma_f32_16x16x32_bf16 v[120:123], v[88:91], v[52:55], v[120:123]
s_add_u32 m0, s50, 73728
s_add_u32 s93, s58, 557056
v_mfma_f32_16x16x32_bf16 v[136:139], v[88:91], v[56:59], v[136:139]
buffer_load_dwordx4 v5, s[40:43], s93 offen lds
v_mfma_f32_16x16x32_bf16 v[152:155], v[88:91], v[60:63], v[152:155]
buffer_load_dwordx4 v6, s[40:43], s93 offen offset:2048 lds
s_add_u32 m0, s94, 65536
s_add_u32 s92, s57, 17408
v_mfma_f32_16x16x32_bf16 v[108:111], v[92:95], v[48:51], v[108:111]
buffer_load_dwordx4 v3, s[8:11], s92 offen sc1 lds
v_mfma_f32_16x16x32_bf16 v[124:127], v[92:95], v[52:55], v[124:127]
buffer_load_dwordx4 v4, s[8:11], s92 offen offset:1024 sc1 lds
v_mfma_f32_16x16x32_bf16 v[140:143], v[92:95], v[56:59], v[140:143]
v_mfma_f32_16x16x32_bf16 v[156:159], v[92:95], v[60:63], v[156:159]
s_waitcnt vmcnt(20) lgkmcnt(0)
s_barrier
v_mfma_f32_16x16x32_bf16 v[96:99], v[64:67], v[32:35], v[96:99]
ds_read_b64_tr_b16 v[80:81], v18 offset:49152
ds_read_b64_tr_b16 v[82:83], v18 offset:51200
v_mfma_f32_16x16x32_bf16 v[112:115], v[64:67], v[36:39], v[112:115]
ds_read_b128 v[48:51], v20 offset:49152
v_mfma_f32_16x16x32_bf16 v[128:131], v[64:67], v[40:43], v[128:131]
ds_read_b64_tr_b16 v[84:85], v19 offset:49152
ds_read_b64_tr_b16 v[86:87], v19 offset:51200
v_mfma_f32_16x16x32_bf16 v[144:147], v[64:67], v[44:47], v[144:147]
ds_read_b128 v[52:55], v21 offset:49152
v_mfma_f32_16x16x32_bf16 v[100:103], v[68:71], v[32:35], v[100:103]
ds_read_b64_tr_b16 v[88:89], v18 offset:49664
ds_read_b64_tr_b16 v[90:91], v18 offset:51712
v_mfma_f32_16x16x32_bf16 v[116:119], v[68:71], v[36:39], v[116:119]
ds_read_b128 v[56:59], v22 offset:49152
v_mfma_f32_16x16x32_bf16 v[132:135], v[68:71], v[40:43], v[132:135]
ds_read_b64_tr_b16 v[92:93], v19 offset:49664
ds_read_b64_tr_b16 v[94:95], v19 offset:51712
v_mfma_f32_16x16x32_bf16 v[148:151], v[68:71], v[44:47], v[148:151]
ds_read_b128 v[60:63], v23 offset:49152
v_mfma_f32_16x16x32_bf16 v[104:107], v[72:75], v[32:35], v[104:107]
v_mfma_f32_16x16x32_bf16 v[120:123], v[72:75], v[36:39], v[120:123]
s_add_u32 m0, s50, 90112
s_add_u32 s93, s58, 589824
v_mfma_f32_16x16x32_bf16 v[136:139], v[72:75], v[40:43], v[136:139]
buffer_load_dwordx4 v5, s[40:43], s93 offen lds
v_mfma_f32_16x16x32_bf16 v[152:155], v[72:75], v[44:47], v[152:155]
buffer_load_dwordx4 v6, s[40:43], s93 offen offset:2048 lds
s_add_u32 m0, s94, 81920
s_add_u32 s92, s57, 18432
v_mfma_f32_16x16x32_bf16 v[108:111], v[76:79], v[32:35], v[108:111]
buffer_load_dwordx4 v3, s[8:11], s92 offen sc1 lds
v_mfma_f32_16x16x32_bf16 v[124:127], v[76:79], v[36:39], v[124:127]
buffer_load_dwordx4 v4, s[8:11], s92 offen offset:1024 sc1 lds
v_mfma_f32_16x16x32_bf16 v[140:143], v[76:79], v[40:43], v[140:143]
v_mfma_f32_16x16x32_bf16 v[156:159], v[76:79], v[44:47], v[156:159]
s_waitcnt vmcnt(20) lgkmcnt(0)
s_barrier
v_mfma_f32_16x16x32_bf16 v[96:99], v[80:83], v[48:51], v[96:99]
ds_read_b64_tr_b16 v[64:65], v7 offset:0
ds_read_b64_tr_b16 v[66:67], v7 offset:2048
v_mfma_f32_16x16x32_bf16 v[112:115], v[80:83], v[52:55], v[112:115]
ds_read_b128 v[32:35], v9 offset:0
v_mfma_f32_16x16x32_bf16 v[128:131], v[80:83], v[56:59], v[128:131]
ds_read_b64_tr_b16 v[68:69], v8 offset:0
ds_read_b64_tr_b16 v[70:71], v8 offset:2048
v_mfma_f32_16x16x32_bf16 v[144:147], v[80:83], v[60:63], v[144:147]
ds_read_b128 v[36:39], v10 offset:0
v_mfma_f32_16x16x32_bf16 v[100:103], v[84:87], v[48:51], v[100:103]
ds_read_b64_tr_b16 v[72:73], v7 offset:512
ds_read_b64_tr_b16 v[74:75], v7 offset:2560
v_mfma_f32_16x16x32_bf16 v[116:119], v[84:87], v[52:55], v[116:119]
ds_read_b128 v[40:43], v11 offset:0
v_mfma_f32_16x16x32_bf16 v[132:135], v[84:87], v[56:59], v[132:135]
ds_read_b64_tr_b16 v[76:77], v8 offset:512
ds_read_b64_tr_b16 v[78:79], v8 offset:2560
v_mfma_f32_16x16x32_bf16 v[148:151], v[84:87], v[60:63], v[148:151]
ds_read_b128 v[44:47], v12 offset:0
v_mfma_f32_16x16x32_bf16 v[104:107], v[88:91], v[48:51], v[104:107]
v_mfma_f32_16x16x32_bf16 v[120:123], v[88:91], v[52:55], v[120:123]
s_add_u32 m0, s50, 106496
s_add_u32 s93, s58, 622592
v_mfma_f32_16x16x32_bf16 v[136:139], v[88:91], v[56:59], v[136:139]
buffer_load_dwordx4 v5, s[40:43], s93 offen lds
v_mfma_f32_16x16x32_bf16 v[152:155], v[88:91], v[60:63], v[152:155]
buffer_load_dwordx4 v6, s[40:43], s93 offen offset:2048 lds
s_add_u32 m0, s94, 98304
s_add_u32 s92, s57, 19456
v_mfma_f32_16x16x32_bf16 v[108:111], v[92:95], v[48:51], v[108:111]
buffer_load_dwordx4 v3, s[8:11], s92 offen sc1 lds
v_mfma_f32_16x16x32_bf16 v[124:127], v[92:95], v[52:55], v[124:127]
buffer_load_dwordx4 v4, s[8:11], s92 offen offset:1024 sc1 lds
v_mfma_f32_16x16x32_bf16 v[140:143], v[92:95], v[56:59], v[140:143]
v_mfma_f32_16x16x32_bf16 v[156:159], v[92:95], v[60:63], v[156:159]
s_waitcnt vmcnt(20) lgkmcnt(0)
s_barrier
v_mfma_f32_16x16x32_bf16 v[96:99], v[64:67], v[32:35], v[96:99]
ds_read_b64_tr_b16 v[80:81], v7 offset:16384
ds_read_b64_tr_b16 v[82:83], v7 offset:18432
v_mfma_f32_16x16x32_bf16 v[112:115], v[64:67], v[36:39], v[112:115]
ds_read_b128 v[48:51], v9 offset:16384
v_mfma_f32_16x16x32_bf16 v[128:131], v[64:67], v[40:43], v[128:131]
ds_read_b64_tr_b16 v[84:85], v8 offset:16384
ds_read_b64_tr_b16 v[86:87], v8 offset:18432
v_mfma_f32_16x16x32_bf16 v[144:147], v[64:67], v[44:47], v[144:147]
ds_read_b128 v[52:55], v10 offset:16384
v_mfma_f32_16x16x32_bf16 v[100:103], v[68:71], v[32:35], v[100:103]
ds_read_b64_tr_b16 v[88:89], v7 offset:16896
ds_read_b64_tr_b16 v[90:91], v7 offset:18944
v_mfma_f32_16x16x32_bf16 v[116:119], v[68:71], v[36:39], v[116:119]
ds_read_b128 v[56:59], v11 offset:16384
v_mfma_f32_16x16x32_bf16 v[132:135], v[68:71], v[40:43], v[132:135]
ds_read_b64_tr_b16 v[92:93], v8 offset:16896
ds_read_b64_tr_b16 v[94:95], v8 offset:18944
v_mfma_f32_16x16x32_bf16 v[148:151], v[68:71], v[44:47], v[148:151]
ds_read_b128 v[60:63], v12 offset:16384
v_mfma_f32_16x16x32_bf16 v[104:107], v[72:75], v[32:35], v[104:107]
v_mfma_f32_16x16x32_bf16 v[120:123], v[72:75], v[36:39], v[120:123]
s_add_u32 m0, s50, 122880
s_add_u32 s93, s58, 655360
v_mfma_f32_16x16x32_bf16 v[136:139], v[72:75], v[40:43], v[136:139]
buffer_load_dwordx4 v5, s[40:43], s93 offen lds
v_mfma_f32_16x16x32_bf16 v[152:155], v[72:75], v[44:47], v[152:155]
buffer_load_dwordx4 v6, s[40:43], s93 offen offset:2048 lds
s_add_u32 m0, s94, 114688
)ASM"
R"ASM(
s_add_u32 s92, s57, 20480
v_mfma_f32_16x16x32_bf16 v[108:111], v[76:79], v[32:35], v[108:111]
buffer_load_dwordx4 v3, s[8:11], s92 offen sc1 lds
v_mfma_f32_16x16x32_bf16 v[124:127], v[76:79], v[36:39], v[124:127]
buffer_load_dwordx4 v4, s[8:11], s92 offen offset:1024 sc1 lds
v_mfma_f32_16x16x32_bf16 v[140:143], v[76:79], v[40:43], v[140:143]
v_mfma_f32_16x16x32_bf16 v[156:159], v[76:79], v[44:47], v[156:159]
s_waitcnt vmcnt(20) lgkmcnt(0)
s_barrier
v_mfma_f32_16x16x32_bf16 v[96:99], v[80:83], v[48:51], v[96:99]
ds_read_b64_tr_b16 v[64:65], v7 offset:32768
ds_read_b64_tr_b16 v[66:67], v7 offset:34816
v_mfma_f32_16x16x32_bf16 v[112:115], v[80:83], v[52:55], v[112:115]
ds_read_b128 v[32:35], v9 offset:32768
v_mfma_f32_16x16x32_bf16 v[128:131], v[80:83], v[56:59], v[128:131]
ds_read_b64_tr_b16 v[68:69], v8 offset:32768
ds_read_b64_tr_b16 v[70:71], v8 offset:34816
v_mfma_f32_16x16x32_bf16 v[144:147], v[80:83], v[60:63], v[144:147]
ds_read_b128 v[36:39], v10 offset:32768
v_mfma_f32_16x16x32_bf16 v[100:103], v[84:87], v[48:51], v[100:103]
ds_read_b64_tr_b16 v[72:73], v7 offset:33280
ds_read_b64_tr_b16 v[74:75], v7 offset:35328
v_mfma_f32_16x16x32_bf16 v[116:119], v[84:87], v[52:55], v[116:119]
ds_read_b128 v[40:43], v11 offset:32768
v_mfma_f32_16x16x32_bf16 v[132:135], v[84:87], v[56:59], v[132:135]
ds_read_b64_tr_b16 v[76:77], v8 offset:33280
ds_read_b64_tr_b16 v[78:79], v8 offset:35328
v_mfma_f32_16x16x32_bf16 v[148:151], v[84:87], v[60:63], v[148:151]
ds_read_b128 v[44:47], v12 offset:32768
v_mfma_f32_16x16x32_bf16 v[104:107], v[88:91], v[48:51], v[104:107]
v_mfma_f32_16x16x32_bf16 v[120:123], v[88:91], v[52:55], v[120:123]
s_add_u32 m0, s50, 8192
s_add_u32 s93, s58, 688128
v_mfma_f32_16x16x32_bf16 v[136:139], v[88:91], v[56:59], v[136:139]
buffer_load_dwordx4 v5, s[40:43], s93 offen lds
v_mfma_f32_16x16x32_bf16 v[152:155], v[88:91], v[60:63], v[152:155]
buffer_load_dwordx4 v6, s[40:43], s93 offen offset:2048 lds
s_add_u32 m0, s94, 0
s_add_u32 s92, s57, 21504
v_mfma_f32_16x16x32_bf16 v[108:111], v[92:95], v[48:51], v[108:111]
buffer_load_dwordx4 v3, s[8:11], s92 offen sc1 lds
v_mfma_f32_16x16x32_bf16 v[124:127], v[92:95], v[52:55], v[124:127]
buffer_load_dwordx4 v4, s[8:11], s92 offen offset:1024 sc1 lds
v_mfma_f32_16x16x32_bf16 v[140:143], v[92:95], v[56:59], v[140:143]
v_mfma_f32_16x16x32_bf16 v[156:159], v[92:95], v[60:63], v[156:159]
s_waitcnt vmcnt(20) lgkmcnt(0)
s_barrier
v_mfma_f32_16x16x32_bf16 v[96:99], v[64:67], v[32:35], v[96:99]
ds_read_b64_tr_b16 v[80:81], v7 offset:49152
ds_read_b64_tr_b16 v[82:83], v7 offset:51200
v_mfma_f32_16x16x32_bf16 v[112:115], v[64:67], v[36:39], v[112:115]
ds_read_b128 v[48:51], v9 offset:49152
v_mfma_f32_16x16x32_bf16 v[128:131], v[64:67], v[40:43], v[128:131]
ds_read_b64_tr_b16 v[84:85], v8 offset:49152
ds_read_b64_tr_b16 v[86:87], v8 offset:51200
v_mfma_f32_16x16x32_bf16 v[144:147], v[64:67], v[44:47], v[144:147]
ds_read_b128 v[52:55], v10 offset:49152
v_mfma_f32_16x16x32_bf16 v[100:103], v[68:71], v[32:35], v[100:103]
ds_read_b64_tr_b16 v[88:89], v7 offset:49664
ds_read_b64_tr_b16 v[90:91], v7 offset:51712
v_mfma_f32_16x16x32_bf16 v[116:119], v[68:71], v[36:39], v[116:119]
ds_read_b128 v[56:59], v11 offset:49152
v_mfma_f32_16x16x32_bf16 v[132:135], v[68:71], v[40:43], v[132:135]
ds_read_b64_tr_b16 v[92:93], v8 offset:49664
ds_read_b64_tr_b16 v[94:95], v8 offset:51712
v_mfma_f32_16x16x32_bf16 v[148:151], v[68:71], v[44:47], v[148:151]
ds_read_b128 v[60:63], v12 offset:49152
v_mfma_f32_16x16x32_bf16 v[104:107], v[72:75], v[32:35], v[104:107]
v_mfma_f32_16x16x32_bf16 v[120:123], v[72:75], v[36:39], v[120:123]
s_add_u32 m0, s50, 24576
s_add_u32 s93, s58, 720896
v_mfma_f32_16x16x32_bf16 v[136:139], v[72:75], v[40:43], v[136:139]
buffer_load_dwordx4 v5, s[40:43], s93 offen lds
v_mfma_f32_16x16x32_bf16 v[152:155], v[72:75], v[44:47], v[152:155]
buffer_load_dwordx4 v6, s[40:43], s93 offen offset:2048 lds
s_add_u32 m0, s94, 16384
s_add_u32 s92, s57, 22528
v_mfma_f32_16x16x32_bf16 v[108:111], v[76:79], v[32:35], v[108:111]
buffer_load_dwordx4 v3, s[8:11], s92 offen sc1 lds
v_mfma_f32_16x16x32_bf16 v[124:127], v[76:79], v[36:39], v[124:127]
buffer_load_dwordx4 v4, s[8:11], s92 offen offset:1024 sc1 lds
v_mfma_f32_16x16x32_bf16 v[140:143], v[76:79], v[40:43], v[140:143]
v_mfma_f32_16x16x32_bf16 v[156:159], v[76:79], v[44:47], v[156:159]
s_waitcnt vmcnt(20) lgkmcnt(0)
s_barrier
v_mfma_f32_16x16x32_bf16 v[96:99], v[80:83], v[48:51], v[96:99]
ds_read_b64_tr_b16 v[64:65], v18 offset:0
ds_read_b64_tr_b16 v[66:67], v18 offset:2048
v_mfma_f32_16x16x32_bf16 v[112:115], v[80:83], v[52:55], v[112:115]
ds_read_b128 v[32:35], v20 offset:0
v_mfma_f32_16x16x32_bf16 v[128:131], v[80:83], v[56:59], v[128:131]
ds_read_b64_tr_b16 v[68:69], v19 offset:0
ds_read_b64_tr_b16 v[70:71], v19 offset:2048
v_mfma_f32_16x16x32_bf16 v[144:147], v[80:83], v[60:63], v[144:147]
ds_read_b128 v[36:39], v21 offset:0
v_mfma_f32_16x16x32_bf16 v[100:103], v[84:87], v[48:51], v[100:103]
ds_read_b64_tr_b16 v[72:73], v18 offset:512
ds_read_b64_tr_b16 v[74:75], v18 offset:2560
v_mfma_f32_16x16x32_bf16 v[116:119], v[84:87], v[52:55], v[116:119]
ds_read_b128 v[40:43], v22 offset:0
v_mfma_f32_16x16x32_bf16 v[132:135], v[84:87], v[56:59], v[132:135]
ds_read_b64_tr_b16 v[76:77], v19 offset:512
ds_read_b64_tr_b16 v[78:79], v19 offset:2560
v_mfma_f32_16x16x32_bf16 v[148:151], v[84:87], v[60:63], v[148:151]
ds_read_b128 v[44:47], v23 offset:0
v_mfma_f32_16x16x32_bf16 v[104:107], v[88:91], v[48:51], v[104:107]
v_mfma_f32_16x16x32_bf16 v[120:123], v[88:91], v[52:55], v[120:123]
s_add_u32 m0, s50, 40960
s_add_u32 s93, s58, 753664
v_mfma_f32_16x16x32_bf16 v[136:139], v[88:91], v[56:59], v[136:139]
buffer_load_dwordx4 v5, s[40:43], s93 offen lds
v_mfma_f32_16x16x32_bf16 v[152:155], v[88:91], v[60:63], v[152:155]
buffer_load_dwordx4 v6, s[40:43], s93 offen offset:2048 lds
s_add_u32 m0, s94, 32768
s_add_u32 s92, s57, 23552
v_mfma_f32_16x16x32_bf16 v[108:111], v[92:95], v[48:51], v[108:111]
buffer_load_dwordx4 v3, s[8:11], s92 offen sc1 lds
v_mfma_f32_16x16x32_bf16 v[124:127], v[92:95], v[52:55], v[124:127]
buffer_load_dwordx4 v4, s[8:11], s92 offen offset:1024 sc1 lds
v_mfma_f32_16x16x32_bf16 v[140:143], v[92:95], v[56:59], v[140:143]
v_mfma_f32_16x16x32_bf16 v[156:159], v[92:95], v[60:63], v[156:159]
s_waitcnt vmcnt(20) lgkmcnt(0)
s_barrier
v_mfma_f32_16x16x32_bf16 v[96:99], v[64:67], v[32:35], v[96:99]
ds_read_b64_tr_b16 v[80:81], v18 offset:16384
ds_read_b64_tr_b16 v[82:83], v18 offset:18432
v_mfma_f32_16x16x32_bf16 v[112:115], v[64:67], v[36:39], v[112:115]
ds_read_b128 v[48:51], v20 offset:16384
v_mfma_f32_16x16x32_bf16 v[128:131], v[64:67], v[40:43], v[128:131]
ds_read_b64_tr_b16 v[84:85], v19 offset:16384
ds_read_b64_tr_b16 v[86:87], v19 offset:18432
v_mfma_f32_16x16x32_bf16 v[144:147], v[64:67], v[44:47], v[144:147]
ds_read_b128 v[52:55], v21 offset:16384
v_mfma_f32_16x16x32_bf16 v[100:103], v[68:71], v[32:35], v[100:103]
ds_read_b64_tr_b16 v[88:89], v18 offset:16896
ds_read_b64_tr_b16 v[90:91], v18 offset:18944
v_mfma_f32_16x16x32_bf16 v[116:119], v[68:71], v[36:39], v[116:119]
ds_read_b128 v[56:59], v22 offset:16384
v_mfma_f32_16x16x32_bf16 v[132:135], v[68:71], v[40:43], v[132:135]
ds_read_b64_tr_b16 v[92:93], v19 offset:16896
ds_read_b64_tr_b16 v[94:95], v19 offset:18944
v_mfma_f32_16x16x32_bf16 v[148:151], v[68:71], v[44:47], v[148:151]
ds_read_b128 v[60:63], v23 offset:16384
v_mfma_f32_16x16x32_bf16 v[104:107], v[72:75], v[32:35], v[104:107]
v_mfma_f32_16x16x32_bf16 v[120:123], v[72:75], v[36:39], v[120:123]
s_add_u32 m0, s50, 57344
s_add_u32 s93, s58, 786432
v_mfma_f32_16x16x32_bf16 v[136:139], v[72:75], v[40:43], v[136:139]
buffer_load_dwordx4 v5, s[40:43], s93 offen lds
v_mfma_f32_16x16x32_bf16 v[152:155], v[72:75], v[44:47], v[152:155]
buffer_load_dwordx4 v6, s[40:43], s93 offen offset:2048 lds
s_add_u32 m0, s94, 49152
s_add_u32 s92, s57, 24576
v_mfma_f32_16x16x32_bf16 v[108:111], v[76:79], v[32:35], v[108:111]
buffer_load_dwordx4 v3, s[8:11], s92 offen sc1 lds
v_mfma_f32_16x16x32_bf16 v[124:127], v[76:79], v[36:39], v[124:127]
buffer_load_dwordx4 v4, s[8:11], s92 offen offset:1024 sc1 lds
v_mfma_f32_16x16x32_bf16 v[140:143], v[76:79], v[40:43], v[140:143]
v_mfma_f32_16x16x32_bf16 v[156:159], v[76:79], v[44:47], v[156:159]
s_waitcnt vmcnt(20) lgkmcnt(0)
s_barrier
v_mfma_f32_16x16x32_bf16 v[96:99], v[80:83], v[48:51], v[96:99]
ds_read_b64_tr_b16 v[64:65], v18 offset:32768
ds_read_b64_tr_b16 v[66:67], v18 offset:34816
v_mfma_f32_16x16x32_bf16 v[112:115], v[80:83], v[52:55], v[112:115]
ds_read_b128 v[32:35], v20 offset:32768
v_mfma_f32_16x16x32_bf16 v[128:131], v[80:83], v[56:59], v[128:131]
ds_read_b64_tr_b16 v[68:69], v19 offset:32768
ds_read_b64_tr_b16 v[70:71], v19 offset:34816
v_mfma_f32_16x16x32_bf16 v[144:147], v[80:83], v[60:63], v[144:147]
ds_read_b128 v[36:39], v21 offset:32768
v_mfma_f32_16x16x32_bf16 v[100:103], v[84:87], v[48:51], v[100:103]
ds_read_b64_tr_b16 v[72:73], v18 offset:33280
ds_read_b64_tr_b16 v[74:75], v18 offset:35328
v_mfma_f32_16x16x32_bf16 v[116:119], v[84:87], v[52:55], v[116:119]
ds_read_b128 v[40:43], v22 offset:32768
v_mfma_f32_16x16x32_bf16 v[132:135], v[84:87], v[56:59], v[132:135]
ds_read_b64_tr_b16 v[76:77], v19 offset:33280
ds_read_b64_tr_b16 v[78:79], v19 offset:35328
v_mfma_f32_16x16x32_bf16 v[148:151], v[84:87], v[60:63], v[148:151]
ds_read_b128 v[44:47], v23 offset:32768
v_mfma_f32_16x16x32_bf16 v[104:107], v[88:91], v[48:51], v[104:107]
v_mfma_f32_16x16x32_bf16 v[120:123], v[88:91], v[52:55], v[120:123]
s_add_u32 m0, s50, 73728
s_add_u32 s93, s58, 819200
v_mfma_f32_16x16x32_bf16 v[136:139], v[88:91], v[56:59], v[136:139]
buffer_load_dwordx4 v5, s[40:43], s93 offen lds
v_mfma_f32_16x16x32_bf16 v[152:155], v[88:91], v[60:63], v[152:155]
buffer_load_dwordx4 v6, s[40:43], s93 offen offset:2048 lds
s_add_u32 m0, s94, 65536
s_add_u32 s92, s57, 25600
v_mfma_f32_16x16x32_bf16 v[108:111], v[92:95], v[48:51], v[108:111]
buffer_load_dwordx4 v3, s[8:11], s92 offen sc1 lds
v_mfma_f32_16x16x32_bf16 v[124:127], v[92:95], v[52:55], v[124:127]
buffer_load_dwordx4 v4, s[8:11], s92 offen offset:1024 sc1 lds
v_mfma_f32_16x16x32_bf16 v[140:143], v[92:95], v[56:59], v[140:143]
v_mfma_f32_16x16x32_bf16 v[156:159], v[92:95], v[60:63], v[156:159]
s_waitcnt vmcnt(20) lgkmcnt(0)
s_barrier
v_mfma_f32_16x16x32_bf16 v[96:99], v[64:67], v[32:35], v[96:99]
)ASM"
R"ASM(
ds_read_b64_tr_b16 v[80:81], v18 offset:49152
ds_read_b64_tr_b16 v[82:83], v18 offset:51200
v_mfma_f32_16x16x32_bf16 v[112:115], v[64:67], v[36:39], v[112:115]
ds_read_b128 v[48:51], v20 offset:49152
v_mfma_f32_16x16x32_bf16 v[128:131], v[64:67], v[40:43], v[128:131]
ds_read_b64_tr_b16 v[84:85], v19 offset:49152
ds_read_b64_tr_b16 v[86:87], v19 offset:51200
v_mfma_f32_16x16x32_bf16 v[144:147], v[64:67], v[44:47], v[144:147]
ds_read_b128 v[52:55], v21 offset:49152
v_mfma_f32_16x16x32_bf16 v[100:103], v[68:71], v[32:35], v[100:103]
ds_read_b64_tr_b16 v[88:89], v18 offset:49664
ds_read_b64_tr_b16 v[90:91], v18 offset:51712
v_mfma_f32_16x16x32_bf16 v[116:119], v[68:71], v[36:39], v[116:119]
ds_read_b128 v[56:59], v22 offset:49152
v_mfma_f32_16x16x32_bf16 v[132:135], v[68:71], v[40:43], v[132:135]
ds_read_b64_tr_b16 v[92:93], v19 offset:49664
ds_read_b64_tr_b16 v[94:95], v19 offset:51712
v_mfma_f32_16x16x32_bf16 v[148:151], v[68:71], v[44:47], v[148:151]
ds_read_b128 v[60:63], v23 offset:49152
v_mfma_f32_16x16x32_bf16 v[104:107], v[72:75], v[32:35], v[104:107]
v_mfma_f32_16x16x32_bf16 v[120:123], v[72:75], v[36:39], v[120:123]
s_add_u32 m0, s50, 90112
s_add_u32 s93, s58, 851968
v_mfma_f32_16x16x32_bf16 v[136:139], v[72:75], v[40:43], v[136:139]
buffer_load_dwordx4 v5, s[40:43], s93 offen lds
v_mfma_f32_16x16x32_bf16 v[152:155], v[72:75], v[44:47], v[152:155]
buffer_load_dwordx4 v6, s[40:43], s93 offen offset:2048 lds
s_add_u32 m0, s94, 81920
s_add_u32 s92, s57, 26624
v_mfma_f32_16x16x32_bf16 v[108:111], v[76:79], v[32:35], v[108:111]
buffer_load_dwordx4 v3, s[8:11], s92 offen sc1 lds
v_mfma_f32_16x16x32_bf16 v[124:127], v[76:79], v[36:39], v[124:127]
buffer_load_dwordx4 v4, s[8:11], s92 offen offset:1024 sc1 lds
v_mfma_f32_16x16x32_bf16 v[140:143], v[76:79], v[40:43], v[140:143]
v_mfma_f32_16x16x32_bf16 v[156:159], v[76:79], v[44:47], v[156:159]
s_waitcnt vmcnt(20) lgkmcnt(0)
s_barrier
v_mfma_f32_16x16x32_bf16 v[96:99], v[80:83], v[48:51], v[96:99]
ds_read_b64_tr_b16 v[64:65], v7 offset:0
ds_read_b64_tr_b16 v[66:67], v7 offset:2048
v_mfma_f32_16x16x32_bf16 v[112:115], v[80:83], v[52:55], v[112:115]
ds_read_b128 v[32:35], v9 offset:0
v_mfma_f32_16x16x32_bf16 v[128:131], v[80:83], v[56:59], v[128:131]
ds_read_b64_tr_b16 v[68:69], v8 offset:0
ds_read_b64_tr_b16 v[70:71], v8 offset:2048
v_mfma_f32_16x16x32_bf16 v[144:147], v[80:83], v[60:63], v[144:147]
ds_read_b128 v[36:39], v10 offset:0
v_mfma_f32_16x16x32_bf16 v[100:103], v[84:87], v[48:51], v[100:103]
ds_read_b64_tr_b16 v[72:73], v7 offset:512
ds_read_b64_tr_b16 v[74:75], v7 offset:2560
v_mfma_f32_16x16x32_bf16 v[116:119], v[84:87], v[52:55], v[116:119]
ds_read_b128 v[40:43], v11 offset:0
v_mfma_f32_16x16x32_bf16 v[132:135], v[84:87], v[56:59], v[132:135]
ds_read_b64_tr_b16 v[76:77], v8 offset:512
ds_read_b64_tr_b16 v[78:79], v8 offset:2560
v_mfma_f32_16x16x32_bf16 v[148:151], v[84:87], v[60:63], v[148:151]
ds_read_b128 v[44:47], v12 offset:0
v_mfma_f32_16x16x32_bf16 v[104:107], v[88:91], v[48:51], v[104:107]
v_mfma_f32_16x16x32_bf16 v[120:123], v[88:91], v[52:55], v[120:123]
s_add_u32 m0, s50, 106496
s_add_u32 s93, s58, 884736
v_mfma_f32_16x16x32_bf16 v[136:139], v[88:91], v[56:59], v[136:139]
buffer_load_dwordx4 v5, s[40:43], s93 offen lds
v_mfma_f32_16x16x32_bf16 v[152:155], v[88:91], v[60:63], v[152:155]
buffer_load_dwordx4 v6, s[40:43], s93 offen offset:2048 lds
s_add_u32 m0, s94, 98304
s_add_u32 s92, s57, 27648
v_mfma_f32_16x16x32_bf16 v[108:111], v[92:95], v[48:51], v[108:111]
buffer_load_dwordx4 v3, s[8:11], s92 offen sc1 lds
v_mfma_f32_16x16x32_bf16 v[124:127], v[92:95], v[52:55], v[124:127]
buffer_load_dwordx4 v4, s[8:11], s92 offen offset:1024 sc1 lds
v_mfma_f32_16x16x32_bf16 v[140:143], v[92:95], v[56:59], v[140:143]
v_mfma_f32_16x16x32_bf16 v[156:159], v[92:95], v[60:63], v[156:159]
s_waitcnt vmcnt(20) lgkmcnt(0)
s_barrier
v_mfma_f32_16x16x32_bf16 v[96:99], v[64:67], v[32:35], v[96:99]
ds_read_b64_tr_b16 v[80:81], v7 offset:16384
ds_read_b64_tr_b16 v[82:83], v7 offset:18432
v_mfma_f32_16x16x32_bf16 v[112:115], v[64:67], v[36:39], v[112:115]
ds_read_b128 v[48:51], v9 offset:16384
v_mfma_f32_16x16x32_bf16 v[128:131], v[64:67], v[40:43], v[128:131]
ds_read_b64_tr_b16 v[84:85], v8 offset:16384
ds_read_b64_tr_b16 v[86:87], v8 offset:18432
v_mfma_f32_16x16x32_bf16 v[144:147], v[64:67], v[44:47], v[144:147]
ds_read_b128 v[52:55], v10 offset:16384
v_mfma_f32_16x16x32_bf16 v[100:103], v[68:71], v[32:35], v[100:103]
ds_read_b64_tr_b16 v[88:89], v7 offset:16896
ds_read_b64_tr_b16 v[90:91], v7 offset:18944
v_mfma_f32_16x16x32_bf16 v[116:119], v[68:71], v[36:39], v[116:119]
ds_read_b128 v[56:59], v11 offset:16384
v_mfma_f32_16x16x32_bf16 v[132:135], v[68:71], v[40:43], v[132:135]
ds_read_b64_tr_b16 v[92:93], v8 offset:16896
ds_read_b64_tr_b16 v[94:95], v8 offset:18944
v_mfma_f32_16x16x32_bf16 v[148:151], v[68:71], v[44:47], v[148:151]
ds_read_b128 v[60:63], v12 offset:16384
v_mfma_f32_16x16x32_bf16 v[104:107], v[72:75], v[32:35], v[104:107]
v_mfma_f32_16x16x32_bf16 v[120:123], v[72:75], v[36:39], v[120:123]
s_add_u32 m0, s50, 122880
s_add_u32 s93, s58, 917504
v_mfma_f32_16x16x32_bf16 v[136:139], v[72:75], v[40:43], v[136:139]
buffer_load_dwordx4 v5, s[40:43], s93 offen lds
v_mfma_f32_16x16x32_bf16 v[152:155], v[72:75], v[44:47], v[152:155]
buffer_load_dwordx4 v6, s[40:43], s93 offen offset:2048 lds
s_add_u32 m0, s94, 114688
s_add_u32 s92, s57, 28672
v_mfma_f32_16x16x32_bf16 v[108:111], v[76:79], v[32:35], v[108:111]
buffer_load_dwordx4 v3, s[8:11], s92 offen sc1 lds
v_mfma_f32_16x16x32_bf16 v[124:127], v[76:79], v[36:39], v[124:127]
buffer_load_dwordx4 v4, s[8:11], s92 offen offset:1024 sc1 lds
v_mfma_f32_16x16x32_bf16 v[140:143], v[76:79], v[40:43], v[140:143]
v_mfma_f32_16x16x32_bf16 v[156:159], v[76:79], v[44:47], v[156:159]
s_waitcnt vmcnt(20) lgkmcnt(0)
s_barrier
v_mfma_f32_16x16x32_bf16 v[96:99], v[80:83], v[48:51], v[96:99]
ds_read_b64_tr_b16 v[64:65], v7 offset:32768
ds_read_b64_tr_b16 v[66:67], v7 offset:34816
v_mfma_f32_16x16x32_bf16 v[112:115], v[80:83], v[52:55], v[112:115]
ds_read_b128 v[32:35], v9 offset:32768
v_mfma_f32_16x16x32_bf16 v[128:131], v[80:83], v[56:59], v[128:131]
ds_read_b64_tr_b16 v[68:69], v8 offset:32768
ds_read_b64_tr_b16 v[70:71], v8 offset:34816
v_mfma_f32_16x16x32_bf16 v[144:147], v[80:83], v[60:63], v[144:147]
ds_read_b128 v[36:39], v10 offset:32768
v_mfma_f32_16x16x32_bf16 v[100:103], v[84:87], v[48:51], v[100:103]
ds_read_b64_tr_b16 v[72:73], v7 offset:33280
ds_read_b64_tr_b16 v[74:75], v7 offset:35328
v_mfma_f32_16x16x32_bf16 v[116:119], v[84:87], v[52:55], v[116:119]
ds_read_b128 v[40:43], v11 offset:32768
v_mfma_f32_16x16x32_bf16 v[132:135], v[84:87], v[56:59], v[132:135]
ds_read_b64_tr_b16 v[76:77], v8 offset:33280
ds_read_b64_tr_b16 v[78:79], v8 offset:35328
v_mfma_f32_16x16x32_bf16 v[148:151], v[84:87], v[60:63], v[148:151]
ds_read_b128 v[44:47], v12 offset:32768
v_mfma_f32_16x16x32_bf16 v[104:107], v[88:91], v[48:51], v[104:107]
v_mfma_f32_16x16x32_bf16 v[120:123], v[88:91], v[52:55], v[120:123]
s_add_u32 m0, s50, 8192
s_add_u32 s93, s58, 950272
v_mfma_f32_16x16x32_bf16 v[136:139], v[88:91], v[56:59], v[136:139]
buffer_load_dwordx4 v5, s[40:43], s93 offen lds
v_mfma_f32_16x16x32_bf16 v[152:155], v[88:91], v[60:63], v[152:155]
buffer_load_dwordx4 v6, s[40:43], s93 offen offset:2048 lds
s_add_u32 m0, s94, 0
s_add_u32 s92, s57, 29696
v_mfma_f32_16x16x32_bf16 v[108:111], v[92:95], v[48:51], v[108:111]
buffer_load_dwordx4 v3, s[8:11], s92 offen sc1 lds
v_mfma_f32_16x16x32_bf16 v[124:127], v[92:95], v[52:55], v[124:127]
buffer_load_dwordx4 v4, s[8:11], s92 offen offset:1024 sc1 lds
v_mfma_f32_16x16x32_bf16 v[140:143], v[92:95], v[56:59], v[140:143]
v_mfma_f32_16x16x32_bf16 v[156:159], v[92:95], v[60:63], v[156:159]
s_waitcnt vmcnt(20) lgkmcnt(0)
s_barrier
v_mfma_f32_16x16x32_bf16 v[96:99], v[64:67], v[32:35], v[96:99]
ds_read_b64_tr_b16 v[80:81], v7 offset:49152
ds_read_b64_tr_b16 v[82:83], v7 offset:51200
v_mfma_f32_16x16x32_bf16 v[112:115], v[64:67], v[36:39], v[112:115]
ds_read_b128 v[48:51], v9 offset:49152
v_mfma_f32_16x16x32_bf16 v[128:131], v[64:67], v[40:43], v[128:131]
ds_read_b64_tr_b16 v[84:85], v8 offset:49152
ds_read_b64_tr_b16 v[86:87], v8 offset:51200
v_mfma_f32_16x16x32_bf16 v[144:147], v[64:67], v[44:47], v[144:147]
ds_read_b128 v[52:55], v10 offset:49152
v_mfma_f32_16x16x32_bf16 v[100:103], v[68:71], v[32:35], v[100:103]
ds_read_b64_tr_b16 v[88:89], v7 offset:49664
ds_read_b64_tr_b16 v[90:91], v7 offset:51712
v_mfma_f32_16x16x32_bf16 v[116:119], v[68:71], v[36:39], v[116:119]
ds_read_b128 v[56:59], v11 offset:49152
v_mfma_f32_16x16x32_bf16 v[132:135], v[68:71], v[40:43], v[132:135]
ds_read_b64_tr_b16 v[92:93], v8 offset:49664
ds_read_b64_tr_b16 v[94:95], v8 offset:51712
v_mfma_f32_16x16x32_bf16 v[148:151], v[68:71], v[44:47], v[148:151]
ds_read_b128 v[60:63], v12 offset:49152
v_mfma_f32_16x16x32_bf16 v[104:107], v[72:75], v[32:35], v[104:107]
v_mfma_f32_16x16x32_bf16 v[120:123], v[72:75], v[36:39], v[120:123]
s_add_u32 m0, s50, 24576
s_add_u32 s93, s58, 983040
v_mfma_f32_16x16x32_bf16 v[136:139], v[72:75], v[40:43], v[136:139]
buffer_load_dwordx4 v5, s[40:43], s93 offen lds
v_mfma_f32_16x16x32_bf16 v[152:155], v[72:75], v[44:47], v[152:155]
buffer_load_dwordx4 v6, s[40:43], s93 offen offset:2048 lds
s_add_u32 m0, s94, 16384
s_add_u32 s92, s57, 30720
v_mfma_f32_16x16x32_bf16 v[108:111], v[76:79], v[32:35], v[108:111]
buffer_load_dwordx4 v3, s[8:11], s92 offen sc1 lds
v_mfma_f32_16x16x32_bf16 v[124:127], v[76:79], v[36:39], v[124:127]
buffer_load_dwordx4 v4, s[8:11], s92 offen offset:1024 sc1 lds
v_mfma_f32_16x16x32_bf16 v[140:143], v[76:79], v[40:43], v[140:143]
v_mfma_f32_16x16x32_bf16 v[156:159], v[76:79], v[44:47], v[156:159]
s_waitcnt vmcnt(20) lgkmcnt(0)
s_barrier
v_mfma_f32_16x16x32_bf16 v[96:99], v[80:83], v[48:51], v[96:99]
ds_read_b64_tr_b16 v[64:65], v18 offset:0
ds_read_b64_tr_b16 v[66:67], v18 offset:2048
v_mfma_f32_16x16x32_bf16 v[112:115], v[80:83], v[52:55], v[112:115]
ds_read_b128 v[32:35], v20 offset:0
v_mfma_f32_16x16x32_bf16 v[128:131], v[80:83], v[56:59], v[128:131]
ds_read_b64_tr_b16 v[68:69], v19 offset:0
ds_read_b64_tr_b16 v[70:71], v19 offset:2048
v_mfma_f32_16x16x32_bf16 v[144:147], v[80:83], v[60:63], v[144:147]
ds_read_b128 v[36:39], v21 offset:0
v_mfma_f32_16x16x32_bf16 v[100:103], v[84:87], v[48:51], v[100:103]
)ASM"
R"ASM(
ds_read_b64_tr_b16 v[72:73], v18 offset:512
ds_read_b64_tr_b16 v[74:75], v18 offset:2560
v_mfma_f32_16x16x32_bf16 v[116:119], v[84:87], v[52:55], v[116:119]
ds_read_b128 v[40:43], v22 offset:0
v_mfma_f32_16x16x32_bf16 v[132:135], v[84:87], v[56:59], v[132:135]
ds_read_b64_tr_b16 v[76:77], v19 offset:512
ds_read_b64_tr_b16 v[78:79], v19 offset:2560
v_mfma_f32_16x16x32_bf16 v[148:151], v[84:87], v[60:63], v[148:151]
ds_read_b128 v[44:47], v23 offset:0
v_mfma_f32_16x16x32_bf16 v[104:107], v[88:91], v[48:51], v[104:107]
v_mfma_f32_16x16x32_bf16 v[120:123], v[88:91], v[52:55], v[120:123]
s_add_u32 m0, s50, 40960
s_add_u32 s93, s58, 1015808
v_mfma_f32_16x16x32_bf16 v[136:139], v[88:91], v[56:59], v[136:139]
buffer_load_dwordx4 v5, s[40:43], s93 offen lds
v_mfma_f32_16x16x32_bf16 v[152:155], v[88:91], v[60:63], v[152:155]
buffer_load_dwordx4 v6, s[40:43], s93 offen offset:2048 lds
s_add_u32 m0, s94, 32768
s_add_u32 s92, s57, 31744
v_mfma_f32_16x16x32_bf16 v[108:111], v[92:95], v[48:51], v[108:111]
buffer_load_dwordx4 v3, s[8:11], s92 offen sc1 lds
v_mfma_f32_16x16x32_bf16 v[124:127], v[92:95], v[52:55], v[124:127]
buffer_load_dwordx4 v4, s[8:11], s92 offen offset:1024 sc1 lds
v_mfma_f32_16x16x32_bf16 v[140:143], v[92:95], v[56:59], v[140:143]
v_mfma_f32_16x16x32_bf16 v[156:159], v[92:95], v[60:63], v[156:159]
s_waitcnt vmcnt(20) lgkmcnt(0)
s_barrier
v_mfma_f32_16x16x32_bf16 v[96:99], v[64:67], v[32:35], v[96:99]
ds_read_b64_tr_b16 v[80:81], v18 offset:16384
ds_read_b64_tr_b16 v[82:83], v18 offset:18432
v_mfma_f32_16x16x32_bf16 v[112:115], v[64:67], v[36:39], v[112:115]
ds_read_b128 v[48:51], v20 offset:16384
v_mfma_f32_16x16x32_bf16 v[128:131], v[64:67], v[40:43], v[128:131]
ds_read_b64_tr_b16 v[84:85], v19 offset:16384
ds_read_b64_tr_b16 v[86:87], v19 offset:18432
v_mfma_f32_16x16x32_bf16 v[144:147], v[64:67], v[44:47], v[144:147]
ds_read_b128 v[52:55], v21 offset:16384
v_mfma_f32_16x16x32_bf16 v[100:103], v[68:71], v[32:35], v[100:103]
ds_read_b64_tr_b16 v[88:89], v18 offset:16896
ds_read_b64_tr_b16 v[90:91], v18 offset:18944
v_mfma_f32_16x16x32_bf16 v[116:119], v[68:71], v[36:39], v[116:119]
ds_read_b128 v[56:59], v22 offset:16384
v_mfma_f32_16x16x32_bf16 v[132:135], v[68:71], v[40:43], v[132:135]
ds_read_b64_tr_b16 v[92:93], v19 offset:16896
ds_read_b64_tr_b16 v[94:95], v19 offset:18944
v_mfma_f32_16x16x32_bf16 v[148:151], v[68:71], v[44:47], v[148:151]
ds_read_b128 v[60:63], v23 offset:16384
v_mfma_f32_16x16x32_bf16 v[104:107], v[72:75], v[32:35], v[104:107]
v_mfma_f32_16x16x32_bf16 v[120:123], v[72:75], v[36:39], v[120:123]
v_mfma_f32_16x16x32_bf16 v[136:139], v[72:75], v[40:43], v[136:139]
v_mfma_f32_16x16x32_bf16 v[152:155], v[72:75], v[44:47], v[152:155]
v_mfma_f32_16x16x32_bf16 v[108:111], v[76:79], v[32:35], v[108:111]
v_mfma_f32_16x16x32_bf16 v[124:127], v[76:79], v[36:39], v[124:127]
v_mfma_f32_16x16x32_bf16 v[140:143], v[76:79], v[40:43], v[140:143]
v_mfma_f32_16x16x32_bf16 v[156:159], v[76:79], v[44:47], v[156:159]
s_waitcnt vmcnt(16) lgkmcnt(0)
s_barrier
v_mfma_f32_16x16x32_bf16 v[96:99], v[80:83], v[48:51], v[96:99]
ds_read_b64_tr_b16 v[64:65], v18 offset:32768
ds_read_b64_tr_b16 v[66:67], v18 offset:34816
v_mfma_f32_16x16x32_bf16 v[112:115], v[80:83], v[52:55], v[112:115]
ds_read_b128 v[32:35], v20 offset:32768
v_mfma_f32_16x16x32_bf16 v[128:131], v[80:83], v[56:59], v[128:131]
ds_read_b64_tr_b16 v[68:69], v19 offset:32768
ds_read_b64_tr_b16 v[70:71], v19 offset:34816
v_mfma_f32_16x16x32_bf16 v[144:147], v[80:83], v[60:63], v[144:147]
ds_read_b128 v[36:39], v21 offset:32768
v_mfma_f32_16x16x32_bf16 v[100:103], v[84:87], v[48:51], v[100:103]
ds_read_b64_tr_b16 v[72:73], v18 offset:33280
ds_read_b64_tr_b16 v[74:75], v18 offset:35328
v_mfma_f32_16x16x32_bf16 v[116:119], v[84:87], v[52:55], v[116:119]
ds_read_b128 v[40:43], v22 offset:32768
v_mfma_f32_16x16x32_bf16 v[132:135], v[84:87], v[56:59], v[132:135]
ds_read_b64_tr_b16 v[76:77], v19 offset:33280
ds_read_b64_tr_b16 v[78:79], v19 offset:35328
v_mfma_f32_16x16x32_bf16 v[148:151], v[84:87], v[60:63], v[148:151]
ds_read_b128 v[44:47], v23 offset:32768
v_mfma_f32_16x16x32_bf16 v[104:107], v[88:91], v[48:51], v[104:107]
v_mfma_f32_16x16x32_bf16 v[120:123], v[88:91], v[52:55], v[120:123]
v_mfma_f32_16x16x32_bf16 v[136:139], v[88:91], v[56:59], v[136:139]
v_mfma_f32_16x16x32_bf16 v[152:155], v[88:91], v[60:63], v[152:155]
v_mfma_f32_16x16x32_bf16 v[108:111], v[92:95], v[48:51], v[108:111]
v_mfma_f32_16x16x32_bf16 v[124:127], v[92:95], v[52:55], v[124:127]
v_mfma_f32_16x16x32_bf16 v[140:143], v[92:95], v[56:59], v[140:143]
v_mfma_f32_16x16x32_bf16 v[156:159], v[92:95], v[60:63], v[156:159]
s_waitcnt vmcnt(12) lgkmcnt(0)
s_barrier
v_mfma_f32_16x16x32_bf16 v[96:99], v[64:67], v[32:35], v[96:99]
ds_read_b64_tr_b16 v[80:81], v18 offset:49152
ds_read_b64_tr_b16 v[82:83], v18 offset:51200
v_mfma_f32_16x16x32_bf16 v[112:115], v[64:67], v[36:39], v[112:115]
ds_read_b128 v[48:51], v20 offset:49152
v_mfma_f32_16x16x32_bf16 v[128:131], v[64:67], v[40:43], v[128:131]
ds_read_b64_tr_b16 v[84:85], v19 offset:49152
ds_read_b64_tr_b16 v[86:87], v19 offset:51200
v_mfma_f32_16x16x32_bf16 v[144:147], v[64:67], v[44:47], v[144:147]
ds_read_b128 v[52:55], v21 offset:49152
v_mfma_f32_16x16x32_bf16 v[100:103], v[68:71], v[32:35], v[100:103]
ds_read_b64_tr_b16 v[88:89], v18 offset:49664
ds_read_b64_tr_b16 v[90:91], v18 offset:51712
v_mfma_f32_16x16x32_bf16 v[116:119], v[68:71], v[36:39], v[116:119]
ds_read_b128 v[56:59], v22 offset:49152
v_mfma_f32_16x16x32_bf16 v[132:135], v[68:71], v[40:43], v[132:135]
ds_read_b64_tr_b16 v[92:93], v19 offset:49664
ds_read_b64_tr_b16 v[94:95], v19 offset:51712
v_mfma_f32_16x16x32_bf16 v[148:151], v[68:71], v[44:47], v[148:151]
ds_read_b128 v[60:63], v23 offset:49152
v_mfma_f32_16x16x32_bf16 v[104:107], v[72:75], v[32:35], v[104:107]
v_mfma_f32_16x16x32_bf16 v[120:123], v[72:75], v[36:39], v[120:123]
v_mfma_f32_16x16x32_bf16 v[136:139], v[72:75], v[40:43], v[136:139]
v_mfma_f32_16x16x32_bf16 v[152:155], v[72:75], v[44:47], v[152:155]
v_mfma_f32_16x16x32_bf16 v[108:111], v[76:79], v[32:35], v[108:111]
v_mfma_f32_16x16x32_bf16 v[124:127], v[76:79], v[36:39], v[124:127]
v_mfma_f32_16x16x32_bf16 v[140:143], v[76:79], v[40:43], v[140:143]
v_mfma_f32_16x16x32_bf16 v[156:159], v[76:79], v[44:47], v[156:159]
s_waitcnt vmcnt(8) lgkmcnt(0)
s_barrier
v_mfma_f32_16x16x32_bf16 v[96:99], v[80:83], v[48:51], v[96:99]
ds_read_b64_tr_b16 v[64:65], v7 offset:0
ds_read_b64_tr_b16 v[66:67], v7 offset:2048
v_mfma_f32_16x16x32_bf16 v[112:115], v[80:83], v[52:55], v[112:115]
ds_read_b128 v[32:35], v9 offset:0
v_mfma_f32_16x16x32_bf16 v[128:131], v[80:83], v[56:59], v[128:131]
ds_read_b64_tr_b16 v[68:69], v8 offset:0
ds_read_b64_tr_b16 v[70:71], v8 offset:2048
v_mfma_f32_16x16x32_bf16 v[144:147], v[80:83], v[60:63], v[144:147]
ds_read_b128 v[36:39], v10 offset:0
v_mfma_f32_16x16x32_bf16 v[100:103], v[84:87], v[48:51], v[100:103]
ds_read_b64_tr_b16 v[72:73], v7 offset:512
ds_read_b64_tr_b16 v[74:75], v7 offset:2560
v_mfma_f32_16x16x32_bf16 v[116:119], v[84:87], v[52:55], v[116:119]
ds_read_b128 v[40:43], v11 offset:0
v_mfma_f32_16x16x32_bf16 v[132:135], v[84:87], v[56:59], v[132:135]
ds_read_b64_tr_b16 v[76:77], v8 offset:512
ds_read_b64_tr_b16 v[78:79], v8 offset:2560
v_mfma_f32_16x16x32_bf16 v[148:151], v[84:87], v[60:63], v[148:151]
ds_read_b128 v[44:47], v12 offset:0
v_mfma_f32_16x16x32_bf16 v[104:107], v[88:91], v[48:51], v[104:107]
v_mfma_f32_16x16x32_bf16 v[120:123], v[88:91], v[52:55], v[120:123]
buffer_load_dwordx2 v[160:161], v13, s[24:27], 0 offen offset:0
buffer_load_dwordx2 v[168:169], v14, s[16:19], 0 offen offset:0
v_mfma_f32_16x16x32_bf16 v[136:139], v[88:91], v[56:59], v[136:139]
buffer_load_dwordx2 v[162:163], v13, s[24:27], 0 offen offset:32
buffer_load_dwordx2 v[170:171], v14, s[16:19], 0 offen offset:32
v_mfma_f32_16x16x32_bf16 v[152:155], v[88:91], v[60:63], v[152:155]
buffer_load_dwordx2 v[164:165], v13, s[24:27], 0 offen offset:64
buffer_load_dwordx2 v[172:173], v14, s[16:19], 0 offen offset:64
v_mfma_f32_16x16x32_bf16 v[108:111], v[92:95], v[48:51], v[108:111]
buffer_load_dwordx2 v[166:167], v13, s[24:27], 0 offen offset:96
buffer_load_dwordx2 v[174:175], v14, s[16:19], 0 offen offset:96
v_mfma_f32_16x16x32_bf16 v[124:127], v[92:95], v[52:55], v[124:127]
v_mfma_f32_16x16x32_bf16 v[140:143], v[92:95], v[56:59], v[140:143]
v_mfma_f32_16x16x32_bf16 v[156:159], v[92:95], v[60:63], v[156:159]
s_waitcnt vmcnt(12) lgkmcnt(0)
s_barrier
v_mfma_f32_16x16x32_bf16 v[96:99], v[64:67], v[32:35], v[96:99]
ds_read_b64_tr_b16 v[80:81], v7 offset:16384
ds_read_b64_tr_b16 v[82:83], v7 offset:18432
v_mfma_f32_16x16x32_bf16 v[112:115], v[64:67], v[36:39], v[112:115]
ds_read_b128 v[48:51], v9 offset:16384
v_mfma_f32_16x16x32_bf16 v[128:131], v[64:67], v[40:43], v[128:131]
ds_read_b64_tr_b16 v[84:85], v8 offset:16384
ds_read_b64_tr_b16 v[86:87], v8 offset:18432
v_mfma_f32_16x16x32_bf16 v[144:147], v[64:67], v[44:47], v[144:147]
ds_read_b128 v[52:55], v10 offset:16384
v_mfma_f32_16x16x32_bf16 v[100:103], v[68:71], v[32:35], v[100:103]
ds_read_b64_tr_b16 v[88:89], v7 offset:16896
ds_read_b64_tr_b16 v[90:91], v7 offset:18944
v_mfma_f32_16x16x32_bf16 v[116:119], v[68:71], v[36:39], v[116:119]
ds_read_b128 v[56:59], v11 offset:16384
v_mfma_f32_16x16x32_bf16 v[132:135], v[68:71], v[40:43], v[132:135]
ds_read_b64_tr_b16 v[92:93], v8 offset:16896
ds_read_b64_tr_b16 v[94:95], v8 offset:18944
v_mfma_f32_16x16x32_bf16 v[148:151], v[68:71], v[44:47], v[148:151]
ds_read_b128 v[60:63], v12 offset:16384
v_mfma_f32_16x16x32_bf16 v[104:107], v[72:75], v[32:35], v[104:107]
v_mfma_f32_16x16x32_bf16 v[120:123], v[72:75], v[36:39], v[120:123]
v_mfma_f32_16x16x32_bf16 v[136:139], v[72:75], v[40:43], v[136:139]
v_mfma_f32_16x16x32_bf16 v[152:155], v[72:75], v[44:47], v[152:155]
v_mfma_f32_16x16x32_bf16 v[108:111], v[76:79], v[32:35], v[108:111]
v_mfma_f32_16x16x32_bf16 v[124:127], v[76:79], v[36:39], v[124:127]
v_mfma_f32_16x16x32_bf16 v[140:143], v[76:79], v[40:43], v[140:143]
v_mfma_f32_16x16x32_bf16 v[156:159], v[76:79], v[44:47], v[156:159]
s_waitcnt vmcnt(8) lgkmcnt(0)
s_barrier
v_mfma_f32_16x16x32_bf16 v[96:99], v[80:83], v[48:51], v[96:99]
ds_read_b64_tr_b16 v[64:65], v7 offset:32768
ds_read_b64_tr_b16 v[66:67], v7 offset:34816
v_mfma_f32_16x16x32_bf16 v[112:115], v[80:83], v[52:55], v[112:115]
ds_read_b128 v[32:35], v9 offset:32768
v_mfma_f32_16x16x32_bf16 v[128:131], v[80:83], v[56:59], v[128:131]
ds_read_b64_tr_b16 v[68:69], v8 offset:32768
ds_read_b64_tr_b16 v[70:71], v8 offset:34816
v_mfma_f32_16x16x32_bf16 v[144:147], v[80:83], v[60:63], v[144:147]
ds_read_b128 v[36:39], v10 offset:32768
v_mfma_f32_16x16x32_bf16 v[100:103], v[84:87], v[48:51], v[100:103]
ds_read_b64_tr_b16 v[72:73], v7 offset:33280
ds_read_b64_tr_b16 v[74:75], v7 offset:35328
v_mfma_f32_16x16x32_bf16 v[116:119], v[84:87], v[52:55], v[116:119]
ds_read_b128 v[40:43], v11 offset:32768
)ASM"
R"ASM(
v_mfma_f32_16x16x32_bf16 v[132:135], v[84:87], v[56:59], v[132:135]
ds_read_b64_tr_b16 v[76:77], v8 offset:33280
ds_read_b64_tr_b16 v[78:79], v8 offset:35328
v_mfma_f32_16x16x32_bf16 v[148:151], v[84:87], v[60:63], v[148:151]
ds_read_b128 v[44:47], v12 offset:32768
v_mfma_f32_16x16x32_bf16 v[104:107], v[88:91], v[48:51], v[104:107]
v_mfma_f32_16x16x32_bf16 v[120:123], v[88:91], v[52:55], v[120:123]
v_mfma_f32_16x16x32_bf16 v[136:139], v[88:91], v[56:59], v[136:139]
v_mfma_f32_16x16x32_bf16 v[152:155], v[88:91], v[60:63], v[152:155]
v_mfma_f32_16x16x32_bf16 v[108:111], v[92:95], v[48:51], v[108:111]
v_mfma_f32_16x16x32_bf16 v[124:127], v[92:95], v[52:55], v[124:127]
v_mfma_f32_16x16x32_bf16 v[140:143], v[92:95], v[56:59], v[140:143]
v_mfma_f32_16x16x32_bf16 v[156:159], v[92:95], v[60:63], v[156:159]
s_waitcnt lgkmcnt(0)
v_mfma_f32_16x16x32_bf16 v[96:99], v[64:67], v[32:35], v[96:99]
v_mfma_f32_16x16x32_bf16 v[112:115], v[64:67], v[36:39], v[112:115]
v_mfma_f32_16x16x32_bf16 v[128:131], v[64:67], v[40:43], v[128:131]
v_mfma_f32_16x16x32_bf16 v[144:147], v[64:67], v[44:47], v[144:147]
v_mfma_f32_16x16x32_bf16 v[100:103], v[68:71], v[32:35], v[100:103]
v_mfma_f32_16x16x32_bf16 v[116:119], v[68:71], v[36:39], v[116:119]
v_mfma_f32_16x16x32_bf16 v[132:135], v[68:71], v[40:43], v[132:135]
v_mfma_f32_16x16x32_bf16 v[148:151], v[68:71], v[44:47], v[148:151]
v_mfma_f32_16x16x32_bf16 v[104:107], v[72:75], v[32:35], v[104:107]
v_mfma_f32_16x16x32_bf16 v[120:123], v[72:75], v[36:39], v[120:123]
v_mfma_f32_16x16x32_bf16 v[136:139], v[72:75], v[40:43], v[136:139]
v_mfma_f32_16x16x32_bf16 v[152:155], v[72:75], v[44:47], v[152:155]
v_mfma_f32_16x16x32_bf16 v[108:111], v[76:79], v[32:35], v[108:111]
v_mfma_f32_16x16x32_bf16 v[124:127], v[76:79], v[36:39], v[124:127]
v_mfma_f32_16x16x32_bf16 v[140:143], v[76:79], v[40:43], v[140:143]
v_mfma_f32_16x16x32_bf16 v[156:159], v[76:79], v[44:47], v[156:159]
s_nop 4
v_cvt_pk_bf16_f32 v112, v112, v113
v_cvt_pk_bf16_f32 v113, v114, v115
v_cvt_pk_bf16_f32 v114, v116, v117
v_cvt_pk_bf16_f32 v115, v118, v119
v_cvt_pk_bf16_f32 v116, v120, v121
v_cvt_pk_bf16_f32 v117, v122, v123
v_cvt_pk_bf16_f32 v118, v124, v125
v_cvt_pk_bf16_f32 v119, v126, v127
s_xor_b32 s98, s59, 32768
s_add_u32 s98, s98, 0
buffer_store_dwordx4 v[112:115], v2, s[8:11], s98 offen offset:0 sc1
buffer_store_dwordx4 v[116:119], v2, s[8:11], s98 offen offset:1024 sc1
v_cvt_pk_bf16_f32 v128, v128, v129
v_cvt_pk_bf16_f32 v129, v130, v131
v_cvt_pk_bf16_f32 v130, v132, v133
v_cvt_pk_bf16_f32 v131, v134, v135
v_cvt_pk_bf16_f32 v132, v136, v137
v_cvt_pk_bf16_f32 v133, v138, v139
v_cvt_pk_bf16_f32 v134, v140, v141
v_cvt_pk_bf16_f32 v135, v142, v143
s_xor_b32 s98, s59, 65536
s_add_u32 s98, s98, 2048
buffer_store_dwordx4 v[128:131], v2, s[8:11], s98 offen offset:0 sc1
buffer_store_dwordx4 v[132:135], v2, s[8:11], s98 offen offset:1024 sc1
v_cvt_pk_bf16_f32 v144, v144, v145
v_cvt_pk_bf16_f32 v145, v146, v147
v_cvt_pk_bf16_f32 v146, v148, v149
v_cvt_pk_bf16_f32 v147, v150, v151
v_cvt_pk_bf16_f32 v148, v152, v153
v_cvt_pk_bf16_f32 v149, v154, v155
v_cvt_pk_bf16_f32 v150, v156, v157
v_cvt_pk_bf16_f32 v151, v158, v159
s_xor_b32 s98, s59, 98304
s_add_u32 s98, s98, 4096
buffer_store_dwordx4 v[144:147], v2, s[8:11], s98 offen offset:0 sc1
buffer_store_dwordx4 v[148:151], v2, s[8:11], s98 offen offset:1024 sc1
s_waitcnt vmcnt(6)
v_lshlrev_b32 v176, 16, v160
v_and_b32 v177, 0xffff0000, v160
v_pk_add_f32 v[96:97], v[96:97], v[176:177]
v_lshlrev_b32 v176, 16, v168
v_and_b32 v177, 0xffff0000, v168
v_pk_add_f32 v[96:97], v[96:97], v[176:177]
v_lshlrev_b32 v176, 16, v161
v_and_b32 v177, 0xffff0000, v161
v_pk_add_f32 v[98:99], v[98:99], v[176:177]
v_lshlrev_b32 v176, 16, v169
v_and_b32 v177, 0xffff0000, v169
v_pk_add_f32 v[98:99], v[98:99], v[176:177]
v_lshlrev_b32 v176, 16, v162
v_and_b32 v177, 0xffff0000, v162
v_pk_add_f32 v[100:101], v[100:101], v[176:177]
v_lshlrev_b32 v176, 16, v170
v_and_b32 v177, 0xffff0000, v170
v_pk_add_f32 v[100:101], v[100:101], v[176:177]
v_lshlrev_b32 v176, 16, v163
v_and_b32 v177, 0xffff0000, v163
v_pk_add_f32 v[102:103], v[102:103], v[176:177]
v_lshlrev_b32 v176, 16, v171
v_and_b32 v177, 0xffff0000, v171
v_pk_add_f32 v[102:103], v[102:103], v[176:177]
v_lshlrev_b32 v176, 16, v164
v_and_b32 v177, 0xffff0000, v164
v_pk_add_f32 v[104:105], v[104:105], v[176:177]
v_lshlrev_b32 v176, 16, v172
v_and_b32 v177, 0xffff0000, v172
v_pk_add_f32 v[104:105], v[104:105], v[176:177]
v_lshlrev_b32 v176, 16, v165
v_and_b32 v177, 0xffff0000, v165
v_pk_add_f32 v[106:107], v[106:107], v[176:177]
v_lshlrev_b32 v176, 16, v173
v_and_b32 v177, 0xffff0000, v173
v_pk_add_f32 v[106:107], v[106:107], v[176:177]
v_lshlrev_b32 v176, 16, v166
v_and_b32 v177, 0xffff0000, v166
v_pk_add_f32 v[108:109], v[108:109], v[176:177]
v_lshlrev_b32 v176, 16, v174
v_and_b32 v177, 0xffff0000, v174
v_pk_add_f32 v[108:109], v[108:109], v[176:177]
v_lshlrev_b32 v176, 16, v167
v_and_b32 v177, 0xffff0000, v167
v_pk_add_f32 v[110:111], v[110:111], v[176:177]
v_lshlrev_b32 v176, 16, v175
v_and_b32 v177, 0xffff0000, v175
v_pk_add_f32 v[110:111], v[110:111], v[176:177]
s_waitcnt vmcnt(0)
s_barrier
s_add_u32 s98, s100, 4
v_mov_b32 v178, s98
v_mov_b32 v179, 0
buffer_store_dword v178, v179, s[8:11], s84 offen sc1
v_and_b32 v180, 48, v2
v_lshlrev_b32 v180, 7, v180
buffer_load_dword v181, v180, s[8:11], s61 offen sc1
s_waitcnt vmcnt(0)
s_add_u32 s98, s100, 4
v_subrev_u32 v181, s98, v181
v_cmp_lt_u32 vcc, 0, v181
s_cmp_lg_u64 vcc, 0
s_cbranch_scc1 .Lsp5_%=
.Lsr5_%=:
s_add_u32 s98, s59, 4096
buffer_load_dwordx4 v[184:187], v2, s[8:11], s59 offen offset:0 sc1
buffer_load_dwordx4 v[188:191], v2, s[8:11], s59 offen offset:1024 sc1
buffer_load_dwordx4 v[192:195], v2, s[8:11], s59 offen offset:2048 sc1
buffer_load_dwordx4 v[196:199], v2, s[8:11], s59 offen offset:3072 sc1
buffer_load_dwordx4 v[200:203], v2, s[8:11], s98 offen offset:0 sc1
buffer_load_dwordx4 v[204:207], v2, s[8:11], s98 offen offset:1024 sc1
s_waitcnt vmcnt(4)
v_lshlrev_b32 v176, 16, v184
v_and_b32 v177, 0xffff0000, v184
v_pk_add_f32 v[96:97], v[96:97], v[176:177]
v_lshlrev_b32 v176, 16, v185
v_and_b32 v177, 0xffff0000, v185
v_pk_add_f32 v[98:99], v[98:99], v[176:177]
v_lshlrev_b32 v176, 16, v186
v_and_b32 v177, 0xffff0000, v186
v_pk_add_f32 v[100:101], v[100:101], v[176:177]
v_lshlrev_b32 v176, 16, v187
v_and_b32 v177, 0xffff0000, v187
v_pk_add_f32 v[102:103], v[102:103], v[176:177]
v_lshlrev_b32 v176, 16, v188
v_and_b32 v177, 0xffff0000, v188
v_pk_add_f32 v[104:105], v[104:105], v[176:177]
v_lshlrev_b32 v176, 16, v189
v_and_b32 v177, 0xffff0000, v189
v_pk_add_f32 v[106:107], v[106:107], v[176:177]
v_lshlrev_b32 v176, 16, v190
v_and_b32 v177, 0xffff0000, v190
v_pk_add_f32 v[108:109], v[108:109], v[176:177]
v_lshlrev_b32 v176, 16, v191
v_and_b32 v177, 0xffff0000, v191
v_pk_add_f32 v[110:111], v[110:111], v[176:177]
s_waitcnt vmcnt(2)
v_lshlrev_b32 v176, 16, v192
v_and_b32 v177, 0xffff0000, v192
v_pk_add_f32 v[96:97], v[96:97], v[176:177]
v_lshlrev_b32 v176, 16, v193
v_and_b32 v177, 0xffff0000, v193
v_pk_add_f32 v[98:99], v[98:99], v[176:177]
v_lshlrev_b32 v176, 16, v194
v_and_b32 v177, 0xffff0000, v194
v_pk_add_f32 v[100:101], v[100:101], v[176:177]
v_lshlrev_b32 v176, 16, v195
v_and_b32 v177, 0xffff0000, v195
v_pk_add_f32 v[102:103], v[102:103], v[176:177]
v_lshlrev_b32 v176, 16, v196
v_and_b32 v177, 0xffff0000, v196
v_pk_add_f32 v[104:105], v[104:105], v[176:177]
v_lshlrev_b32 v176, 16, v197
v_and_b32 v177, 0xffff0000, v197
v_pk_add_f32 v[106:107], v[106:107], v[176:177]
v_lshlrev_b32 v176, 16, v198
v_and_b32 v177, 0xffff0000, v198
v_pk_add_f32 v[108:109], v[108:109], v[176:177]
v_lshlrev_b32 v176, 16, v199
v_and_b32 v177, 0xffff0000, v199
v_pk_add_f32 v[110:111], v[110:111], v[176:177]
s_waitcnt vmcnt(0)
v_lshlrev_b32 v176, 16, v200
v_and_b32 v177, 0xffff0000, v200
v_pk_add_f32 v[96:97], v[96:97], v[176:177]
v_lshlrev_b32 v176, 16, v201
v_and_b32 v177, 0xffff0000, v201
v_pk_add_f32 v[98:99], v[98:99], v[176:177]
v_lshlrev_b32 v176, 16, v202
v_and_b32 v177, 0xffff0000, v202
v_pk_add_f32 v[100:101], v[100:101], v[176:177]
v_lshlrev_b32 v176, 16, v203
)ASM"
R"ASM(
v_and_b32 v177, 0xffff0000, v203
v_pk_add_f32 v[102:103], v[102:103], v[176:177]
v_lshlrev_b32 v176, 16, v204
v_and_b32 v177, 0xffff0000, v204
v_pk_add_f32 v[104:105], v[104:105], v[176:177]
v_lshlrev_b32 v176, 16, v205
v_and_b32 v177, 0xffff0000, v205
v_pk_add_f32 v[106:107], v[106:107], v[176:177]
v_lshlrev_b32 v176, 16, v206
v_and_b32 v177, 0xffff0000, v206
v_pk_add_f32 v[108:109], v[108:109], v[176:177]
v_lshlrev_b32 v176, 16, v207
v_and_b32 v177, 0xffff0000, v207
v_pk_add_f32 v[110:111], v[110:111], v[176:177]
v_cvt_pk_bf16_f32 v96, v96, v97
v_cvt_pk_bf16_f32 v97, v98, v99
v_cvt_pk_bf16_f32 v98, v100, v101
v_cvt_pk_bf16_f32 v99, v102, v103
v_cvt_pk_bf16_f32 v100, v104, v105
v_cvt_pk_bf16_f32 v101, v106, v107
v_cvt_pk_bf16_f32 v102, v108, v109
v_cvt_pk_bf16_f32 v103, v110, v111
buffer_store_dwordx2 v[96:97], v13, s[20:23], 0 offen offset:0
buffer_store_dwordx2 v[98:99], v13, s[20:23], 0 offen offset:32
buffer_store_dwordx2 v[100:101], v13, s[20:23], 0 offen offset:64
buffer_store_dwordx2 v[102:103], v13, s[20:23], 0 offen offset:96
s_branch .Lend_%=
.Lsp4_%=:
s_mov_b32 s15, 0x100000
.Lsq4_%=:
s_sleep 1
buffer_load_dword v179, v178, s[8:11], s60 offen sc1
s_waitcnt vmcnt(0)
v_subrev_u32 v179, s98, v179
v_cmp_lt_u32 vcc, 1, v179
s_sub_u32 s15, s15, 1
s_cmp_eq_u32 s15, 0
s_cbranch_scc1 .Lsr4_%=
s_cmp_lg_u64 vcc, 0
s_cbranch_scc1 .Lsq4_%=
s_branch .Lsr4_%=
.Lsp5_%=:
s_mov_b32 s15, 0x100000
.Lsq5_%=:
s_sleep 1
buffer_load_dword v181, v180, s[8:11], s61 offen sc1
s_waitcnt vmcnt(0)
v_subrev_u32 v181, s98, v181
v_cmp_lt_u32 vcc, 0, v181
s_sub_u32 s15, s15, 1
s_cmp_eq_u32 s15, 0
s_cbranch_scc1 .Lsr5_%=
s_cmp_lg_u64 vcc, 0
s_cbranch_scc1 .Lsq5_%=
s_branch .Lsr5_%=
.Lend_%=:
)ASM"
        : "+{s[8:11]}"(t_ws), "+{s[16:23]}"(u_bo), "+{s[24:27]}"(t_x), "+{s[40:47]}"(sc), "+{s[48:55]}"(t_d0), "+{s[56:63]}"(u_d1), "+{s[84:87]}"(sf1), "+{s[88:91]}"(sf2), "+{s[92:95]}"(t_k4), "+{v[2:17]}"(uv), "+{s[100:101]}"(tg)
        :
        : "memory", "scc", "vcc", "s15", "s98", "s99", "v18", "v19", "v20", "v21", "v22", "v23", "v24", "v25", "v26", "v27", "v28", "v29", "v30", "v31", "v32", "v33", "v34", "v35", "v36", "v37", "v38", "v39", "v40", "v41", "v42", "v43", "v44", "v45", "v46", "v47", "v48", "v49", "v50", "v51", "v52", "v53", "v54", "v55", "v56", "v57", "v58", "v59", "v60", "v61", "v62", "v63", "v64", "v65", "v66", "v67", "v68", "v69", "v70", "v71", "v72", "v73", "v74", "v75", "v76", "v77", "v78", "v79", "v80", "v81", "v82", "v83", "v84", "v85", "v86", "v87", "v88", "v89", "v90", "v91", "v92", "v93", "v94", "v95", "v96", "v97", "v98", "v99", "v100", "v101", "v102", "v103", "v104", "v105", "v106", "v107", "v108", "v109", "v110", "v111", "v112", "v113", "v114", "v115", "v116", "v117", "v118", "v119", "v120", "v121", "v122", "v123", "v124", "v125", "v126", "v127", "v128", "v129", "v130", "v131", "v132", "v133", "v134", "v135", "v136", "v137", "v138", "v139", "v140", "v141", "v142", "v143", "v144", "v145", "v146", "v147", "v148", "v149", "v150", "v151", "v152", "v153", "v154", "v155", "v156", "v157", "v158", "v159", "v160", "v161", "v162", "v163", "v164", "v165", "v166", "v167", "v168", "v169", "v170", "v171", "v172", "v173", "v174", "v175", "v176", "v177", "v178", "v179", "v180", "v181", "v182", "v183", "v184", "v185", "v186", "v187", "v188", "v189", "v190", "v191", "v192", "v193", "v194", "v195", "v196", "v197", "v198", "v199", "v200", "v201", "v202", "v203", "v204", "v205", "v206", "v207");
}

extern "C" void kernel_launch(void* const* d_in, const int* in_sizes, int n_in, void* d_out, int out_size, void* d_ws, size_t ws_size, hipStream_t stream) {
    (void)hipFuncSetAttribute((const void*)mk_block, hipFuncAttributeMaxDynamicSharedMemorySize, 163840);
    mk_block<<<256, 256, 163840, stream>>>((const u16*)d_in[0], (const u16*)d_in[1], (const u16*)d_in[2], (const u16*)d_in[3], (const u16*)d_in[4], (const u16*)d_in[5],
                                           (const u16*)d_in[7], (const u16*)d_in[8], (const u16*)d_in[9], (const u16*)d_in[10], (char*)d_ws, (u16*)d_out);
}
```
